```python
import torch

def _softmax(x, axis=-1):
    return torch.softmax(x, dim=axis)
def _gelu(x):
    return torch.nn.functional.gelu(x, approximate="tanh")
def _silu(x):
    return torch.nn.functional.silu(x)
def _sigmoid(x):
    return torch.sigmoid(x)
def _layernorm(x, axis=-1, eps=1e-5):
    m = x.mean(dim=axis, keepdim=True)
    v = x.var(dim=axis, keepdim=True, unbiased=False)
    return (x - m) / torch.sqrt(v + eps)
def _scatter_add(tgt, idx, src, axis=0):
    return tgt.clone().index_add_(axis, idx, src)
def _segment_sum(src, seg, n):
    out = torch.zeros((n,) + src.shape[1:], dtype=src.dtype)
    return out.index_add_(0, seg, src)
def _pad(x, pw):
    flat = tuple(v for pair in reversed(pw) for v in pair)
    return torch.nn.functional.pad(x, flat)

kernel_name = 'concat_heads__ssm_conv_s4__grouped_kv_mqa__prompt'

def setup_inputs():

    _w = (lambda a: (lambda t: t.to(torch.bfloat16) if t.is_floating_point() else t)(torch.as_tensor(a)))
    def _A_setup():
        B, L, D, N = 1, 2048, 576, 128
        A_re = -(lambda _x: torch.exp(torch.as_tensor(_x)))((lambda *s: torch.randn(*s, dtype=torch.bfloat16) * 3.2)(D, N))
        A_im = torch.pi * (lambda *s: torch.randn(*s, dtype=torch.bfloat16) * 0.05)(D, N)
        B_re = (lambda *s: torch.randn(*s, dtype=torch.bfloat16) * 0.1)(D, N)
        B_im = (lambda *s: torch.randn(*s, dtype=torch.bfloat16) * 0.1)(D, N)
        C_re = (lambda *s: torch.randn(*s, dtype=torch.bfloat16) * 0.1)(D, N)
        C_im = (lambda *s: torch.randn(*s, dtype=torch.bfloat16) * 0.1)(D, N)
        log_dt = (lambda *s: torch.randn(*s, dtype=torch.bfloat16) * 0.05)(D) * 0.5 - 4.0
        Dskip = (lambda *s: torch.randn(*s, dtype=torch.bfloat16) * 0.05)(D)
        return {
            "u": _w((lambda *s: torch.randn(*s, dtype=torch.bfloat16) * 0.05)(B, L, D)),
            "A_re": _w(A_re),
            "A_im": _w(A_im),
            "B_re": _w(B_re),
            "B_im": _w(B_im),
            "C_re": _w(C_re),
            "C_im": _w(C_im),
            "log_dt": _w(log_dt),
            "Dskip": _w(Dskip),
        }
    _A_in = _A_setup()

    def _B_setup():
        B, S, D, H = 1, 2048, 576, 32
        Dh = D // H
        return {
            "x": _w((lambda *s: torch.randn(*s, dtype=torch.bfloat16) * 0.05)(B, S, D)),
            "wq": _w((lambda *s: torch.randn(*s, dtype=torch.bfloat16) * 0.8)(D, H * Dh)),
            "wk": _w((lambda *s: torch.randn(*s, dtype=torch.bfloat16) * 0.8)(D, Dh)),
            "wv": _w((lambda *s: torch.randn(*s, dtype=torch.bfloat16) * 0.05)(D, Dh)),
            "wo": _w((lambda *s: torch.randn(*s, dtype=torch.bfloat16) * 0.05)(H * Dh, D)),
        }
    _B_in = _B_setup()

    return {
        "A_u": _A_in["u"],
        "A_A_re": _A_in["A_re"],
        "A_A_im": _A_in["A_im"],
        "A_B_re": _A_in["B_re"],
        "A_B_im": _A_in["B_im"],
        "A_C_re": _A_in["C_re"],
        "A_C_im": _A_in["C_im"],
        "A_log_dt": _A_in["log_dt"],
        "A_Dskip": _A_in["Dskip"],
        "B_x": _B_in["x"],
        "B_wq": _B_in["wq"],
        "B_wk": _B_in["wk"],
        "B_wv": _B_in["wv"],
        "B_wo": _B_in["wo"],
    }

def reference(A_u, A_A_re, A_A_im, A_B_re, A_B_im, A_C_re, A_C_im, A_log_dt, A_Dskip, B_x, B_wq, B_wk, B_wv, B_wo):
    def _A_ref(u, A_re, A_im, B_re, B_im, C_re, C_im, log_dt, Dskip):
        Bsz, L, D = u.shape
        dt = torch.exp(log_dt)[:, None]
        A = A_re + 1j * A_im
        Bc = B_re + 1j * B_im
        Cc = C_re + 1j * C_im
        logdA = dt * A
        dB = dt * Bc
        l = (lambda x, dt: x.to(dt))(torch.arange(L), A_re.dtype)
        P = torch.exp(logdA[:, :, None] * l[None, None, :])
        K = torch.einsum("dn,dnl,dn->dl", Cc, P, dB).real
        L2 = 2 * L
        Kp = _pad(K, ((0, 0), (0, L)))
        up = (lambda x, *d: x.permute(*d))(u, 0, 2, 1)
        up = _pad(up, ((0, 0), (0, 0), (0, L)))
        Kf = (lambda x, axes=(-2,-1): torch.fft.rfft2(x.float(), dim=axes))(Kp[:, None, :], axes=(-2, -1))
        Uf = (lambda x, axes=(-2,-1): torch.fft.rfft2(x.float(), dim=axes))(up[:, :, None, :], axes=(-2, -1))
        Yf = Uf * Kf[None, :, :, :]
        y = (lambda x, s, axes=(-2,-1): torch.fft.irfft2(x, s=s, dim=axes).to(torch.bfloat16))(Yf, (1, L2), axes=(-2, -1))[:, :, 0, :L]
        y = (lambda x, *d: x.permute(*d))(y, 0, 2, 1) + u * Dskip[None, None, :]
        return y

    def _B_ref(x, wq, wk, wv, wo):
        B, S, D = x.shape
        H = 32
        Dh = D // H
        q = (lambda x, *d: x.permute(*d))((x @ wq).reshape(B, S, H, Dh), 0, 2, 1, 3)
        k = x @ wk
        v = x @ wv
        scores = torch.einsum("bhqd,bkd->bhqk", q, k) * (Dh ** -0.5)
        p = _softmax(scores, axis=-1)
        o = torch.einsum("bhqk,bkd->bhqd", p, v)
        return (lambda x, *d: x.permute(*d))(o, 0, 2, 1, 3).reshape(B, S, H * Dh) @ wo

    a = _A_ref(A_u, A_A_re, A_A_im, A_B_re, A_B_im, A_C_re, A_C_im, A_log_dt, A_Dskip)
    b = _B_ref(B_x, B_wq, B_wk, B_wv, B_wo)
    return torch.concatenate([a, b], axis=-1)
```

```cpp
#include <hip/hip_runtime.h>
#include <hip/hip_bf16.h>
#include <stdint.h>

typedef __bf16 bf16x8 __attribute__((ext_vector_type(8)));
typedef __bf16 bf16x16 __attribute__((ext_vector_type(16)));
typedef __bf16 bf16x2 __attribute__((ext_vector_type(2)));
typedef short s16x4 __attribute__((ext_vector_type(4)));
typedef short s16x8 __attribute__((ext_vector_type(8)));
typedef float f32x2 __attribute__((ext_vector_type(2)));
typedef float f32x4 __attribute__((ext_vector_type(4)));
typedef float f32x8 __attribute__((ext_vector_type(8)));
typedef float f32x16 __attribute__((ext_vector_type(16)));
typedef float f32x32 __attribute__((ext_vector_type(32)));
typedef unsigned u32x2 __attribute__((ext_vector_type(2)));
typedef unsigned u32x4 __attribute__((ext_vector_type(4)));
typedef unsigned short u16;
#define LAS __attribute__((address_space(3)))
#define DI __device__ __forceinline__

constexpr int SEQ = 2048, DM = 576, NH = 32, DH = 18, NS = 128, OUTLD = 2 * DM;
constexpr int CH_BYTES = 19456, KV_BYTES = 8 * CH_BYTES, CH_TAIL = 8192, CH_V = 9216, V_OCT = 320;
constexpr int CV_R = 0, CV_U0 = 4160, CV_U1 = 4160 + 4640, CV_BYTES = 14336, U_PAD = 224;
constexpr size_t WS_Q = 0;
constexpr size_t WS_O = WS_Q + (size_t)SEQ * DM * 2;
constexpr size_t WS_KV = WS_O + (size_t)SEQ * DM * 2;
constexpr size_t WS_MAX = WS_KV + KV_BYTES;
constexpr size_t WS_CV = WS_MAX + 1024;
constexpr float QSCALE = 0.23570226039551584f * 1.4426950408889634f;

DI float bf2f(u16 x) { return __uint_as_float((unsigned)x << 16); }
DI unsigned pk2(float lo, float hi) { f32x2 v = {lo, hi}; return __builtin_bit_cast(unsigned, __builtin_convertvector(v, bf16x2)); }
DI float rbf(float x) { return __uint_as_float(pk2(x, x) & 0xffff0000u); }
DI void glds16(const void* g, unsigned lds_base) {
  unsigned sv;
  asm volatile("s_mov_b32 %0, m0\n\ts_mov_b32 m0, %2\n\ts_nop 0\n\tglobal_load_lds_dwordx4 %1, off\n\ts_mov_b32 m0, %0"
               : "=&s"(sv) : "v"(g), "s"(lds_base) : "memory");
}
DI s16x4 trd(LAS const char* p) { return __builtin_amdgcn_ds_read_tr16_b64_v4i16((LAS s16x4*)p); }
#define MFMA16(a, b, c) __builtin_amdgcn_mfma_f32_16x16x32_bf16((a), (b), (c), 0, 0, 0)
#define MFMA32(a, b, c) __builtin_amdgcn_mfma_f32_32x32x16_bf16((a), (b), (c), 0, 0, 0)

DI void gemm_tile(const u16* __restrict__ A, const u16* __restrict__ W, u16* __restrict__ C, int ldc, float scale,
                  int m0, int n0, char* lds) {
  const int tid = threadIdx.x, lane = tid & 63, wid = __builtin_amdgcn_readfirstlane(tid >> 6);
  const int l15 = lane & 15, g = lane >> 4;
  const unsigned lds0 = (unsigned)(uintptr_t)lds;
  bf16x8 xa[18];
  const u16* ap = A + (size_t)(m0 + wid * 16 + l15) * DM + g * 8;
#pragma unroll
  for (int kk = 0; kk < 18; ++kk) asm volatile("global_load_dwordx4 %0, %1, off offset:%2" : "=v"(xa[kk]) : "v"(ap), "i"(kk * 64));
  {
    const int rr = lane >> 3, cp = lane & 7;
#pragma unroll
    for (int i = 0; i < 18; ++i) {
      const int piece = i * 4 + wid, row = piece * 8 + rr;
      const int ch = cp ^ (2 * (((row >> 1) & 1) | (((row >> 3) & 1) << 1)));
      glds16(W + (size_t)row * DM + n0 + ch * 8, lds0 + piece * 1024);
    }
  }
  asm volatile("s_waitcnt vmcnt(0)\n\ts_barrier" ::: "memory");
#pragma unroll
  for (int kk = 0; kk < 18; ++kk) asm volatile("" : "+v"(xa[kk]));
  f32x4 acc[4];
#pragma unroll
  for (int nt = 0; nt < 4; ++nt) acc[nt] = (f32x4){0.f, 0.f, 0.f, 0.f};
  const int q = l15 >> 2, p = l15 & 3;
  const int sw = 2 * (((q >> 1) & 1) | ((g & 1) << 1));
  LAS const char* wb = (LAS const char*)lds + (g * 8 + q) * 128 + (p & 1) * 8;
#pragma unroll
  for (int kk = 0; kk < 18; ++kk) {
#pragma unroll
    for (int nt = 0; nt < 4; ++nt) {
      const int ch = (2 * nt + (p >> 1)) ^ sw;
      s16x4 lo = trd(wb + kk * 4096 + ch * 16), hi = trd(wb + kk * 4096 + 512 + ch * 16);
      s16x8 wf = __builtin_shufflevector(lo, hi, 0, 1, 2, 3, 4, 5, 6, 7);
      acc[nt] = MFMA16(__builtin_bit_cast(bf16x8, wf), xa[kk], acc[nt]);
    }
  }
  u16* cp = C + (size_t)(m0 + wid * 16 + l15) * ldc + n0 + g * 4;
#pragma unroll
  for (int nt = 0; nt < 4; ++nt) {
    u32x2 v = {pk2(acc[nt][0] * scale, acc[nt][1] * scale), pk2(acc[nt][2] * scale, acc[nt][3] * scale)};
    *(u32x2*)(cp + nt * 16) = v;
  }
}

DI void kv_block(const u16* __restrict__ X, const u16* __restrict__ Wk, const u16* __restrict__ Wv, char* __restrict__ img,
                 float* __restrict__ mx, int blk, char* lds) {
  const int tid = threadIdx.x, lane = tid & 63, wid = tid >> 6, l15 = lane & 15, g = lane >> 4;
  const int key0 = blk * 64 + wid * 16;
  bf16x8 xa[18];
  const u16* ap = X + (size_t)(key0 + l15) * DM + g * 8;
#pragma unroll
  for (int kk = 0; kk < 18; ++kk) xa[kk] = *(const bf16x8*)(ap + kk * 32);
  for (int i = tid; i < 1296; i += 256) {
    u32x4 a = *(const u32x4*)(Wk + i * 8), b = *(const u32x4*)(Wv + i * 8);
#pragma unroll
    for (int e = 0; e < 4; ++e) {
      const int dw = i * 4 + e, k = dw / 9, dp = dw - k * 9;
      *(unsigned*)(lds + k * 96 + (dp < 8 ? dp * 4 : 64)) = a[e];
      *(unsigned*)(lds + k * 96 + (dp < 8 ? 32 + dp * 4 : 68)) = b[e];
    }
  }
  __syncthreads();
  f32x4 aK = {0.f, 0.f, 0.f, 0.f}, aV = aK, aKt = aK, aVt = aK;
  const int q = l15 >> 2, p = l15 & 3;
  LAS const char* wb = (LAS const char*)lds + (g * 8 + q) * 96 + p * 8;
#pragma unroll
  for (int kk = 0; kk < 18; ++kk) {
    bf16x8 wf[3];
#pragma unroll
    for (int nt = 0; nt < 3; ++nt) {
      s16x4 lo = trd(wb + kk * 3072 + nt * 32), hi = trd(wb + kk * 3072 + 384 + nt * 32);
      wf[nt] = __builtin_bit_cast(bf16x8, __builtin_shufflevector(lo, hi, 0, 1, 2, 3, 4, 5, 6, 7));
    }
    aK = MFMA16(wf[0], xa[kk], aK);
    aV = MFMA16(xa[kk], wf[1], aV);
    aKt = MFMA16(wf[2], xa[kk], aKt);
    aVt = MFMA16(xa[kk], wf[2], aVt);
  }
  char* cb = img + (key0 >> 8) * CH_BYTES;
  {
    const int key = key0 + l15;
    u32x2 v = {pk2(aK[0], aK[1]), pk2(aK[2], aK[3])};
    *(u32x2*)(cb + ((key >> 5) & 7) * 1024 + (g >> 1) * 512 + (key & 31) * 16 + (g & 1) * 8) = v;
    const unsigned t = pk2(aKt[0], aKt[1]);
    if (g == 0) *(unsigned*)(cb + CH_TAIL + (key & 63) * 16 + ((key >> 6) & 3) * 4) = t;
    float n2 = 0.f;
#pragma unroll
    for (int e = 0; e < 4; ++e) { const float r = rbf(aK[e]); n2 += r * r; }
    if (g == 0) { const float r0 = rbf(aKt[0]), r1 = rbf(aKt[1]); n2 += r0 * r0 + r1 * r1; }
    n2 += __shfl_xor(n2, 16); n2 += __shfl_xor(n2, 32);
#pragma unroll
    for (int o = 1; o < 16; o <<= 1) { const float other = __shfl_xor(n2, o); n2 = (other > n2 || other != other) ? other : n2; }
    if (lane == 0) mx[blk * 4 + wid] = n2;
  }
  {
    const int key = key0 + g * 4;
    char* vb = cb + CH_V + ((key >> 3) & 31) * V_OCT + (key & 4) * 2;
    const int row = (l15 & 3) | ((l15 & 4) << 1) | ((l15 & 8) >> 1);
    u32x2 v = {pk2(aV[0], aV[1]), pk2(aV[2], aV[3])};
    *(u32x2*)(vb + row * 16) = v;
    u32x2 t = {pk2(aVt[0], aVt[1]), pk2(aVt[2], aVt[3])};
    if (l15 == 4) { t[0] = 0x3f803f80u; t[1] = 0x3f803f80u; }
    if (l15 >= 2 && l15 <= 4) *(u32x2*)(vb + (14 + l15) * 16) = t;
    float am = 0.f;
#pragma unroll
    for (int e = 0; e < 4; ++e) {
      const float a = fabsf(aV[e]); am = (a > am || a != a) ? a : am;
      const float b = fabsf(aVt[e]); if (l15 == 2 || l15 == 3) am = (b > am || b != b) ? b : am;
    }
#pragma unroll
    for (int o = 1; o < 64; o <<= 1) { const float other = __shfl_xor(am, o); am = (other > am || other != other) ? other : am; }
    if (lane == 0) mx[128 + blk * 4 + wid] = am;
  }
}

struct cf { float re, im; };
DI cf cmul(cf a, cf b) { cf r; r.re = a.re * b.re - a.im * b.im; r.im = a.re * b.im + a.im * b.re; return r; }
DI cf cpow(float zr, float zi, float c) {
  const float e = __builtin_amdgcn_exp2f(zr * c), th = __builtin_amdgcn_fractf(zi * c);
  cf r; r.re = e * __builtin_amdgcn_cosf(th); r.im = e * __builtin_amdgcn_sinf(th); return r;
}
constexpr int KG_ROW = 528, KG_TAB = 53 * KG_ROW, KG_T = 2 * KG_TAB + 11 * KG_ROW;
DI void kgen_block(const u16* __restrict__ Are, const u16* __restrict__ Aim, const u16* __restrict__ Bre, const u16* __restrict__ Bim,
                   const u16* __restrict__ Cre, const u16* __restrict__ Cim, const u16* __restrict__ logdt, char* __restrict__ cv,
                   int blk, char* lds) {
  const int tid = threadIdx.x, lane = tid & 63, wid = tid >> 6, chl = wid >> 1, d = blk * 2 + chl, n = tid & 127;
  const int ix = d * NS + n;
  const float dt = rbf(expf(bf2f(logdt[d])));
  const float zr = (dt * bf2f(Are[ix])) * 1.4426950408889634f, zi = (dt * bf2f(Aim[ix])) * 0.15915494309189535f;
  const cf dB = {dt * bf2f(Bre[ix]), dt * bf2f(Bim[ix])}, Cc = {bf2f(Cre[ix]), bf2f(Cim[ix])};
  char* tb = lds + chl * KG_TAB + n * 4;
#define PUTA(row, v) *(unsigned*)(tb + (row) * KG_ROW) = pk2((v).re, -(v).im)
#define PUTB(row, v) *(unsigned*)(tb + (21 + (row)) * KG_ROW) = pk2((v).re, (v).im)
  const cf w1 = cpow(zr, zi, 1.f), w8 = cpow(zr, zi, 8.f), w256 = cpow(zr, zi, 256.f);
  const cf w2 = cmul(w1, w1), w3 = cmul(w2, w1), w4 = cmul(w2, w2), w5 = cmul(w4, w1), w6 = cmul(w4, w2), w7 = cmul(w4, w3);
  const cf one = {1.f, 0.f};
  PUTA(0, one); PUTA(1, w1); PUTA(2, w2); PUTA(3, w3); PUTA(4, w4); PUTA(5, w5); PUTA(6, w6); PUTA(7, w7);
  PUTA(8, w256); PUTA(9, cmul(w256, w2)); PUTA(10, cmul(w256, w4)); PUTA(11, cmul(w256, w6));
  const cf w512 = cmul(w256, w256), w768 = cmul(w512, w256), w1024 = cmul(w512, w512);
  PUTA(12, w512); PUTA(13, cmul(w512, w4)); PUTA(14, w768); PUTA(15, cmul(w768, w4));
  PUTA(16, w1024); PUTA(17, cmul(w1024, w256)); PUTA(18, cmul(w1024, w512)); PUTA(19, cmul(w1024, w768)); PUTA(20, cmul(w1024, w1024));
  const cf w16 = cmul(w8, w8), w32 = cmul(w16, w16);
  cf b0 = cmul(Cc, dB), b1 = cmul(b0, w8), b2 = cmul(b0, w16), b3 = cmul(b1, w16);
#pragma unroll
  for (int i = 0; i < 8; ++i) {
    PUTB(4 * i, b0); PUTB(4 * i + 1, b1); PUTB(4 * i + 2, b2); PUTB(4 * i + 3, b3);
    if (i < 7) { b0 = cmul(b0, w32); b1 = cmul(b1, w32); b2 = cmul(b2, w32); b3 = cmul(b3, w32); }
  }
  __syncthreads();
  const int l15 = lane & 15, g = lane >> 4, ct = wid & 1;
  const char* fb = lds + chl * KG_TAB + l15 * KG_ROW + (g & 1) * 256 + (g >> 1) * 128;
  f32x4 d0 = {0.f, 0.f, 0.f, 0.f}, d1 = d0;
#pragma unroll
  for (int kk = 0; kk < 8; ++kk) {
    const bf16x8 a0 = *(const bf16x8*)(fb + kk * 16), a1 = *(const bf16x8*)(fb + 16 * KG_ROW + kk * 16);
    const bf16x8 b = *(const bf16x8*)(fb + (21 + 16 * ct) * KG_ROW + kk * 16);
    d0 = MFMA16(a0, b, d0); d1 = MFMA16(a1, b, d1);
  }
  u16* T = (u16*)(lds + KG_T) + chl * 1424;
  const int lo8 = (16 * ct + l15) * 8;
#pragma unroll
  for (int r = 0; r < 4; ++r) {
    const int rho = 4 * g + r;
    const int base = rho < 8 ? rho : (rho < 12 ? 256 + 2 * (rho - 8) : 512 + 256 * ((rho - 12) >> 1) + 4 * (rho & 1));
    int bits = (int)(__float_as_uint((float)(base + lo8)) >> 16) - 0x3F7F; bits = bits < 0 ? 0 : bits;
    T[bits] = (u16)pk2(d0[r], d0[r]);
    const int l2 = 1024 + 256 * rho + lo8;
    if (l2 <= 2048 && rho <= 4) T[(int)(__float_as_uint((float)l2) >> 16) - 0x3F7F] = (u16)pk2(d1[r], d1[r]);
  }
  __syncthreads();
  const u16* T0 = (const u16*)(lds + KG_T);
  u32x4 o0, o1;
#pragma unroll
  for (int e = 0; e < 4; ++e) {
    const int s0 = 2047 - (8 * tid + 2 * e);
    const unsigned pb = pk2((float)s0, (float)(s0 - 1));
    int i0 = (int)(pb & 0xffffu) - 0x3F7F, i1 = (int)(pb >> 16) - 0x3F7F; i1 = i1 < 0 ? 0 : i1;
    o0[e] = (unsigned)T0[i0] | ((unsigned)T0[i1] << 16);
    o1[e] = (unsigned)T0[1424 + i0] | ((unsigned)T0[1424 + i1] << 16);
  }
  char* c0 = cv + (size_t)(blk * 2) * CV_BYTES;
  *(u32x4*)(c0 + CV_R + tid * 16) = o0;
  *(u32x4*)(c0 + CV_BYTES + CV_R + tid * 16) = o1;
  const u32x4 z = {0u, 0u, 0u, 0u};
  if (tid < 120) {
    const int c = tid / 60, j = tid - c * 60;
    const int off = j < 4 ? CV_R + 4096 + j * 16 : (j < 32 ? CV_U0 + (j - 4) * 16 : CV_U1 + (j - 32) * 16);
    *(u32x4*)(c0 + c * CV_BYTES + off) = z;
  }
}

DI void ut_wave(const u16* __restrict__ U, char* __restrict__ cv, int tile, int lane) {
  const int tb = tile / 9, db = tile - tb * 9, dg = lane & 7, tg = lane >> 3;
  const int t0 = tb * 64 + tg * 8, d0 = db * 64 + dg * 8;
  u32x4 r[9];
#pragma unroll
  for (int i = 0; i < 9; ++i) {
    const int t = t0 + i - 1;
    r[i] = *(const u32x4*)(U + (size_t)(t < 0 ? 0 : t) * DM + d0);
  }
  if (t0 == 0) r[0] = (u32x4){0u, 0u, 0u, 0u};
#pragma unroll
  for (int c = 0; c < 8; ++c) {
    const unsigned sel = (c & 1) ? 0x07060302u : 0x05040100u;
    u32x4 a, b;
#pragma unroll
    for (int j = 0; j < 4; ++j) {
      a[j] = __builtin_amdgcn_perm(r[2 * j + 1][c >> 1], r[2 * j][c >> 1], sel);
      b[j] = __builtin_amdgcn_perm(r[2 * j + 2][c >> 1], r[2 * j + 1][c >> 1], sel);
    }
    char* o = cv + (size_t)(d0 + c) * CV_BYTES + (U_PAD + t0) * 2;
    *(u32x4*)(o + CV_U0) = a;
    *(u32x4*)(o + CV_U1) = b;
  }
}

constexpr int K1_KG = 288, K1_KV = 32, K1_UT = 72, K1_Q = 288;
__global__ __launch_bounds__(256, 2) void k1_kernel(const u16* __restrict__ U, const u16* __restrict__ Are, const u16* __restrict__ Aim,
                                                    const u16* __restrict__ Bre, const u16* __restrict__ Bim, const u16* __restrict__ Cre,
                                                    const u16* __restrict__ Cim, const u16* __restrict__ logdt, const u16* __restrict__ X,
                                                    const u16* __restrict__ Wq, const u16* __restrict__ Wk, const u16* __restrict__ Wv,
                                                    char* __restrict__ ws) {
  __shared__ __attribute__((aligned(16))) char lds[73728];
  int b = blockIdx.x;
  if (b < K1_KV) { kv_block(X, Wk, Wv, ws + WS_KV, (float*)(ws + WS_MAX), b, lds); return; }
  b -= K1_KV;
  if (b < K1_Q) { gemm_tile(X, Wq, (u16*)(ws + WS_Q), DM, QSCALE, (b / 9) * 64, (b % 9) * 64, lds); return; }
  b -= K1_Q;
  if (b < K1_KG) { kgen_block(Are, Aim, Bre, Bim, Cre, Cim, logdt, ws + WS_CV, b, lds); return; }
  b -= K1_KG;
  ut_wave(U, ws + WS_CV, b * 4 + (threadIdx.x >> 6), threadIdx.x & 63);
}

#define SMF(a, b, c) __builtin_amdgcn_smfmac_f32_32x32x32_bf16((a), (b), (c), (int)0xdddddddd, 0, 0)
DI bf16x16 sub8(const f32x32& s, int o) {
  f32x8 t = {s[o], s[o + 1], s[o + 2], s[o + 3], s[o + 4], s[o + 5], s[o + 6], s[o + 7]};
  return __builtin_bit_cast(bf16x16, t);
}
DI f32x32 qk_pair(LAS const char* kp, const bf16x8& qm, s16x4 kt, s16x4 qt, const f32x16& cinit) {
  const bf16x8 k0 = *(LAS const bf16x8*)kp, k1 = *(LAS const bf16x8*)(kp + 1024);
  const f32x16 a = MFMA32(k0, qm, cinit), b = MFMA32(k1, qm, cinit);
  f32x32 s = __builtin_shufflevector(a, b, 0, 1, 2, 3, 4, 5, 6, 7, 8, 9, 10, 11, 12, 13, 14, 15, 16, 17, 18, 19, 20, 21, 22, 23, 24, 25, 26, 27, 28, 29, 30, 31);
  return __builtin_amdgcn_mfma_f32_32x32x4bf16_1k(kt, qt, s, 0, 0, 0);
}
DI void pv_pair(f32x32& s, LAS const char* vp, f32x16& o) {
#pragma unroll
  for (int i = 0; i < 4; ++i) {
    const bf16x8 v = *(LAS const bf16x8*)(vp + i * 2 * V_OCT);
#pragma unroll
    for (int j = 0; j < 8; ++j) s[8 * i + j] = __builtin_amdgcn_exp2f(s[8 * i + j]);
    o = SMF(v, sub8(s, 8 * i), o);
  }
}
__global__ __launch_bounds__(512, 2) void k2_kernel(const char* __restrict__ ws_in, u16* __restrict__ O) {
  __shared__ __attribute__((aligned(16))) char lds[KV_BYTES + 64];
  const int tid = threadIdx.x, lane = tid & 63, wid = __builtin_amdgcn_readfirstlane(tid >> 6);
  const int r32 = lane & 31, h = lane >> 5;
  const int qb = blockIdx.x >> 3, hg = blockIdx.x & 7;
  const int head = hg * 4 + (wid >> 1), qrow = qb * 64 + (wid & 1) * 32 + r32;
  const unsigned lds0 = (unsigned)(uintptr_t)lds;
  bf16x8 qm; unsigned qtl; f32x2 km, vm;
  {
    const char* qp = ws_in + WS_Q + ((size_t)qrow * DM + head * DH) * 2;
    const char* qp2 = qp + h * 16;
    const char* mp = ws_in + WS_MAX + lane * 8;
    asm volatile("global_load_dwordx4 %0, %1, off" : "=v"(qm) : "v"(qp2));
    asm volatile("global_load_dword %0, %1, off offset:32" : "=v"(qtl) : "v"(qp));
    asm volatile("global_load_dwordx2 %0, %1, off" : "=v"(km) : "v"(mp));
    asm volatile("global_load_dwordx2 %0, %1, off offset:512" : "=v"(vm) : "v"(mp));
  }
  {
    const char* src = ws_in + WS_KV + wid * 1024 + lane * 16;
#pragma unroll
    for (int k = 0; k < 19; ++k) glds16(src + k * 8192, lds0 + wid * 1024 + k * 8192);
  }
  asm volatile("s_waitcnt vmcnt(19)" : "+v"(qm), "+v"(qtl), "+v"(km), "+v"(vm));
  {
    float n2 = 0.f;
#pragma unroll
    for (int j = 0; j < 8; ++j) { const float x = (float)qm[j]; n2 += x * x; }
    n2 += __shfl_xor(n2, 32);
    const float t0 = __uint_as_float(qtl << 16), t1 = __uint_as_float(qtl & 0xffff0000u);
    n2 += t0 * t0 + t1 * t1;
    float kk = fmaxf(km[0], km[1]), vv = fmaxf(vm[0], vm[1]);
    bool bad = !(km[0] == km[0] && km[1] == km[1] && vm[0] == vm[0] && vm[1] == vm[1]);
#pragma unroll
    for (int o = 1; o < 64; o <<= 1) { kk = fmaxf(kk, __shfl_xor(kk, o)); vv = fmaxf(vv, __shfl_xor(vv, o)); }
    bad = bad || !(n2 * kk <= 3600.f) || !(vv <= 1.0995116e12f) || !(vv >= 9.094947e-13f);
    const bool any_bad = __any(bad);
    if (lane == 0) *(LAS int*)((LAS char*)lds + KV_BYTES + wid * 4) = any_bad ? 1 : 0;
  }
  asm volatile("s_waitcnt vmcnt(16) lgkmcnt(0)\n\ts_barrier" ::: "memory");
  int slow;
  {
    const u32x4 f0 = *(LAS const u32x4*)((LAS char*)lds + KV_BYTES), f1 = *(LAS const u32x4*)((LAS char*)lds + KV_BYTES + 16);
    slow = __builtin_amdgcn_readfirstlane((int)(f0[0] | f0[1] | f0[2] | f0[3] | f1[0] | f1[1] | f1[2] | f1[3]));
  }
  const s16x4 qa = __builtin_bit_cast(s16x4, (u32x2){qtl, 0u}), qc = __builtin_bit_cast(s16x4, (u32x2){0u, qtl});
  LAS const char* kbase = (LAS const char*)lds + lane * 16;
  LAS const char* vbase = (LAS const char*)lds + CH_V + h * V_OCT + (r32 < 19 ? r32 : r32 - 16) * 16;
  f32x16 cinit;
#pragma unroll
  for (int j = 0; j < 16; ++j) cinit[j] = 0.f;
  if (slow) {
    asm volatile("s_waitcnt vmcnt(0)\n\ts_barrier" ::: "memory");
    float m = -INFINITY;
    for (int c = 0; c < 8; ++c) {
      const u32x4 tl = *(LAS const u32x4*)(kbase + c * CH_BYTES + CH_TAIL);
#pragma unroll
      for (int p = 0; p < 4; ++p) {
        const s16x4 kt = __builtin_bit_cast(s16x4, (u32x2){tl[p & 2], tl[(p & 2) + 1]});
        const f32x32 s = qk_pair(kbase + c * CH_BYTES + p * 2048, qm, kt, (p & 1) ? qc : qa, cinit);
#pragma unroll
        for (int j = 0; j < 32; ++j) m = fmaxf(m, s[j]);
      }
    }
    m = fmaxf(m, __shfl_xor(m, 32));
#pragma unroll
    for (int j = 0; j < 16; ++j) cinit[j] = -m;
  }
  f32x16 o;
#pragma unroll
  for (int j = 0; j < 16; ++j) o[j] = 0.f;
  for (int c = 0; c < 8; ++c) {
    if (c == 1) asm volatile("s_waitcnt vmcnt(14)\n\ts_barrier" ::: "memory");
    if (c == 2) asm volatile("s_waitcnt vmcnt(0)\n\ts_barrier" ::: "memory");
    LAS const char* kc = kbase + c * CH_BYTES;
    LAS const char* vc = vbase + c * CH_BYTES;
    const u32x4 tl = *(LAS const u32x4*)(kc + CH_TAIL);
    const s16x4 kt0 = __builtin_bit_cast(s16x4, (u32x2){tl[0], tl[1]}), kt1 = __builtin_bit_cast(s16x4, (u32x2){tl[2], tl[3]});
    f32x32 sa = qk_pair(kc, qm, kt0, qa, cinit);
    f32x32 sb = qk_pair(kc + 2048, qm, kt0, qc, cinit);
    pv_pair(sa, vc, o);
    sa = qk_pair(kc + 4096, qm, kt1, qa, cinit);
    pv_pair(sb, vc + 8 * V_OCT, o);
    sb = qk_pair(kc + 6144, qm, kt1, qc, cinit);
    pv_pair(sa, vc + 16 * V_OCT, o);
    pv_pair(sb, vc + 24 * V_OCT, o);
  }
  const float l = __shfl(o[10], r32);
  const float inv = 1.0f / l;
  u16* op = O + (size_t)qrow * DM + head * DH;
  u32x4 v = {pk2(o[0] * inv, o[1] * inv), pk2(o[2] * inv, o[3] * inv), pk2(o[4] * inv, o[5] * inv), pk2(o[6] * inv, o[7] * inv)};
  struct __attribute__((packed, aligned(4))) V4 { u32x4 x; };
  ((V4*)(op + h * 8))->x = v;
  if (h == 0) *(unsigned*)(op + 16) = pk2(o[8] * inv, o[9] * inv);
}

DI void conv_block(const char* __restrict__ cv, const u16* __restrict__ Dskip, u16* __restrict__ out, int blk, char* lds) {
  const int tid = threadIdx.x, lane = tid & 63, wid = __builtin_amdgcn_readfirstlane(tid >> 6);
  const unsigned lds0 = (unsigned)(uintptr_t)lds;
  {
    const char* src = cv + (size_t)blk * 2 * CV_BYTES + wid * 1024 + lane * 16;
#pragma unroll
    for (int k = 0; k < 7; ++k) glds16(src + k * 4096, lds0 + wid * 1024 + k * 4096);
  }
  asm volatile("s_waitcnt vmcnt(0)\n\ts_barrier" ::: "memory");
  const int l15 = lane & 15, g = lane >> 4, chl = wid >> 1, half = wid & 1;
  LAS const char* cb = (LAS const char*)lds + chl * CV_BYTES;
  LAS const unsigned* rb = (LAS const unsigned*)(cb + CV_R) + 1023 - l15 + 4 * g - 64 * half;
  LAS const char* ub = cb + ((l15 & 1) ? CV_U1 : CV_U0) + (U_PAD + 32 * (l15 >> 1) + 8 * g) * 2 - 256 * half;
  f32x4 acc[8];
#pragma unroll
  for (int c = 0; c < 8; ++c) acc[c] = (f32x4){0.f, 0.f, 0.f, 0.f};
#pragma unroll
  for (int mi = 0; mi < 4; ++mi) {
    bf16x8 uf[8];
#pragma unroll
    for (int z = 0; z < 8; ++z) uf[z] = *(LAS const bf16x8*)(ub + (8 * z - mi) * 64);
#pragma unroll
    for (int m1 = 0; m1 < 8; ++m1) {
      LAS const unsigned* rp = rb - 16 * (8 * m1 + mi);
      const u32x4 kv = {rp[0], rp[1], rp[2], rp[3]};
      const bf16x8 kf = __builtin_bit_cast(bf16x8, kv);
#pragma unroll
      for (int c = m1; c < 8; ++c) acc[c] = MFMA16(uf[c - m1], kf, acc[c]);
    }
  }
  LAS float* red = (LAS float*)((LAS char*)lds + 2 * CV_BYTES) + wid * 2048;
#pragma unroll
  for (int c = 0; c < 8; ++c) {
    *(LAS f32x2*)(red + 256 * c + 64 * g + 2 * l15) = (f32x2){acc[c][0], acc[c][1]};
    *(LAS f32x2*)(red + 256 * c + 64 * g + 32 + 2 * l15) = (f32x2){acc[c][2], acc[c][3]};
  }
  __syncthreads();
  const unsigned dd = *(const unsigned*)(Dskip + blk * 2);
  const float ds0 = __uint_as_float(dd << 16), ds1 = __uint_as_float(dd & 0xffff0000u);
  LAS const float* rd = (LAS const float*)((LAS char*)lds + 2 * CV_BYTES);
  LAS const u16* u0 = (LAS const u16*)((LAS char*)lds + CV_U1) + U_PAD;
  LAS const u16* u1 = (LAS const u16*)((LAS char*)lds + CV_BYTES + CV_U1) + U_PAD;
#pragma unroll
  for (int i = 0; i < 8; ++i) {
    const int t = tid + 256 * i;
    const float y0 = rbf(rd[t] + rd[2048 + t]) + rbf(bf2f(u0[t]) * ds0);
    const float y1 = rbf(rd[4096 + t] + rd[6144 + t]) + rbf(bf2f(u1[t]) * ds1);
    *(unsigned*)(out + (size_t)t * OUTLD + blk * 2) = pk2(y0, y1);
  }
}

__global__ __launch_bounds__(256, 2) void k3_kernel(const char* __restrict__ ws, const u16* __restrict__ Wo, const u16* __restrict__ Dskip,
                                                    u16* __restrict__ out) {
  __shared__ __attribute__((aligned(16))) char lds[73728];
  int b = blockIdx.x;
  if (b < 288) { conv_block(ws + WS_CV, Dskip, out, b, lds); return; }
  b -= 288;
  gemm_tile((const u16*)(ws + WS_O), Wo, out + DM, OUTLD, 1.0f, (b / 9) * 64, (b % 9) * 64, lds);
}

extern "C" void kernel_launch(void* const* d_in, const int* in_sizes, int n_in, void* d_out, int out_size, void* d_ws, size_t ws_size,
                              hipStream_t stream) {
  const u16* U = (const u16*)d_in[0];
  char* ws = (char*)d_ws;
  k1_kernel<<<K1_KV + K1_Q + K1_KG + K1_UT, 256, 0, stream>>>(U, (const u16*)d_in[1], (const u16*)d_in[2], (const u16*)d_in[3],
                                                               (const u16*)d_in[4], (const u16*)d_in[5], (const u16*)d_in[6],
                                                               (const u16*)d_in[7], (const u16*)d_in[9], (const u16*)d_in[10],
                                                               (const u16*)d_in[11], (const u16*)d_in[12], ws);
  k2_kernel<<<256, 512, 0, stream>>>(ws, (u16*)(ws + WS_O));
  k3_kernel<<<576, 256, 0, stream>>>(ws, (const u16*)d_in[13], (const u16*)d_in[8], (u16*)d_out);
}
```

```cpp
#include <hip/hip_runtime.h>
#include <hip/hip_bf16.h>
#include <stdint.h>

typedef __bf16 bf16x8 __attribute__((ext_vector_type(8)));
typedef __bf16 bf16x16 __attribute__((ext_vector_type(16)));
typedef __bf16 bf16x2 __attribute__((ext_vector_type(2)));
typedef short s16x4 __attribute__((ext_vector_type(4)));
typedef short s16x8 __attribute__((ext_vector_type(8)));
typedef float f32x2 __attribute__((ext_vector_type(2)));
typedef float f32x4 __attribute__((ext_vector_type(4)));
typedef float f32x8 __attribute__((ext_vector_type(8)));
typedef float f32x16 __attribute__((ext_vector_type(16)));
typedef float f32x32 __attribute__((ext_vector_type(32)));
typedef unsigned u32x2 __attribute__((ext_vector_type(2)));
typedef unsigned u32x4 __attribute__((ext_vector_type(4)));
typedef unsigned short u16;
#define LAS __attribute__((address_space(3)))
#define DI __device__ __forceinline__
typedef LAS char* lptr;

constexpr int SEQ = 2048, DM = 576, NS = 128, OUTLD = 2 * DM, ROWB = DM * 2;
constexpr int CH_BYTES = 19456, KV_BYTES = 8 * CH_BYTES, CH_TAIL = 8192, CH_V = 9216, V_OCT = 320;
constexpr int R_BYTES = 4160, CV_U = 13312, CV_UCH = 4640 + 4544, CV_U1 = 4640, U_PADB = 448, UT_ROW = 4224;
constexpr size_t WS_E = 0, WS_F = 4096, WS_KV = WS_F + 256 * 128;
constexpr size_t WS_UT = WS_KV + KV_BYTES;
constexpr size_t WS_YT = WS_UT + (size_t)DM * UT_ROW;
constexpr size_t WS_O = WS_YT + (size_t)DM * SEQ * 2;
constexpr size_t WS_DBG = 64u << 20;
constexpr int LDS_BYTES = 163840, KV_LDS = 0, SPARE = KV_BYTES, CVB = 6 * CH_BYTES;
constexpr int A_UT = 0, A_WKV = 9216, A_X0 = 18432, A_Q = 36864, Q_ROW = 148, A_CTRL = A_Q + 64 * Q_ROW, A_SCR = A_CTRL + 128, A_W = 80896, B_CTRL = LDS_BYTES - 64;
constexpr int CV_SCR = 0, C_REM = 73728, C_YT = 82944, C_RED = C_YT + 9216, C_OX = C_RED + 20480, C_RED2 = C_OX + 24576;
static_assert(A_SCR + 3 * 11264 <= A_W && A_SCR % 16 == 0 && A_W + 82944 == LDS_BYTES && CVB + CV_U + 3 * CV_UCH <= B_CTRL && C_OX + 24576 <= 7 * CH_BYTES + 1024 &&
              C_RED2 + 20480 <= B_CTRL, "LDS map");
constexpr float QSCALE = 0.23570226039551584f * 1.4426950408889634f;
constexpr unsigned SENT = 0xffffffffu;
constexpr int SPIN_MAX = 1 << 16;
#define POLL1 12
#define POLL0 8
#define OSLEEP 20
#define ORETRY 8

DI float bf2f(u16 x) { return __uint_as_float((unsigned)x << 16); }
DI float bflo(unsigned x) { return __uint_as_float(x << 16); }
DI float bfhi(unsigned x) { return __uint_as_float(x & 0xffff0000u); }
DI unsigned pk2(float lo, float hi) { f32x2 v = {lo, hi}; return __builtin_bit_cast(unsigned, __builtin_convertvector(v, bf16x2)); }
DI float rbf(float x) { return bfhi(pk2(x, x)); }
DI unsigned nosent(unsigned x) { return x < 0xfffffffeu ? x : 0xfffffffeu; }
#define GLDS(SC) "s_mov_b32 %0, m0\n\ts_mov_b32 m0, %2\n\ts_nop 0\n\tglobal_load_lds_dwordx4 %1, off" SC "\n\ts_mov_b32 m0, %0"
DI void glds16(const void* g, unsigned l) { unsigned sv; asm volatile(GLDS("") : "=&s"(sv) : "v"(g), "s"(l) : "memory"); }
DI void glds16c(const void* g, unsigned l) { unsigned sv; asm volatile(GLDS(" sc1") : "=&s"(sv) : "v"(g), "s"(l) : "memory"); }
#define GLDSS(SC) "s_mov_b32 %0, m0\n\ts_mov_b32 m0, %3\n\ts_nop 0\n\tglobal_load_lds_dwordx4 %1, %2" SC "\n\ts_mov_b32 m0, %0"
DI void gldss(unsigned voff, const void* sbase, unsigned l) { unsigned sv; asm volatile(GLDSS("") : "=&s"(sv) : "v"(voff), "s"(sbase), "s"(l) : "memory"); }
DI void gldssc(unsigned voff, const void* sbase, unsigned l) { unsigned sv; asm volatile(GLDSS(" sc1") : "=&s"(sv) : "v"(voff), "s"(sbase), "s"(l) : "memory"); }
DI void glds9(const unsigned (&vo)[9], const void* sbase, unsigned l) {
  unsigned sv;
  asm volatile("s_mov_b32 %0, m0\n\ts_mov_b32 m0, %11\n\ts_nop 0\n\t"
               "global_load_lds_dwordx4 %1, %10\n\tglobal_load_lds_dwordx4 %2, %10 offset:1024\n\t"
               "global_load_lds_dwordx4 %3, %10 offset:2048\n\tglobal_load_lds_dwordx4 %4, %10 offset:3072\n\t"
               "s_add_u32 m0, m0, 0x1000\n\ts_nop 0\n\t"
               "global_load_lds_dwordx4 %5, %10\n\tglobal_load_lds_dwordx4 %6, %10 offset:1024\n\t"
               "global_load_lds_dwordx4 %7, %10 offset:2048\n\tglobal_load_lds_dwordx4 %8, %10 offset:3072\n\t"
               "s_add_u32 m0, m0, 0x1000\n\ts_nop 0\n\tglobal_load_lds_dwordx4 %9, %10\n\ts_mov_b32 m0, %0"
               : "=&s"(sv) : "v"(vo[0]), "v"(vo[1]), "v"(vo[2]), "v"(vo[3]), "v"(vo[4]), "v"(vo[5]), "v"(vo[6]), "v"(vo[7]), "v"(vo[8]),
                 "s"(sbase), "s"(l) : "memory", "scc");
}
DI void glds4c(unsigned voff, const void* sbase, unsigned l) {
  unsigned sv;
  asm volatile("s_mov_b32 %0, m0\n\ts_mov_b32 m0, %3\n\ts_nop 0\n\t"
               "global_load_lds_dwordx4 %1, %2 sc1\n\tglobal_load_lds_dwordx4 %1, %2 offset:1024 sc1\n\t"
               "global_load_lds_dwordx4 %1, %2 offset:2048 sc1\n\tglobal_load_lds_dwordx4 %1, %2 offset:3072 sc1\n\ts_mov_b32 m0, %0"
               : "=&s"(sv) : "v"(voff), "s"(sbase), "s"(l) : "memory");
}
DI void slice_offsets(unsigned (&vo)[9], int lane) {
#pragma unroll
  for (int r = 0; r < 9; ++r) { const int c = r * 64 + lane, row = c / 9; vo[r] = row * (ROWB - 144) + c * 16 - (r & 3) * 1024; }
}
DI void kv_group(const char* ws, unsigned lds0, int slot, int rot, int grp, unsigned l16) {
  const int off = grp < 4 ? grp * 4096 : 15360;
  glds4c(l16, ws + WS_KV + ((slot + rot) & 7) * CH_BYTES + off, lds0 + KV_LDS + slot * CH_BYTES + off);
}
DI void lds_wait(LAS unsigned* p, unsigned n) {
  while (__hip_atomic_load(p, __ATOMIC_RELAXED, __HIP_MEMORY_SCOPE_WORKGROUP) < n) __builtin_amdgcn_s_sleep(1);
  asm volatile("" ::: "memory");
}
DI void st16c(void* p, u32x4 v) { asm volatile("global_store_dwordx4 %0, %1, off sc1\n\ts_nop 1" ::"v"(p), "v"(v) : "memory"); }
DI void st8c(void* p, u32x2 v) { asm volatile("global_store_dwordx2 %0, %1, off sc1" ::"v"(p), "v"(v) : "memory"); }
DI void st4c(void* p, unsigned v) { asm volatile("global_store_dword %0, %1, off sc1" ::"v"(p), "v"(v) : "memory"); }
#define LD4(d, p, SC) asm volatile("global_load_dword %0, %1, off" SC : "=v"(d) : "v"(p) : "memory")
#define LD8(d, p, SC) asm volatile("global_load_dwordx2 %0, %1, off" SC : "=v"(d) : "v"(p) : "memory")
#define LD16(d, p, SC) asm volatile("global_load_dwordx4 %0, %1, off" SC : "=v"(d) : "v"(p) : "memory")
#define PIN(x) asm volatile("" : "+v"(x))
#define VMWAIT(n) asm volatile("s_waitcnt vmcnt(" #n ")" ::: "memory")
#define BAR() asm volatile("s_waitcnt lgkmcnt(0)\n\ts_barrier" ::: "memory")
#define BARVM(n) asm volatile("s_waitcnt vmcnt(" #n ") lgkmcnt(0)\n\ts_barrier" ::: "memory")
#define CFENCE() asm volatile("" ::: "memory")
DI s16x4 trd(lptr p) { return __builtin_amdgcn_ds_read_tr16_b64_v4i16((LAS s16x4*)p); }
DI bf16x8 trd2(lptr a, lptr b) { return __builtin_bit_cast(bf16x8, __builtin_shufflevector(trd(a), trd(b), 0, 1, 2, 3, 4, 5, 6, 7)); }
#define MFMA16(a, b, c) __builtin_amdgcn_mfma_f32_16x16x32_bf16((a), (b), (c), 0, 0, 0)
#define MFMA32(a, b, c) __builtin_amdgcn_mfma_f32_32x32x16_bf16((a), (b), (c), 0, 0, 0)
#define SMF(a, b, c) __builtin_amdgcn_smfmac_f32_32x32x32_bf16((a), (b), (c), (int)0xdddddddd, 0, 0)
template <int CTRL> DI unsigned dppu(unsigned x) { return (unsigned)__builtin_amdgcn_update_dpp((int)x, (int)x, CTRL, 0xf, 0xf, false); }
DI unsigned umax(unsigned a, unsigned b) { return a > b ? a : b; }
DI unsigned max8(unsigned x) { x = umax(x, dppu<0xB1>(x)); x = umax(x, dppu<0x4E>(x)); return umax(x, dppu<0x141>(x)); }
DI unsigned max16(unsigned x) { x = max8(x); return umax(x, dppu<0x140>(x)); }

#define STAMP(i)

struct cf2 { f32x2 re, im; };
DI cf2 cmul(cf2 a, cf2 b) { cf2 r; r.re = a.re * b.re - a.im * b.im; r.im = a.re * b.im + a.im * b.re; return r; }
DI cf2 cpow(f32x2 zr, f32x2 zi, float c) {
  cf2 r;
#pragma unroll
  for (int i = 0; i < 2; ++i) {
    const float e = __builtin_amdgcn_exp2f(zr[i] * c), th = __builtin_amdgcn_fractf(zi[i] * c);
    r.re[i] = e * __builtin_amdgcn_cosf(th); r.im[i] = e * __builtin_amdgcn_sinf(th);
  }
  return r;
}
DI f32x2 up2(unsigned x) { return (f32x2){bflo(x), bfhi(x)}; }
constexpr int KG_ROW = 528;
DI void kgen_wave(unsigned are, unsigned aim, unsigned bre, unsigned bim, unsigned cre, unsigned cim, float ldt, lptr img,
                  LAS unsigned* gate, lptr tb, int lane) {
  const float dt = rbf(expf(ldt));
  const f32x2 zr = (dt * up2(are)) * 1.4426950408889634f, zi = (dt * up2(aim)) * 0.15915494309189535f;
  const cf2 dB = {dt * up2(bre), dt * up2(bim)}, Cc = {up2(cre), up2(cim)};
  lptr tw = tb + lane * 8;
#define PUT(row, v) do { const cf2 v_ = (v); *(LAS u32x2*)(tw + (row) * KG_ROW) = (u32x2){pk2(v_.re[0], v_.im[0]), pk2(v_.re[1], v_.im[1])}; } while (0)
  const cf2 w1 = cpow(zr, -zi, 1.f), w8 = cpow(zr, zi, 8.f), w256 = cpow(zr, -zi, 256.f);
  const cf2 w2 = cmul(w1, w1), w3 = cmul(w2, w1), w4 = cmul(w2, w2), w6 = cmul(w4, w2);
  *(LAS u32x2*)tw = (u32x2){0x3f80u, 0x3f80u};
  PUT(1, w1); PUT(2, w2); PUT(3, w3); PUT(4, w4); PUT(5, cmul(w4, w1)); PUT(6, w6); PUT(7, cmul(w4, w3));
  PUT(8, w256); PUT(9, cmul(w256, w2)); PUT(10, cmul(w256, w4)); PUT(11, cmul(w256, w6));
  const cf2 w512 = cmul(w256, w256), w768 = cmul(w512, w256), w1024 = cmul(w512, w512);
  PUT(12, w512); PUT(13, cmul(w512, w4)); PUT(14, w768); PUT(15, cmul(w768, w4));
  PUT(16, w1024); PUT(17, cmul(w1024, w256)); PUT(18, cmul(w1024, w512)); PUT(19, cmul(w1024, w768)); PUT(20, cmul(w1024, w1024));
  CFENCE();
  const int l15 = lane & 15, g = lane >> 4;
  lptr fb = tb + l15 * KG_ROW + (g & 1) * 256 + (g >> 1) * 128;
  lptr fb1 = tb + (16 + (l15 & 7)) * KG_ROW + (g & 1) * 256 + (g >> 1) * 128;
  bf16x8 a0[8], a1[8];
#pragma unroll
  for (int kk = 0; kk < 8; ++kk) { a0[kk] = *(LAS const bf16x8*)(fb + kk * 16); a1[kk] = *(LAS const bf16x8*)(fb1 + kk * 16); }
  CFENCE();
  const cf2 w16 = cmul(w8, w8), w32 = cmul(w16, w16);
  cf2 b0 = cmul(Cc, dB), b1 = cmul(b0, w8), b2 = cmul(b0, w16), b3 = cmul(b1, w16);
  f32x4 d[2][2];
#pragma unroll
  for (int ct = 0; ct < 2; ++ct) {
#pragma unroll
    for (int i = 0; i < 4; ++i) {
      PUT(4 * i, b0); PUT(4 * i + 1, b1); PUT(4 * i + 2, b2); PUT(4 * i + 3, b3);
      if (ct == 0 || i < 3) { b0 = cmul(b0, w32); b1 = cmul(b1, w32); b2 = cmul(b2, w32); b3 = cmul(b3, w32); }
    }
    CFENCE();
    bf16x8 bf[8];
#pragma unroll
    for (int kk = 0; kk < 8; ++kk) bf[kk] = *(LAS const bf16x8*)(fb + kk * 16);
    CFENCE();
    d[ct][0] = (f32x4){0.f, 0.f, 0.f, 0.f}; d[ct][1] = d[ct][0];
#pragma unroll
    for (int kk = 0; kk < 8; ++kk) { d[ct][0] = MFMA16(a0[kk], bf[kk], d[ct][0]); d[ct][1] = MFMA16(a1[kk], bf[kk], d[ct][1]); }
  }
  {
    const int e0 = g < 2 ? 8 * l15 + 4 * g : (g == 2 ? 256 + 4 * l15 : 384 + 2 * l15), es = g < 2 ? 128 : (g == 2 ? 64 : 32), e1 = g == 3 ? 64 : 2;
    const int f0 = g == 0 ? 512 + l15 : (lane == 16 ? 640 : 700);
#pragma unroll
    for (int ct = 0; ct < 2; ++ct) {
      const lptr p = tb + (e0 + ct * es) * 2;
      *(LAS unsigned*)p = pk2(d[ct][0][0], d[ct][0][1]); *(LAS unsigned*)(p + e1 * 2) = pk2(d[ct][0][2], d[ct][0][3]);
      LAS u16* q = (LAS u16*)tb + f0 + ct * 16;
      const unsigned x0 = pk2(d[ct][1][0], d[ct][1][1]), x1 = pk2(d[ct][1][2], d[ct][1][3]);
      q[0] = (u16)x0; q[32] = (u16)(x0 >> 16); q[64] = (u16)x1; q[96] = (u16)(x1 >> 16);
    }
  }
  lds_wait(gate, 7u);
  LAS const u16* C = (LAS const u16*)tb;
  const int rl = 63 - lane;
#pragma unroll
  for (int r = 0; r < 2; ++r) {
    const unsigned a = C[512 + 64 * (1 - r) + rl], bb = C[513 + 64 * (1 - r) + rl];
    const unsigned aa = a | (a << 16), b2x = bb | (bb << 16);
    *(LAS u32x4*)(img + r * 1024 + lane * 16) = (u32x4){b2x, (rl & 1) ? b2x : (bb | (a << 16)), aa, aa};
  }
  {
    const unsigned ab = *(LAS const unsigned*)(C + 384 + 2 * rl), c = C[386 + 2 * rl];
    *(LAS u32x4*)(img + 2048 + lane * 16) = (u32x4){c | (c << 16), __builtin_amdgcn_perm(ab, ab, 0x03020302u), __builtin_amdgcn_perm(ab, ab, 0x01000302u),
                                                __builtin_amdgcn_perm(ab, ab, 0x01000100u)};
  }
  {
    const bool hi = lane < 32;
    const int e = hi ? 128 + 4 * rl : 8 * rl;
    const u32x2 w01 = *(LAS const u32x2*)(C + e), w23 = *(LAS const u32x2*)(C + e + 4);
    const unsigned o0 = hi ? __builtin_amdgcn_perm(w01[1], w23[0], 0x07060100u) : __builtin_amdgcn_perm(w23[1], w23[1], 0x01000302u);
    const unsigned o1 = __builtin_amdgcn_perm(w23[0], w01[1], hi ? 0x01000100u : 0x05040706u);
    const unsigned o2 = __builtin_amdgcn_perm(w01[1], w01[0], hi ? 0x03020504u : 0x05040706u);
    const unsigned o3 = __builtin_amdgcn_perm(w01[0], w01[0], hi ? 0x01000100u : 0x01000302u);
    *(LAS u32x4*)(img + 3072 + lane * 16) = (u32x4){o0, o1, o2, o3};
  }
  if (lane < 4) *(LAS u32x4*)(img + 4096 + lane * 16) = (u32x4){0u, 0u, 0u, 0u};
}

template <int NT, int K0, int K1> DI void qstage(lptr wq, const bf16x8 (&xa)[18], f32x4 (&acc)[NT]) {
  bf16x8 wf[K1 - K0][NT];
#pragma unroll
  for (int kk = K0; kk < K1; ++kk)
#pragma unroll
    for (int i = 0; i < NT; ++i) wf[kk - K0][i] = trd2(wq + kk * 4608 + i * 32, wq + kk * 4608 + 144 + i * 32);
  CFENCE();
#pragma unroll
  for (int kk = K0; kk < K1; ++kk)
#pragma unroll
    for (int i = 0; i < NT; ++i) acc[i] = MFMA16(wf[kk - K0][i], xa[kk], acc[i]);
}
template <int NT> DI void qgemm(lptr lds, LAS unsigned* ctrl, const bf16x8 (&xa)[18], int mt, int lane) {
  const int l15 = lane & 15, g = lane >> 4;
  constexpr int nt0 = NT == 2 ? 3 : 0;
  f32x4 acc[NT];
#pragma unroll
  for (int i = 0; i < NT; ++i) acc[i] = (f32x4){0.f, 0.f, 0.f, 0.f};
  const lptr wq = lds + A_W + (g * 8 + (l15 >> 2) * 2) * 144 + (l15 & 3) * 8 + nt0 * 32;
  lds_wait(&ctrl[4], 1u); qstage<NT, 0, 2>(wq, xa, acc); qstage<NT, 2, 4>(wq, xa, acc); qstage<NT, 4, 6>(wq, xa, acc);
  lds_wait(&ctrl[4], 2u); qstage<NT, 6, 8>(wq, xa, acc); qstage<NT, 8, 10>(wq, xa, acc);
  lds_wait(&ctrl[4], 3u); qstage<NT, 10, 12>(wq, xa, acc); qstage<NT, 12, 14>(wq, xa, acc);
  lds_wait(&ctrl[4], 4u); qstage<NT, 14, 16>(wq, xa, acc);
  lds_wait(&ctrl[4], 5u); qstage<NT, 16, 18>(wq, xa, acc);
  LAS unsigned* qw = (LAS unsigned*)(lds + A_Q + (mt * 16 + l15) * Q_ROW + nt0 * 32 + g * 8);
#pragma unroll
  for (int i = 0; i < NT; ++i)
    if (nt0 + i < 4 || g < 2) { qw[8 * i] = pk2(acc[i][0] * QSCALE, acc[i][1] * QSCALE); qw[8 * i + 1] = pk2(acc[i][2] * QSCALE, acc[i][3] * QSCALE); }
  CFENCE();
  if (lane == 0) __hip_atomic_fetch_add(&ctrl[5], 1u, __ATOMIC_RELAXED, __HIP_MEMORY_SCOPE_WORKGROUP);
}

DI f32x32 qk_tile(lptr kp, const bf16x8& q0, const bf16x8& q1, unsigned kt, u32x2 qa) {
  const bf16x8 k = *(LAS const bf16x8*)kp;
  f32x16 z;
#pragma unroll
  for (int j = 0; j < 16; ++j) z[j] = 0.f;
  const f32x16 a = MFMA32(k, q0, z), b = MFMA32(k, q1, z);
  f32x32 s = __builtin_shufflevector(a, b, 0, 1, 2, 3, 4, 5, 6, 7, 8, 9, 10, 11, 12, 13, 14, 15, 16, 17, 18, 19, 20, 21, 22, 23, 24, 25, 26, 27, 28, 29, 30, 31);
  return __builtin_amdgcn_mfma_f32_32x32x4bf16_1k(__builtin_bit_cast(s16x4, (u32x2){kt, 0u}), __builtin_bit_cast(s16x4, qa), s, 0, 0, 0);
}
DI bf16x16 sub8(const f32x32& s, int o) {
  f32x8 t = {s[o], s[o + 1], s[o + 2], s[o + 3], s[o + 4], s[o + 5], s[o + 6], s[o + 7]};
  return __builtin_bit_cast(bf16x16, t);
}

__global__ __launch_bounds__(768, 3) void fused_kernel(const u16* __restrict__ U, const u16* __restrict__ Are, const u16* __restrict__ Aim,
                                                       const u16* __restrict__ Bre, const u16* __restrict__ Bim, const u16* __restrict__ Cre,
                                                       const u16* __restrict__ Cim, const u16* __restrict__ logdt, const u16* __restrict__ Dskip,
                                                       const u16* __restrict__ X, const u16* __restrict__ Wq, const u16* __restrict__ Wk,
                                                       const u16* __restrict__ Wv, const u16* __restrict__ Wo, u16* __restrict__ out,
                                                       char* __restrict__ ws) {
  __shared__ __attribute__((aligned(16))) char lds_raw[LDS_BYTES];
  const lptr lds = (lptr)lds_raw;
  const unsigned lds0 = (unsigned)(uintptr_t)lds_raw;
  STAMP(0);
  const int tid = threadIdx.x, lane = tid & 63, wid = __builtin_amdgcn_readfirstlane(tid >> 6);
  const int l15 = lane & 15, g = lane >> 4, r32 = lane & 31, h = lane >> 5, q4 = l15 >> 2, p4 = l15 & 3;
  const int b = blockIdx.x, qb = (b & 7) * 4 + (b >> 6), hg = (b >> 3) & 7;
  const int ce = 512 + (b >> 2), cq = b & 3, mt = wid & 3;
  char* const myflag = ws + WS_F + (qb * 8 + hg) * 128;
  LAS unsigned* ctrl = (LAS unsigned*)(lds + A_CTRL);
  LAS unsigned* bctl = (LAS unsigned*)(lds + B_CTRL);
#define LADD(i) do { if (lane == 0) __hip_atomic_fetch_add(&ctrl[i], 1u, __ATOMIC_RELAXED, __HIP_MEMORY_SCOPE_WORKGROUP); } while (0)
#define LSET(i, v) do { if (lane == 0) __hip_atomic_store(&ctrl[i], (v), __ATOMIC_RELAXED, __HIP_MEMORY_SCOPE_WORKGROUP); } while (0)
  if (tid < 16) ctrl[tid] = 0u;
  unsigned ep;
  { const char* p = ws + WS_E; LD4(ep, p, " sc1"); }
#define TAG() (ep * 2654435761u + 0x9e3779b9u)
  bf16x8 wf[6][4];
  const int kt = wid >> 2;
  auto load_wf = [&]() {
    const int sw = 2 * (((q4 >> 1) & 1) | ((g & 1) << 1));
    const lptr wb = lds + kt * 24576 + (g * 8 + q4) * 128 + (p4 & 1) * 8;
#pragma unroll
    for (int nt = 0; nt < 4; ++nt) {
      const lptr p = wb + (((2 * nt + (p4 >> 1)) ^ sw) * 16);
#pragma unroll
      for (int kk = 0; kk < 6; ++kk) wf[kk][nt] = trd2(p + kk * 4096, p + kk * 4096 + 512);
    }
  };
  auto any_bad = [&]() {
    const u32x4 a = *(LAS const u32x4*)bctl, c = *(LAS const u32x4*)(bctl + 4);
    return __builtin_amdgcn_readfirstlane((int)(a[0] | a[1] | a[2] | a[3] | c[0] | c[1] | c[2] | c[3]));
  };

  if (wid < 8) {
    if (wid == 0) {
      unsigned vo[9];
      slice_offsets(vo, lane);
      glds9(vo, (const char*)U + (size_t)qb * 64 * ROWB + hg * 144, lds0 + A_UT);
      BAR();
      const lptr rp = lds + A_UT + (((4 * g + p4) & 7) * 8 + q4) * 144 + (g >> 1) * 8;
      char* up = ws + WS_UT + (size_t)(hg * 72 + (lane >> 5) * 4 + (lane & 3)) * UT_ROW + 128 + qb * 128 + ((lane >> 2) & 7) * 16;
      VMWAIT(0);
      STAMP(1);
#pragma unroll
      for (int i = 0; i < 9; ++i) st16c(up + i * 8 * UT_ROW, __builtin_bit_cast(u32x4, trd2(rp + i * 16, rp + i * 16 + 576)));
      if (qb == 0) {
        st16c(ws + WS_UT + (size_t)(hg * 72 + lane) * UT_ROW + 112, (u32x4){0u, 0u, 0u, 0u});
        st16c(ws + WS_UT + (size_t)(hg * 72 + 64 + (lane & 7)) * UT_ROW + 112, (u32x4){0u, 0u, 0u, 0u});
      }
      STAMP(2);
      VMWAIT(0);
      PIN(ep);
      const unsigned tg = TAG();
      if (lane == 0) st4c(myflag + 8, tg);
      STAMP(3);
      lds_wait(&ctrl[1], 4u);
      const char* fop = ws + WS_F + ((b & 7) * 32 + r32) * 128;
      unsigned fo = ~tg;
#pragma unroll 1
      for (int spin = 0; spin < SPIN_MAX; ++spin) {
        __builtin_amdgcn_s_sleep(POLL0);
        if (h == 0 && fo != tg) asm volatile("global_load_dword %0, %1, off sc1\n\ts_waitcnt vmcnt(0)" : "+v"(fo) : "v"(fop) : "memory");
        if (__all(h == 1 || fo == tg)) break;
      }
      STAMP(4);
#pragma unroll 1
      for (int i = 0; i < 5; ++i) kv_group(ws, lds0, 0, b & 7, i, lane * 16);
      VMWAIT(0);
      LSET(7, 1u);
      STAMP(5);
    } else {
      const int xoff = mt == 0 ? A_X0 : A_W + 9216 + mt * 18432;
      const lptr xb0 = lds + xoff + l15 * 128 + ((g ^ (l15 >> 1)) * 16), xb1 = lds + xoff + l15 * 128 + (((4 + g) ^ (l15 >> 1)) * 16);
      bf16x8 xa[18];
      if (wid >= 4) {
        __builtin_amdgcn_s_setprio(3);
        typedef unsigned u32x3 __attribute__((ext_vector_type(3)));
        u32x3 wq3[3];
        const u16* Wx = hg < 4 ? Wk : Wv;
#pragma unroll
        for (int r = 0; r < 3; ++r) {
          const u16* p0 = Wx + (mt * 144 + min(lane + 64 * r, 143)) * 18 + 4 * (hg & 3);
          asm volatile("global_load_dwordx3 %0, %1, off" : "=v"(wq3[r]) : "v"(p0) : "memory");
        }
        BAR();
        {
          const int row = lane >> 3;
          const unsigned v0 = row * ROWB + (((lane & 7) ^ (row >> 1)) * 16), v1 = (8 + row) * ROWB + (((lane & 7) ^ (4 + (row >> 1))) * 16);
          const char* xs = (const char*)X + (size_t)(qb * 64 + mt * 16) * ROWB;
#pragma unroll
          for (int kb = 0; kb < 9; ++kb) { gldss(v0, xs + kb * 128, lds0 + xoff + kb * 2048); gldss(v1, xs + kb * 128, lds0 + xoff + kb * 2048 + 1024); }
        }
        STAMP(1);
        VMWAIT(18);
#pragma unroll
        for (int r = 0; r < 3; ++r) {
          PIN(wq3[r]);
          const lptr p = lds + A_WKV + (mt * 144 + min(lane + 64 * r, 143)) * 16;
          *(LAS u32x2*)p = (u32x2){wq3[r][0], wq3[r][1]}; *(LAS unsigned*)(p + 8) = wq3[r][2];
        }
        CFENCE();
        LADD(0);
        lds_wait(&ctrl[0], 4u);
        STAMP(2);
        const lptr wb = lds + A_WKV + (g * 8 + q4) * 16 + (l15 & 1) * 8;
        f32x4 acc = {0.f, 0.f, 0.f, 0.f}, acc2 = acc;
#define XGRP(kb0, n)                                                                                                           \
        { bf16x8 wk[6];                                                                                                        \
          _Pragma("unroll") for (int i = 0; i < 6; ++i) wk[i] = trd2(wb + (2 * (kb0) + i) * 512, wb + (2 * (kb0) + i) * 512 + 64);  \
          VMWAIT(n);                                                                                                           \
          _Pragma("unroll") for (int i = 0; i < 6; ++i) xa[2 * (kb0) + i] = *(LAS const bf16x8*)(((i & 1) ? xb1 : xb0) + ((kb0) + (i >> 1)) * 2048); \
          if ((n) == 0) { CFENCE(); LSET(8 + mt, 1u); LADD(12 + mt); }                                                         \
          if (hg < 4) { _Pragma("unroll") for (int i = 0; i < 6; i += 2) { acc = MFMA16(wk[i], xa[2 * (kb0) + i], acc); acc2 = MFMA16(wk[i + 1], xa[2 * (kb0) + i + 1], acc2); } } \
          else { _Pragma("unroll") for (int i = 0; i < 6; i += 2) { acc = MFMA16(xa[2 * (kb0) + i], wk[i], acc); acc2 = MFMA16(xa[2 * (kb0) + i + 1], wk[i + 1], acc2); } } }
        XGRP(0, 12) XGRP(3, 6) XGRP(6, 0)
        STAMP(3);
        acc += acc2;
        char* cbp = ws + WS_KV + (qb >> 2) * CH_BYTES;
        const int key0 = (qb & 3) * 64 + mt * 16;
        u32x2 v = {pk2(acc[0], acc[1]), pk2(acc[2], acc[3])};
        unsigned mv;
        if (hg < 4) {
          const int key = key0 + l15;
          if (g == 0) st8c(cbp + (key >> 5) * 1024 + (hg >> 1) * 512 + (key & 31) * 16 + (hg & 1) * 8, v);
          if (g == 1 && hg == 3) st4c(cbp + CH_TAIL + (key & 63) * 16 + (key >> 6) * 4, v[0]);
          float n2 = bflo(v[0]) * bflo(v[0]) + bfhi(v[0]) * bfhi(v[0]);
          if (g == 0) n2 += bflo(v[1]) * bflo(v[1]) + bfhi(v[1]) * bfhi(v[1]);
          const unsigned m16 = max16(__float_as_uint(n2) & 0x7fffffffu);
          const float mq = __uint_as_float(__builtin_amdgcn_readlane(m16, 0)), mtl = __uint_as_float(__builtin_amdgcn_readlane(m16, 16));
          mv = __float_as_uint(hg == 3 ? mq + mtl : mq) & 0x7fffffffu;
        } else {
          const int key = key0 + 4 * g, dim = 4 * (hg - 4) + l15;
          unsigned am = umax(umax(v[0] << 16 & 0x7fffffffu, v[0] & 0x7fff0000u), umax(v[1] << 16 & 0x7fffffffu, v[1] & 0x7fff0000u));
          if (l15 >= 6) am = 0u;
          if (l15 == 6) v = (u32x2){0x3f803f80u, 0x3f803f80u};
          if (l15 == 7) v = (u32x2){0u, 0u};
          const int row = l15 < 4 ? ((dim & 3) | ((dim & 4) << 1) | ((dim & 8) >> 1)) : 12 + l15;
          if (l15 < 4 || (hg == 7 && l15 < 8)) st8c(cbp + CH_V + (key >> 3) * V_OCT + (key & 4) * 2 + row * 16, v);
          am = max16(am);
          mv = umax(umax(__builtin_amdgcn_readlane(am, 0), __builtin_amdgcn_readlane(am, 16)), umax(__builtin_amdgcn_readlane(am, 32), __builtin_amdgcn_readlane(am, 48)));
        }
        if (lane == 0) __hip_atomic_fetch_max(&ctrl[2], mv, __ATOMIC_RELAXED, __HIP_MEMORY_SCOPE_WORKGROUP);
        STAMP(4);
      } else {
        BAR();
        __builtin_amdgcn_s_setprio(2);
        lds_wait(&ctrl[8 + mt], 1u);
#pragma unroll
        for (int kk = 0; kk < 18; ++kk) xa[kk] = *(LAS const bf16x8*)(((kk & 1) ? xb1 : xb0) + (kk >> 1) * 2048);
        CFENCE();
        LADD(12 + mt);
        STAMP(3);
        STAMP(4);
      }
#pragma unroll
      for (int kk = 0; kk < 18; ++kk) {
        const u32x4 x = __builtin_bit_cast(u32x4, xa[kk]);
        xa[kk] = __builtin_bit_cast(bf16x8, (u32x4){__builtin_amdgcn_perm(x[1], x[0], 0x05040100u), __builtin_amdgcn_perm(x[3], x[2], 0x05040100u),
                                                    __builtin_amdgcn_perm(x[1], x[0], 0x07060302u), __builtin_amdgcn_perm(x[3], x[2], 0x07060302u)});
        PIN(xa[kk]);
      }
      if (wid >= 4) {
        VMWAIT(0);
        PIN(ep);
        if (lane == 0 && __hip_atomic_fetch_add(&ctrl[1], 1u, __ATOMIC_RELAXED, __HIP_MEMORY_SCOPE_WORKGROUP) == 3u)
          st8c(myflag, (u32x2){TAG(), __hip_atomic_load(&ctrl[2], __ATOMIC_RELAXED, __HIP_MEMORY_SCOPE_WORKGROUP)});
        __builtin_amdgcn_s_setprio(2);
        STAMP(5);
        if (wid == 4) qgemm<5>(lds, ctrl, xa, mt, lane); else qgemm<2>(lds, ctrl, xa, mt, lane);
      } else {
        STAMP(5);
        qgemm<3>(lds, ctrl, xa, mt, lane);
      }
    }
    STAMP(8);
    const int kp = wid >> 2, head = wid & 3;
    f32x16 o0, o1;
#pragma unroll
    for (int j = 0; j < 16; ++j) { o0[j] = 0.f; o1[j] = 0.f; }
    const unsigned ka0 = lds0 + KV_LDS + kp * 1024 + lane * 16, ta0 = lds0 + KV_LDS + CH_TAIL + kp * 512 + r32 * 16,
                   va0 = lds0 + KV_LDS + CH_V + kp * 4 * V_OCT + h * V_OCT + (r32 < 19 ? r32 : 19) * 16;
    u32x4 q0, q1; unsigned qtl;
    lds_wait(&ctrl[5], 7u);
    {
      LAS const unsigned* qp = (LAS const unsigned*)(lds + A_Q + r32 * Q_ROW + head * 36 + h * 16);
#pragma unroll
      for (int j = 0; j < 4; ++j) { q0[j] = qp[j]; q1[j] = qp[32 * Q_ROW / 4 + j]; }
      qtl = *(LAS const unsigned*)(lds + A_Q + lane * Q_ROW + head * 36 + 32);
    }
    CFENCE();
    LADD(6);
    const u32x2 qa = {qtl, 0u}, qc = {0u, qtl};
    STAMP(9);
    lds_wait(&ctrl[7], 1u);
    STAMP(10);
    {
      int it = 8; unsigned ka = ka0, va = va0, ta = ta0;
      asm volatile(
      "ds_read_b128 v[128:131], %[ka]\n"
      "ds_read_b128 v[140:143], %[ta]\n"
      "v_mov_b32 v120, 0\n"
      "v_mov_b32 v121, 0\n"
      "v_mov_b32 v122, 0\n"
      "v_mov_b32 v123, 0\n"
      "v_mov_b32 v124, 0\n"
      "v_mov_b32 v125, 0\n"
      "v_mov_b32 v126, 0\n"
      "v_mov_b32 v127, 0\n"
      "v_mov_b32 v136, 0\n"
      "v_mov_b32 v137, 0\n"
      "v_mov_b32 v138, 0\n"
      "v_mov_b32 v139, 0\n"
      "s_waitcnt lgkmcnt(1)\n"
      "v_mfma_f32_32x32x16_bf16 v[64:79], v[128:131], %[q0], 0\n"
      "v_mfma_f32_32x32x16_bf16 v[80:95], v[128:131], %[q1], 0\n"
      "ds_read_b128 v[128:131], %[ka] offset:2048\n"
      "ds_read_b128 v[132:135], %[va]\n"
      "s_nop 7\n"
      "s_waitcnt lgkmcnt(2)\n"
      "v_mfma_f32_32x32x4_2b_bf16 v[64:95], v[140:141], %[qa], v[64:95]\n"
      "s_nop 15\n"
      "s_nop 1\n"
      "1:\n"
      "s_setprio 1\n"
      "s_waitcnt lgkmcnt(1)\n"
      "v_mfma_f32_32x32x16_bf16 v[96:111], v[128:131], %[q0], 0\n"
      "v_exp_f32 v64, v64\n"
      "v_exp_f32 v65, v65\n"
      "v_exp_f32 v66, v66\n"
      "v_smfmac_f32_32x32x32_bf16 %[o1], v[136:139], v[120:127], %[ix]\n"
      "ds_read_b128 v[136:139], %[va] offset:640\n"
      "v_exp_f32 v67, v67\n"
      "v_exp_f32 v68, v68\n"
      "v_exp_f32 v69, v69\n"
      "v_mfma_f32_32x32x16_bf16 v[112:127], v[128:131], %[q1], 0\n"
      "ds_read_b128 v[128:131], %[ka] offset:4096\n"
      "v_exp_f32 v70, v70\n"
      "v_exp_f32 v71, v71\n"
      "v_exp_f32 v72, v72\n"
      "v_exp_f32 v73, v73\n"
      "s_waitcnt lgkmcnt(2)\n"
      "v_smfmac_f32_32x32x32_bf16 %[o0], v[132:135], v[64:71], %[ix]\n"
      "v_exp_f32 v74, v74\n"
      "v_exp_f32 v75, v75\n"
      "v_exp_f32 v76, v76\n"
      "v_exp_f32 v77, v77\n"
      "v_exp_f32 v78, v78\n"
      "v_mfma_f32_32x32x4_2b_bf16 v[96:127], v[140:141], %[qc], v[96:127]\n"
      "v_exp_f32 v79, v79\n"
      "v_exp_f32 v80, v80\n"
      "v_exp_f32 v81, v81\n"
      "v_exp_f32 v82, v82\n"
      "v_exp_f32 v83, v83\n"
      "v_exp_f32 v84, v84\n"
      "v_exp_f32 v85, v85\n"
      "v_exp_f32 v86, v86\n"
      "s_waitcnt lgkmcnt(1)\n"
      "v_smfmac_f32_32x32x32_bf16 %[o0], v[136:139], v[72:79], %[ix]\n"
      "v_exp_f32 v87, v87\n"
      "v_exp_f32 v88, v88\n"
      "v_exp_f32 v89, v89\n"
      "v_exp_f32 v90, v90\n"
      "v_smfmac_f32_32x32x32_bf16 %[o1], v[132:135], v[80:87], %[ix]\n"
      "ds_read_b128 v[132:135], %[va] offset:2560\n"
      "v_exp_f32 v91, v91\n"
      "v_exp_f32 v92, v92\n"
      "v_exp_f32 v93, v93\n"
      "v_exp_f32 v94, v94\n"
      "v_exp_f32 v95, v95\n"
      "s_setprio 0\n"
      "s_waitcnt lgkmcnt(1)\n"
      "v_mfma_f32_32x32x16_bf16 v[64:79], v[128:131], %[q0], 0\n"
      "v_exp_f32 v96, v96\n"
      "v_exp_f32 v97, v97\n"
      "v_exp_f32 v98, v98\n"
      "v_smfmac_f32_32x32x32_bf16 %[o1], v[136:139], v[88:95], %[ix]\n"
      "ds_read_b128 v[136:139], %[va] offset:3200\n"
      "v_exp_f32 v99, v99\n"
      "v_exp_f32 v100, v100\n"
      "v_exp_f32 v101, v101\n"
      "v_mfma_f32_32x32x16_bf16 v[80:95], v[128:131], %[q1], 0\n"
      "ds_read_b128 v[128:131], %[ka] offset:6144\n"
      "v_exp_f32 v102, v102\n"
      "v_exp_f32 v103, v103\n"
      "v_exp_f32 v104, v104\n"
      "v_exp_f32 v105, v105\n"
      "s_waitcnt lgkmcnt(2)\n"
      "v_smfmac_f32_32x32x32_bf16 %[o0], v[132:135], v[96:103], %[ix]\n"
      "v_exp_f32 v106, v106\n"
      "v_exp_f32 v107, v107\n"
      "v_exp_f32 v108, v108\n"
      "v_exp_f32 v109, v109\n"
      "v_exp_f32 v110, v110\n"
      "v_mfma_f32_32x32x4_2b_bf16 v[64:95], v[142:143], %[qa], v[64:95]\n"
      "v_exp_f32 v111, v111\n"
      "v_exp_f32 v112, v112\n"
      "v_exp_f32 v113, v113\n"
      "v_exp_f32 v114, v114\n"
      "v_exp_f32 v115, v115\n"
      "v_exp_f32 v116, v116\n"
      "v_exp_f32 v117, v117\n"
      "v_exp_f32 v118, v118\n"
      "s_waitcnt lgkmcnt(1)\n"
      "v_smfmac_f32_32x32x32_bf16 %[o0], v[136:139], v[104:111], %[ix]\n"
      "v_exp_f32 v119, v119\n"
      "v_exp_f32 v120, v120\n"
      "v_exp_f32 v121, v121\n"
      "v_exp_f32 v122, v122\n"
      "v_smfmac_f32_32x32x32_bf16 %[o1], v[132:135], v[112:119], %[ix]\n"
      "ds_read_b128 v[132:135], %[va] offset:5120\n"
      "v_exp_f32 v123, v123\n"
      "v_exp_f32 v124, v124\n"
      "v_exp_f32 v125, v125\n"
      "v_exp_f32 v126, v126\n"
      "v_exp_f32 v127, v127\n"
      "s_setprio 3\n"
      "s_waitcnt lgkmcnt(1)\n"
      "v_mfma_f32_32x32x16_bf16 v[96:111], v[128:131], %[q0], 0\n"
      "v_exp_f32 v64, v64\n"
      "v_exp_f32 v65, v65\n"
      "v_exp_f32 v66, v66\n"
      "v_smfmac_f32_32x32x32_bf16 %[o1], v[136:139], v[120:127], %[ix]\n"
      "ds_read_b128 v[136:139], %[va] offset:5760\n"
      "v_exp_f32 v67, v67\n"
      "v_exp_f32 v68, v68\n"
      "v_exp_f32 v69, v69\n"
      "v_mfma_f32_32x32x16_bf16 v[112:127], v[128:131], %[q1], 0\n"
      "s_setprio 2\n"
      "s_waitcnt lgkmcnt(1)\n"
      "v_mfma_f32_32x32x16_bf16 v[64:79], v[128:131], %[q0], 0\n"
      "v_exp_f32 v96, v96\n"
      "v_exp_f32 v97, v97\n"
      "v_exp_f32 v98, v98\n"
      "v_smfmac_f32_32x32x32_bf16 %[o1], v[136:139], v[88:95], %[ix]\n"
      "ds_read_b128 v[136:139], %[va] offset:8320\n"
      "v_exp_f32 v99, v99\n"
      "v_exp_f32 v100, v100\n"
      "v_exp_f32 v101, v101\n"
      "v_mfma_f32_32x32x16_bf16 v[80:95], v[128:131], %[q1], 0\n"
      "ds_read_b128 v[128:131], %[ka] offset:21504\n"
      "v_exp_f32 v102, v102\n"
      "v_exp_f32 v103, v103\n"
      "v_exp_f32 v104, v104\n"
      "v_exp_f32 v105, v105\n"
      "s_waitcnt lgkmcnt(2)\n"
      "v_smfmac_f32_32x32x32_bf16 %[o0], v[132:135], v[96:103], %[ix]\n"
      "v_exp_f32 v106, v106\n"
      "v_exp_f32 v107, v107\n"
      "v_exp_f32 v108, v108\n"
      "v_exp_f32 v109, v109\n"
      "v_exp_f32 v110, v110\n"
      "v_mfma_f32_32x32x4_2b_bf16 v[64:95], v[140:141], %[qa], v[64:95]\n"
      "v_exp_f32 v111, v111\n"
      "v_exp_f32 v112, v112\n"
      "v_exp_f32 v113, v113\n"
      "v_exp_f32 v114, v114\n"
      "v_exp_f32 v115, v115\n"
      "v_exp_f32 v116, v116\n"
      "v_exp_f32 v117, v117\n"
      "v_exp_f32 v118, v118\n"
      "s_waitcnt lgkmcnt(1)\n"
      "v_smfmac_f32_32x32x32_bf16 %[o0], v[136:139], v[104:111], %[ix]\n"
      "v_exp_f32 v119, v119\n"
      "v_exp_f32 v120, v120\n"
      "v_exp_f32 v121, v121\n"
      "v_exp_f32 v122, v122\n"
      "v_smfmac_f32_32x32x32_bf16 %[o1], v[132:135], v[112:119], %[ix]\n"
      "ds_read_b128 v[132:135], %[va] offset:19456\n"
      "v_exp_f32 v123, v123\n"
      "v_exp_f32 v124, v124\n"
      "v_exp_f32 v125, v125\n"
      "v_exp_f32 v126, v126\n"
      "v_exp_f32 v127, v127\n"
      "v_add_u32 %[ka], 19456, %[ka]\n"
      "v_add_u32 %[va], 19456, %[va]\n"
      "v_add_u32 %[ta], 19456, %[ta]\n"
      "s_sub_u32 %[it], %[it], 1\n"
      "s_cmp_lg_u32 %[it], 0\n"
      "s_cbranch_scc1 1b\n"
      "v_smfmac_f32_32x32x32_bf16 %[o1], v[136:139], v[120:127], %[ix]\n"
      "s_setprio 0\n"
      "s_waitcnt lgkmcnt(0)\n"
      "s_nop 15\n"
          : [o0] "+v"(o0), [o1] "+v"(o1), [ka] "+v"(ka), [va] "+v"(va), [ta] "+v"(ta), [it] "+s"(it)
          : [q0] "v"(q0), [q1] "v"(q1), [qa] "v"(qa), [qc] "v"(qc), [ix] "v"(0xdddddddd)
          : "v64", "v65", "v66", "v67", "v68", "v69", "v70", "v71", "v72", "v73", "v74", "v75", "v76", "v77", "v78", "v79", "v80", "v81", "v82", "v83", "v84", "v85", "v86", "v87", "v88", "v89", "v90", "v91", "v92", "v93", "v94", "v95", "v96", "v97", "v98", "v99", "v100", "v101", "v102", "v103", "v104", "v105", "v106", "v107", "v108", "v109", "v110", "v111", "v112", "v113", "v114", "v115", "v116", "v117", "v118", "v119", "v120", "v121", "v122", "v123", "v124", "v125", "v126", "v127", "v128", "v129", "v130", "v131", "v132", "v133", "v134", "v135", "v136", "v137", "v138", "v139", "v140", "v141", "v142", "v143", "scc", "memory");
    }
    STAMP(13);
    const lptr xo = lds + C_OX + lane * 16;
    auto put_half = [&]() {
#pragma unroll
      for (int i = 0; i < 3; ++i) {
        const f32x4 a = {o0[4 * i], o0[4 * i + 1], o0[4 * i + 2], o0[4 * i + 3]}, c = {o1[4 * i], o1[4 * i + 1], o1[4 * i + 2], o1[4 * i + 3]};
        *(LAS f32x4*)(xo + (wid * 3 + i) * 1024) = kp ? a : c;
      }
    };
    put_half();
    {
#define DOT2(x, c) __builtin_amdgcn_fdot2_f32_bf16(__builtin_bit_cast(bf16x2, x), __builtin_bit_cast(bf16x2, x), (c), false)
      const float nt = DOT2(qtl, 0.f);
      float n0 = h ? 0.f : nt, n1 = h ? nt : 0.f;
#pragma unroll
      for (int j = 0; j < 4; ++j) { n0 = DOT2(q0[j], n0); n1 = DOT2(q1[j], n1); }
      const auto sw = __builtin_amdgcn_permlane32_swap(__float_as_uint(n0), __float_as_uint(n1), false, false);
      const float n2 = __uint_as_float(sw[0]) + __uint_as_float(sw[1]);
      const bool bad = __any(!(n2 * __uint_as_float(bctl[8]) <= 8000.f));
      if (lane == 0) bctl[wid] = bad ? 1u : 0u;
    }
    BAR();
    if (any_bad()) {
      BAR(); BAR();
      const bf16x8 qf0 = __builtin_bit_cast(bf16x8, q0), qf1 = __builtin_bit_cast(bf16x8, q1);
      float m0 = -INFINITY, m1 = -INFINITY;
#pragma unroll 1
      for (int i = 0; i < 32; ++i) {
        const int off = (i >> 2) * CH_BYTES;
        const f32x32 s = qk_tile((lptr)(uintptr_t)(ka0 + off + (i & 3) * 2048), qf0, qf1, *(LAS const unsigned*)(uintptr_t)(ta0 + off + (i & 3) * 4), qa);
#pragma unroll
        for (int j = 0; j < 16; ++j) { m0 = fmaxf(m0, s[j]); m1 = fmaxf(m1, s[16 + j]); }
      }
      m0 = fmaxf(m0, __shfl_xor(m0, 32)); m1 = fmaxf(m1, __shfl_xor(m1, 32));
      *(LAS f32x2*)(lds + SPARE + tid * 8) = (f32x2){m0, m1};
      BAR();
      const f32x2 mo = *(LAS const f32x2*)(lds + SPARE + (tid ^ 256) * 8);
      m0 = fmaxf(m0, mo[0]); m1 = fmaxf(m1, mo[1]);
#pragma unroll
      for (int j = 0; j < 16; ++j) { o0[j] = 0.f; o1[j] = 0.f; }
#pragma unroll 1
      for (int i = 0; i < 32; ++i) {
        const int off = (i >> 2) * CH_BYTES;
        f32x32 s = qk_tile((lptr)(uintptr_t)(ka0 + off + (i & 3) * 2048), qf0, qf1, *(LAS const unsigned*)(uintptr_t)(ta0 + off + (i & 3) * 4), qa);
#pragma unroll
        for (int j = 0; j < 16; ++j) { s[j] = __builtin_amdgcn_exp2f(s[j] - m0); s[16 + j] = __builtin_amdgcn_exp2f(s[16 + j] - m1); }
        const bf16x8 v0 = *(LAS const bf16x8*)(uintptr_t)(va0 + off + (i & 3) * 8 * V_OCT), v1 = *(LAS const bf16x8*)(uintptr_t)(va0 + off + ((i & 3) * 8 + 2) * V_OCT);
        o0 = SMF(v0, sub8(s, 0), o0); o0 = SMF(v1, sub8(s, 8), o0); o1 = SMF(v0, sub8(s, 16), o1); o1 = SMF(v1, sub8(s, 24), o1);
      }
      BAR();
      put_half();
      BAR();
    }
    float f[12];
#pragma unroll
    for (int i = 0; i < 3; ++i) {
      const f32x4 p = *(LAS const f32x4*)(xo + ((wid ^ 4) * 3 + i) * 1024);
#pragma unroll
      for (int j = 0; j < 4; ++j) f[4 * i + j] = (kp ? o1[4 * i + j] : o0[4 * i + j]) + p[j];
    }
    const auto sw = __builtin_amdgcn_permlane32_swap(__float_as_uint(f[10]), __float_as_uint(f[10]), false, false);
    const float inv = __builtin_amdgcn_rcpf(__uint_as_float(sw[0]));
    char* ob = ws + WS_O + (size_t)(qb * 4 + kp * 2 + (r32 >> 4)) * 18432 + l15 * 16;
    const int O = 9 * hg + 2 * head + h, Ot = 9 * hg + 8;
    st16c(ob + O * 256, (u32x4){nosent(pk2(f[0] * inv, f[1] * inv)), nosent(pk2(f[2] * inv, f[3] * inv)),
                                nosent(pk2(f[4] * inv, f[5] * inv)), nosent(pk2(f[6] * inv, f[7] * inv))});
    if (h == 0) st4c(ob + Ot * 256 + head * 4, nosent(pk2(f[8] * inv, f[9] * inv)));
    STAMP(11);
    load_wf();
    VMWAIT(0);
  } else {
    const int sv = wid - 8, chl = sv >> 1, half = sv & 1, rot = b & 7;
    const unsigned l16 = lane * 16;
    unsigned dk0, dk1;
    {
      const u16 *p0 = Dskip + 2 * b + chl, *p1 = Dskip + ce;
      asm volatile("global_load_ushort %0, %2, off\n\tglobal_load_ushort %1, %3, off" : "=&v"(dk0), "=&v"(dk1) : "v"(p0), "v"(p1) : "memory");
    }
#define KVC(s0, s1) _Pragma("unroll 1") for (int i = 5 * (s0) + sv; i < 5 * (s1); i += 4) kv_group(ws, lds0, i / 5, rot, i % 5, l16)
    if (sv < 3) {
      __builtin_amdgcn_s_setprio(3);
      const int ch = sv < 2 ? 2 * b + sv : ce, ix = ch * NS + 2 * lane;
      const unsigned are = *(const unsigned*)(Are + ix), aim = *(const unsigned*)(Aim + ix), bre = *(const unsigned*)(Bre + ix),
                     bim = *(const unsigned*)(Bim + ix), cre = *(const unsigned*)(Cre + ix), cim = *(const unsigned*)(Cim + ix);
      const u16 ldt = logdt[ch];
      __builtin_amdgcn_s_setprio(1);
      BAR();
      VMWAIT(0);
      STAMP(1);
      kgen_wave(are, aim, bre, bim, cre, cim, bf2f(ldt), lds + CVB + sv * R_BYTES, &ctrl[5], lds + A_SCR + sv * 11264, lane);
      if (lane < 56) *(LAS u32x4*)(lds + CVB + CV_U + sv * CV_UCH + (lane < 28 ? lane * 16 : CV_U1 + (lane - 28) * 16)) = (u32x4){0u, 0u, 0u, 0u};
      STAMP(4);
      __builtin_amdgcn_s_setprio(3);
    } else {
      unsigned vo[9];
      slice_offsets(vo, lane);
      const char* wsrc = (const char*)Wq + hg * 144;
      BAR();
#pragma unroll 1
      for (int a = 0; a < 3; ++a) glds9(vo, wsrc + a * 64 * ROWB, lds0 + A_W + a * 9216);
      STAMP(1);
      VMWAIT(0); LSET(4, 1u);
      __builtin_amdgcn_s_setprio(3);
#pragma unroll 1
      for (int a = 3; a < 9; ++a) {
        if (a & 1) lds_wait(&ctrl[12 + (a >> 1)], 2u);
        glds9(vo, wsrc + a * 64 * ROWB, lds0 + A_W + a * 9216);
        if (a == 6) { VMWAIT(18); LSET(4, 2u); }
      }
      STAMP(3);
      VMWAIT(18); LSET(4, 3u);
      VMWAIT(9); LSET(4, 4u);
      VMWAIT(0); LSET(4, 5u);
      STAMP(4);
      PIN(ep);
      const unsigned tg = TAG();
      const char* fp = ws + WS_F + lane * 512;
      u32x4 fl[4];
      asm volatile("global_load_dwordx4 %0, %4, off sc1\n\tglobal_load_dwordx4 %1, %4, off offset:128 sc1\n\t"
                   "global_load_dwordx4 %2, %4, off offset:256 sc1\n\tglobal_load_dwordx4 %3, %4, off offset:384 sc1\n\ts_waitcnt vmcnt(0)"
                   : "=&v"(fl[0]), "=&v"(fl[1]), "=&v"(fl[2]), "=&v"(fl[3]) : "v"(fp) : "memory");
#pragma unroll 1
      for (int spin = 0; spin < SPIN_MAX; ++spin) {
        bool miss = false;
#pragma unroll
        for (int i = 0; i < 4; ++i) miss = miss || fl[i][0] != tg || fl[i][2] != tg;
        if (!__any(miss)) break;
        __builtin_amdgcn_s_sleep(POLL1);
#pragma unroll
        for (int i = 0; i < 4; ++i)
          if (fl[i][0] != tg || fl[i][2] != tg) { const char* p = fp + i * 128; asm volatile("global_load_dwordx4 %0, %1, off sc1" : "+v"(fl[i]) : "v"(p) : "memory"); }
        asm volatile("s_waitcnt vmcnt(0)" : "+v"(fl[0]), "+v"(fl[1]), "+v"(fl[2]), "+v"(fl[3]) :: "memory");
      }
      STAMP(9);
#pragma unroll 1
      for (int i = 0; i < 4; ++i) kv_group(ws, lds0, 1, rot, i, l16);
      lds_wait(&ctrl[5], 7u);
#pragma unroll 1
      for (int i = 0; i < 6; ++i)
        glds4c(l16, ws + WS_UT + (size_t)(i < 4 ? 2 * b + (i >> 1) : ce) * UT_ROW + 128 - ((i & 1) ? 0 : 2), lds0 + CVB + CV_U + (i >> 1) * CV_UCH + (i & 1) * CV_U1 + U_PADB);
      lds_wait(&ctrl[6], 8u);
      kv_group(ws, lds0, 1, rot, 4, l16);
      STAMP(10);
      {
        unsigned m[4] = {fl[0][1], fl[1][1], fl[2][1], fl[3][1]};
        float k2 = 0.f; unsigned vm = 0u;
#pragma unroll
        for (int i = 0; i < 4; ++i) {
          m[i] = umax(m[i], dppu<0x4E>(m[i])); m[i] = umax(m[i], dppu<0x124>(m[i])); m[i] = umax(m[i], dppu<0x128>(m[i]));
          k2 += __uint_as_float(umax(umax(__builtin_amdgcn_readlane(m[i], 0), __builtin_amdgcn_readlane(m[i], 16)), umax(__builtin_amdgcn_readlane(m[i], 32), __builtin_amdgcn_readlane(m[i], 48))));
          vm = umax(vm, umax(umax(__builtin_amdgcn_readlane(m[i], 1), __builtin_amdgcn_readlane(m[i], 17)), umax(__builtin_amdgcn_readlane(m[i], 33), __builtin_amdgcn_readlane(m[i], 49))));
        }
        const float vf = __uint_as_float(vm);
        if (lane == 0) bctl[8] = (vf <= 1048576.f && vf >= 9.5367431640625e-7f) ? __float_as_uint(k2) : 0x7fc00000u;
        if (lane == 0) bctl[9] = 0u;
      }
    }
    PIN(ep); PIN(dk0); PIN(dk1);
    const unsigned tag = TAG();
    const float dsk = bflo(dk0), dse = bflo(dk1);
    const int rofs = CVB + (1023 - l15 + 4 * g) * 4, uofs = CVB + CV_U + (l15 & 1) * CV_U1 + U_PADB + (32 * (l15 >> 1) + 8 * g) * 2;
    f32x4 acc[8], ea = {0.f, 0.f, 0.f, 0.f}, eb = ea;
#pragma unroll
    for (int c = 0; c < 8; ++c) acc[c] = ea;
#pragma unroll 1
    for (int iv = 0; iv < 4; ++iv) {
      STAMP(16);
      BARVM(0);
      STAMP(24);
      if (iv < 2) { KVC(2 + 2 * iv, 4 + 2 * iv); }
      if (iv == 2) {
        char* p = ws + WS_O + (qb * 4 + sv) * 18432 + 9 * hg * 256 + l16;
        st16c(p, (u32x4){SENT, SENT, SENT, SENT}); st16c(p + 1024, (u32x4){SENT, SENT, SENT, SENT});
        if (lane < 16) st16c(p + 2048, (u32x4){SENT, SENT, SENT, SENT});
      }
      {
        const int m0 = 4 * half + iv;
        const lptr rb = lds + chl * R_BYTES + rofs - 3584 - m0 * 64, ub = lds + chl * CV_UCH + uofs - m0 * 64;
        bf16x8 uf[8], kf[8];
#pragma unroll
        for (int z = 0; z < 8; ++z) {
          LAS const unsigned* rp = (LAS const unsigned*)(rb + 512 * (7 - z));
          kf[z] = __builtin_bit_cast(bf16x8, (u32x4){rp[0], rp[1], rp[2], rp[3]});
          uf[z] = *(LAS const bf16x8*)(ub + z * 512);
        }
        CFENCE();
#pragma unroll
        for (int c = 0; c < 8; ++c)
#pragma unroll
          for (int m1 = 0; m1 <= c; ++m1) acc[c] = MFMA16(uf[c - m1], kf[m1], acc[c]);
      }
      {
        const int m0 = 2 * sv + (iv >> 1), cB = 7 - cq;
        const lptr rb = lds + 2 * R_BYTES + rofs - m0 * 64, ub = lds + 2 * CV_UCH + uofs - m0 * 64;
        if ((iv & 1) == 0) {
#pragma unroll
          for (int i = 0; i < 5; ++i) {
            LAS const unsigned* rp = (LAS const unsigned*)(rb - 512 * i);
            eb = MFMA16(*(LAS const bf16x8*)(ub + 512 * (cB - i)), __builtin_bit_cast(bf16x8, (u32x4){rp[0], rp[1], rp[2], rp[3]}), eb);
          }
        } else {
          bf16x8 uf[4], kf[4];
#pragma unroll
          for (int i = 5; i < 9; ++i) {
            const bool isB = i <= cB;
            const int m1 = isB ? i : i - cB - 1, z = (isB ? cB : cq) - m1;
            LAS const unsigned* rp = (LAS const unsigned*)(rb - 512 * m1);
            kf[i - 5] = __builtin_bit_cast(bf16x8, (u32x4){rp[0], rp[1], rp[2], rp[3]});
            uf[i - 5] = *(LAS const bf16x8*)(ub + 512 * z);
          }
#pragma unroll
          for (int i = 5; i < 9; ++i) {
            if (i < 8 && i <= cB) eb = MFMA16(uf[i - 5], kf[i - 5], eb);
            else ea = MFMA16(uf[i - 5], kf[i - 5], ea);
          }
        }
      }
    }
    STAMP(6);
    unsigned us[8], ue[2];
    const int ec = chl ? 7 - cq : cq;
#pragma unroll
    for (int i = 0; i < 8; ++i) us[i] = *(LAS const unsigned*)(lds + CVB + CV_U + chl * CV_UCH + CV_U1 + U_PADB + (1024 * half + 256 * (i >> 1) + 64 * g + 32 * (i & 1) + 2 * l15) * 2);
#pragma unroll
    for (int i = 0; i < 2; ++i) ue[i] = *(LAS const unsigned*)(lds + CVB + CV_U + 2 * CV_UCH + CV_U1 + U_PADB + (256 * ec + 64 * g + 32 * i + 2 * l15) * 2);
    const lptr sc = lds + CV_SCR + l16;
    if (half) {
#pragma unroll
      for (int i = 0; i < 4; ++i) { *(LAS f32x4*)(sc + (sv * 4 + i) * 1024) = acc[i]; acc[i] = acc[4 + i]; }
    } else {
#pragma unroll
      for (int i = 0; i < 4; ++i) *(LAS f32x4*)(sc + (sv * 4 + i) * 1024) = acc[4 + i];
    }
    if (sv != 0) *(LAS f32x4*)(sc + 16384 + (sv - 1) * 1024) = ea;
    if (sv != 2) *(LAS f32x4*)(sc + 19456 + (sv > 2 ? 2 : sv) * 1024) = eb;
    STAMP(20);
    BAR();
    STAMP(27);
    KVC(6, 8);
#define FIN(x, y, uu, dd) pk2(rbf(x) + rbf(bflo(uu) * dd), rbf(y) + rbf(bfhi(uu) * dd))
#pragma unroll
    for (int pr = 0; pr < 2; ++pr) {
      const f32x4 f0 = acc[2 * pr] + *(LAS const f32x4*)(sc + ((sv ^ 1) * 4 + 2 * pr) * 1024), f1 = acc[2 * pr + 1] + *(LAS const f32x4*)(sc + ((sv ^ 1) * 4 + 2 * pr + 1) * 1024);
      unsigned a0 = FIN(f0[0], f0[1], us[4 * pr], dsk), a1 = FIN(f0[2], f0[3], us[4 * pr + 1], dsk), a2 = FIN(f1[0], f1[1], us[4 * pr + 2], dsk), a3 = FIN(f1[2], f1[3], us[4 * pr + 3], dsk);
      const bool o1b = lane & 1, o2b = lane & 2;
      unsigned r;
      r = dppu<0xB1>(o1b ? a0 : a1); if (o1b) a0 = r; else a1 = r;
      r = dppu<0xB1>(o1b ? a2 : a3); if (o1b) a2 = r; else a3 = r;
      r = dppu<0x4E>(o2b ? a0 : a2); if (o2b) a0 = r; else a2 = r;
      r = dppu<0x4E>(o2b ? a1 : a3); if (o2b) a1 = r; else a3 = r;
      st16c(ws + WS_YT + (2 * b + chl) * 4096 + (4 * half + 2 * pr + (p4 >> 1)) * 512 + g * 128 + (p4 & 1) * 64 + q4 * 16, (u32x4){a0, a1, a2, a3});
    }
    if (half == 0) {
      f32x4 e = chl ? eb : ea;
#pragma unroll
      for (int s = 0; s < 3; ++s) e += *(LAS const f32x4*)(sc + 16384 + chl * 3072 + s * 1024);
      char* p = ws + WS_YT + ce * 4096 + ec * 512 + g * 128 + l15 * 4;
      st4c(p, FIN(e[0], e[1], ue[0], dse)); st4c(p + 64, FIN(e[2], e[3], ue[1], dse));
    }
    STAMP(21);
    BARVM(0);
    STAMP(28);
    if (wid == 8 && lane == 0) st4c(myflag + 12, tag);
    const int rr = lane >> 3;
    auto load_wo = [&]() {
      const unsigned wsw = ((lane & 7) ^ (2 * ((rr >> 1) & 1))) * 16;
      const unsigned wv0 = rr * ROWB + wsw, wv1 = (18 * (rr >> 1) + 16 + (rr & 1)) * ROWB + (wsw ^ ((sv & 1) * 64));
      const unsigned wva = wv0 ^ ((sv & 1) * 64), wvb = wva ^ 64;
      const char* wo = (const char*)Wo + hg * 144;
#pragma unroll 1
      for (int gq = sv; gq < 8; gq += 4) {
#pragma unroll
        for (int o = 0; o < 9; ++o)
          gldss(o == 8 ? wv1 : ((o & 1) ? wvb : wva), wo + (gq * 72 + (o < 8 ? 18 * (o >> 1) + 8 * (o & 1) : 0)) * ROWB, lds0 + (gq * 9 + o) * 1024);
      }
#pragma unroll 1
      for (int a = sv; a < 9; a += 4) {
        const int O = 8 * a + rr, gq = O / 9, o = O - gq * 9, e8 = lane & 7;
        gldss((gq * 72 + (o < 8 ? 18 * (o >> 1) + 8 * (o & 1) + e8 : 18 * (e8 >> 1) + 16 + (e8 & 1))) * ROWB + 128, wo, lds0 + C_REM + a * 1024);
      }
    };
    load_wo();
    STAMP(22);
    BAR();
    STAMP(29);
    {
      const char* f2r = ws + WS_F + (sv * 64 + lane) * 128 + 12;
      unsigned f2 = ~tag;
#pragma unroll 1
      for (int spin = 0; spin < SPIN_MAX; ++spin) {
        if (f2 != tag) asm volatile("global_load_dword %0, %1, off sc1\n\ts_waitcnt vmcnt(0)" : "+v"(f2) : "v"(f2r) : "memory");
        if (__all(f2 == tag)) break;
        __builtin_amdgcn_s_sleep(POLL1);
      }
      if (lane == 0) __hip_atomic_fetch_add(&bctl[9], 1u, __ATOMIC_RELAXED, __HIP_MEMORY_SCOPE_WORKGROUP);
      lds_wait(&bctl[9], 4u);
      if (b == 0 && wid == 8 && lane == 0) st4c(ws + WS_E, tag);
    }
    {
      const char* yp = ws + WS_YT + hg * 72 * 4096 + qb * 128 + sv * 32;
      const unsigned yv = (lane >> 1) * 4096 + (lane & 1) * 16;
      gldssc(yv, yp, lds0 + C_YT + sv * 2304); gldssc(yv, yp + 32 * 4096, lds0 + C_YT + sv * 2304 + 1024);
      if (lane < 16) gldssc(yv, yp + 64 * 4096, lds0 + C_YT + sv * 2304 + 2048);
    }
    STAMP(23);
    BARVM(0);
    STAMP(12);
#pragma unroll
    for (int n = 0; n < 3; ++n) {
      const int rit = min(16 * n + 4 * g + p4, 35), wit = 16 * n + (lane >> 2);
      const lptr rp = lds + C_YT + sv * 2304 + ((rit % 9) * 8 + q4) * 32 + (rit / 9) * 8;
      const u32x4 v = __builtin_bit_cast(u32x4, trd2(rp, rp + 128));
      if (wit < 36) *(u32x4*)(out + (size_t)(qb * 64 + sv * 16 + (wit / 9) * 4 + (lane & 3)) * OUTLD + hg * 72 + (wit % 9) * 8) = v;
    }
    __builtin_amdgcn_s_setprio(0);
    STAMP(13);
    BAR();
    if (any_bad()) { BAR(); KVC(0, 8); BARVM(0); BAR(); BAR(); load_wo(); BARVM(0); }
    load_wf();
    __builtin_amdgcn_s_sleep(OSLEEP);
  }
  {
    const lptr wr = lds + C_REM + kt * 3072 + (g * 8 + q4) * 16 + (p4 & 1) * 8;
    STAMP(14);
    const char* op = ws + WS_O + (size_t)((qb * 4 + mt) * 18 + kt * 6) * 1024 + lane * 16;
    u32x4 of[6];
    const char* op2 = op + 4096;
    asm volatile("global_load_dwordx4 %0, %6, off sc1\n\tglobal_load_dwordx4 %1, %6, off offset:1024 sc1\n\t"
                 "global_load_dwordx4 %2, %6, off offset:2048 sc1\n\tglobal_load_dwordx4 %3, %6, off offset:3072 sc1\n\t"
                 "global_load_dwordx4 %4, %7, off sc1\n\tglobal_load_dwordx4 %5, %7, off offset:1024 sc1\n\ts_waitcnt vmcnt(0)"
                 : "=&v"(of[0]), "=&v"(of[1]), "=&v"(of[2]), "=&v"(of[3]), "=&v"(of[4]), "=&v"(of[5]) : "v"(op), "v"(op2) : "memory");
#pragma unroll 1
    for (int spin = 0; spin < SPIN_MAX; ++spin) {
      bool miss = false;
#pragma unroll
      for (int kk = 0; kk < 6; ++kk) {
        if (umax(umax(of[kk][0], of[kk][1]), umax(of[kk][2], of[kk][3])) == SENT) {
          const char* p = op + kk * 1024;
          asm volatile("global_load_dwordx4 %0, %1, off sc1" : "+v"(of[kk]) : "v"(p) : "memory");
          miss = true;
        }
      }
      if (!__any(miss)) break;
      __builtin_amdgcn_s_sleep(ORETRY);
      asm volatile("s_waitcnt vmcnt(0)" : "+v"(of[0]), "+v"(of[1]), "+v"(of[2]), "+v"(of[3]), "+v"(of[4]), "+v"(of[5]) :: "memory");
    }
    STAMP(15);
    f32x4 acc[5];
#pragma unroll
    for (int nt = 0; nt < 5; ++nt) acc[nt] = (f32x4){0.f, 0.f, 0.f, 0.f};
#pragma unroll
    for (int kk = 0; kk < 6; ++kk) {
#pragma unroll
      for (int nt = 0; nt < 4; ++nt) acc[nt] = MFMA16(wf[kk][nt], __builtin_bit_cast(bf16x8, of[kk]), acc[nt]);
      acc[4] = MFMA16(trd2(wr + kk * 512, wr + kk * 512 + 64), __builtin_bit_cast(bf16x8, of[kk]), acc[4]);
    }
    const lptr rd = lds + C_RED2 + mt * 5120 + lane * 16, rd2 = lds + C_RED + mt * 5120 + lane * 16;
#pragma unroll
    for (int nt = 0; nt < 5; ++nt) {
      const int own = nt >> 1;
      if (kt != own) *(LAS f32x4*)(((kt + 1) % 3 == own ? rd : rd2) + nt * 1024) = acc[nt];
    }
    BAR();
    u16* cp = out + (size_t)(qb * 64 + mt * 16 + l15) * OUTLD + DM + hg * 72 + g * 4 + kt * 32;
    const f32x4 a0 = kt == 0 ? acc[0] : (kt == 1 ? acc[2] : acc[4]), a1 = kt == 0 ? acc[1] : acc[3];
    const f32x4 v = a0 + *(LAS const f32x4*)(rd + kt * 2048) + *(LAS const f32x4*)(rd2 + kt * 2048);
    if (kt < 2 || g < 2) *(u32x2*)cp = (u32x2){pk2(v[0], v[1]), pk2(v[2], v[3])};
    if (kt < 2) {
      const f32x4 w = a1 + *(LAS const f32x4*)(rd + kt * 2048 + 1024) + *(LAS const f32x4*)(rd2 + kt * 2048 + 1024);
      *(u32x2*)(cp + 16) = (u32x2){pk2(w[0], w[1]), pk2(w[2], w[3])};
    }
  }
}

extern "C" void kernel_launch(void* const* d_in, const int* in_sizes, int n_in, void* d_out, int out_size, void* d_ws, size_t ws_size,
                              hipStream_t stream) {
  fused_kernel<<<256, 768, 0, stream>>>((const u16*)d_in[0], (const u16*)d_in[1], (const u16*)d_in[2], (const u16*)d_in[3], (const u16*)d_in[4],
                                        (const u16*)d_in[5], (const u16*)d_in[6], (const u16*)d_in[7], (const u16*)d_in[8], (const u16*)d_in[9],
                                        (const u16*)d_in[10], (const u16*)d_in[11], (const u16*)d_in[12], (const u16*)d_in[13], (u16*)d_out,
                                        (char*)d_ws);
}
```

```cpp
#include <hip/hip_runtime.h>
#include <hip/hip_bf16.h>
#include <stdint.h>

typedef __bf16 bf16x8 __attribute__((ext_vector_type(8)));
typedef __bf16 bf16x16 __attribute__((ext_vector_type(16)));
typedef __bf16 bf16x2 __attribute__((ext_vector_type(2)));
typedef short s16x4 __attribute__((ext_vector_type(4)));
typedef short s16x8 __attribute__((ext_vector_type(8)));
typedef float f32x2 __attribute__((ext_vector_type(2)));
typedef float f32x4 __attribute__((ext_vector_type(4)));
typedef float f32x8 __attribute__((ext_vector_type(8)));
typedef float f32x16 __attribute__((ext_vector_type(16)));
typedef float f32x32 __attribute__((ext_vector_type(32)));
typedef unsigned u32x2 __attribute__((ext_vector_type(2)));
typedef unsigned u32x4 __attribute__((ext_vector_type(4)));
typedef unsigned short u16;
#define LAS __attribute__((address_space(3)))
#define DI __device__ __forceinline__
typedef LAS char* lptr;

constexpr int SEQ = 2048, DM = 576, NS = 128, OUTLD = 2 * DM, ROWB = DM * 2;
constexpr int CH_BYTES = 19456, KV_BYTES = 8 * CH_BYTES, CH_TAIL = 8192, CH_V = 9216, V_OCT = 320;
constexpr int R_BYTES = 4160, CV_U = 13312, CV_UCH = 4640 + 4544, CV_U1 = 4640, U_PADB = 448, UT_ROW = 4224;
constexpr size_t WS_E = 0, WS_F = 4096, WS_KV = WS_F + 256 * 128;
constexpr size_t WS_UT = WS_KV + KV_BYTES;
constexpr size_t WS_YT = WS_UT + (size_t)DM * UT_ROW;
constexpr size_t WS_O = WS_YT + (size_t)DM * SEQ * 2;
constexpr size_t WS_DBG = 64u << 20;
constexpr int LDS_BYTES = 163840, KV_LDS = 0, SPARE = KV_BYTES, CVB = 6 * CH_BYTES;
constexpr int A_UT = 0, A_WKV = 9216, A_X0 = 18432, A_Q = 36864, Q_ROW = 144, A_CTRL = A_Q + 64 * Q_ROW, A_MISC = A_CTRL + 128, A_SCR = A_MISC + 144, A_W = 80896, B_CTRL = LDS_BYTES - 64;
constexpr int CV_SCR = 0, C_REM = 73728, C_YT = 82944, C_RED = C_YT + 9216, C_OX = C_RED + 20480, C_RED2 = C_OX + 24576;
static_assert(A_SCR + 6 * 5712 <= A_W - 256 && A_SCR % 16 == 0 && A_W + 82944 == LDS_BYTES && CVB + CV_U + 3 * CV_UCH <= B_CTRL - 256 && C_OX + 24576 <= 7 * CH_BYTES + 1024 &&
              C_RED2 + 20480 <= B_CTRL, "LDS map");
constexpr float QSCALE = 0.23570226039551584f * 1.4426950408889634f;
constexpr unsigned SENT = 0xffffffffu;
constexpr int SPIN_MAX = 1 << 16;
#define POLL1 12
#define POLL0 8
#define XLEAD 4u
#define OSLEEP 20
#define ORETRY 8

DI float bf2f(u16 x) { return __uint_as_float((unsigned)x << 16); }
DI float bflo(unsigned x) { return __uint_as_float(x << 16); }
DI float bfhi(unsigned x) { return __uint_as_float(x & 0xffff0000u); }
DI unsigned pk2(float lo, float hi) { f32x2 v = {lo, hi}; return __builtin_bit_cast(unsigned, __builtin_convertvector(v, bf16x2)); }
DI float rbf(float x) { return bfhi(pk2(x, x)); }
DI unsigned nosent(unsigned x) { return x < 0xfffffffeu ? x : 0xfffffffeu; }
#define GLDS(SC) "s_mov_b32 %0, m0\n\ts_mov_b32 m0, %2\n\ts_nop 0\n\tglobal_load_lds_dwordx4 %1, off" SC "\n\ts_mov_b32 m0, %0"
DI void glds16(const void* g, unsigned l) { unsigned sv; asm volatile(GLDS("") : "=&s"(sv) : "v"(g), "s"(l) : "memory"); }
DI void glds16c(const void* g, unsigned l) { unsigned sv; asm volatile(GLDS(" sc1") : "=&s"(sv) : "v"(g), "s"(l) : "memory"); }
#define GLDSS(SC) "s_mov_b32 %0, m0\n\ts_mov_b32 m0, %3\n\ts_nop 0\n\tglobal_load_lds_dwordx4 %1, %2" SC "\n\ts_mov_b32 m0, %0"
DI void gldss(unsigned voff, const void* sbase, unsigned l) { unsigned sv; asm volatile(GLDSS("") : "=&s"(sv) : "v"(voff), "s"(sbase), "s"(l) : "memory"); }
DI void gldssc(unsigned voff, const void* sbase, unsigned l) { unsigned sv; asm volatile(GLDSS(" sc1") : "=&s"(sv) : "v"(voff), "s"(sbase), "s"(l) : "memory"); }
DI void glds9(const unsigned (&vo)[9], const void* sbase, unsigned l) {
  unsigned sv;
  asm volatile("s_mov_b32 %0, m0\n\ts_mov_b32 m0, %11\n\ts_nop 0\n\t"
               "global_load_lds_dwordx4 %1, %10\n\tglobal_load_lds_dwordx4 %2, %10 offset:1024\n\t"
               "global_load_lds_dwordx4 %3, %10 offset:2048\n\tglobal_load_lds_dwordx4 %4, %10 offset:3072\n\t"
               "s_add_u32 m0, m0, 0x1000\n\ts_nop 0\n\t"
               "global_load_lds_dwordx4 %5, %10\n\tglobal_load_lds_dwordx4 %6, %10 offset:1024\n\t"
               "global_load_lds_dwordx4 %7, %10 offset:2048\n\tglobal_load_lds_dwordx4 %8, %10 offset:3072\n\t"
               "s_add_u32 m0, m0, 0x1000\n\ts_nop 0\n\tglobal_load_lds_dwordx4 %9, %10\n\ts_mov_b32 m0, %0"
               : "=&s"(sv) : "v"(vo[0]), "v"(vo[1]), "v"(vo[2]), "v"(vo[3]), "v"(vo[4]), "v"(vo[5]), "v"(vo[6]), "v"(vo[7]), "v"(vo[8]),
                 "s"(sbase), "s"(l) : "memory", "scc");
}
DI void glds4c(unsigned voff, const void* sbase, unsigned l) {
  unsigned sv;
  asm volatile("s_mov_b32 %0, m0\n\ts_mov_b32 m0, %3\n\ts_nop 0\n\t"
               "global_load_lds_dwordx4 %1, %2 sc1\n\tglobal_load_lds_dwordx4 %1, %2 offset:1024 sc1\n\t"
               "global_load_lds_dwordx4 %1, %2 offset:2048 sc1\n\tglobal_load_lds_dwordx4 %1, %2 offset:3072 sc1\n\ts_mov_b32 m0, %0"
               : "=&s"(sv) : "v"(voff), "s"(sbase), "s"(l) : "memory");
}
DI void touch(const void* g, unsigned junk) {
  unsigned sv;
  asm volatile("s_mov_b32 %0, m0\n\ts_mov_b32 m0, %2\n\ts_nop 0\n\tglobal_load_lds_dword %1, off sc1\n\ts_mov_b32 m0, %0" : "=&s"(sv) : "v"(g), "s"(junk) : "memory");
}
template <int W> DI void flag_wait(const char* p, unsigned tag, u32x4& v) {
#pragma unroll 1
  for (int spin = 0; spin < SPIN_MAX; ++spin) {
    if (__all(v[W] == tag)) break;
    __builtin_amdgcn_s_sleep(POLL1);
    if (v[W] != tag) asm volatile("global_load_dwordx4 %0, %1, off sc1\n\ts_waitcnt vmcnt(0)" : "+v"(v) : "v"(p) : "memory");
  }
}
DI void touchs(unsigned voff, const void* sbase, unsigned junk) {
  unsigned sv;
  asm volatile("s_mov_b32 %0, m0\n\ts_mov_b32 m0, %3\n\ts_nop 0\n\tglobal_load_lds_dword %1, %2 sc1\n\ts_mov_b32 m0, %0" : "=&s"(sv) : "v"(voff), "s"(sbase), "s"(junk) : "memory");
}
DI void slice_offsets(unsigned (&vo)[9], int lane) {
#pragma unroll
  for (int r = 0; r < 9; ++r) {
    const int c = r * 64 + lane, row = c / 9, src = (row & ~7) + 4 * (row & 1) + ((row >> 1) & 3);
    vo[r] = src * ROWB + (c - row * 9) * 16 + 4096 - (r & 3) * 1024;
  }
}
DI void kv_group(const char* ws, unsigned lds0, int slot, int rot, int grp, unsigned l16) {
  const int off = grp < 4 ? grp * 4096 : 15360;
  glds4c(l16, ws + WS_KV + ((slot + rot) & 7) * CH_BYTES + off, lds0 + KV_LDS + slot * CH_BYTES + off);
}
DI void lds_wait(LAS unsigned* p, unsigned n) {
  while (__hip_atomic_load(p, __ATOMIC_RELAXED, __HIP_MEMORY_SCOPE_WORKGROUP) < n) __builtin_amdgcn_s_sleep(1);
  asm volatile("" ::: "memory");
}
DI void st16c(void* p, u32x4 v) { asm volatile("global_store_dwordx4 %0, %1, off sc1\n\ts_nop 1" ::"v"(p), "v"(v) : "memory"); }
DI void st8c(void* p, u32x2 v) { asm volatile("global_store_dwordx2 %0, %1, off sc1" ::"v"(p), "v"(v) : "memory"); }
DI void st4c(void* p, unsigned v) { asm volatile("global_store_dword %0, %1, off sc1" ::"v"(p), "v"(v) : "memory"); }
#define LD4(d, p, SC) asm volatile("global_load_dword %0, %1, off" SC : "=v"(d) : "v"(p) : "memory")
#define LD8(d, p, SC) asm volatile("global_load_dwordx2 %0, %1, off" SC : "=v"(d) : "v"(p) : "memory")
#define LD16(d, p, SC) asm volatile("global_load_dwordx4 %0, %1, off" SC : "=v"(d) : "v"(p) : "memory")
#define PIN(x) asm volatile("" : "+v"(x))
#define VMWAIT(n) do { asm volatile("s_waitcnt vmcnt(" #n ")" ::: "memory"); __builtin_amdgcn_sched_barrier(0); } while (0)
#define BAR() asm volatile("s_waitcnt lgkmcnt(0)\n\ts_barrier" ::: "memory")
#define BARVM(n) asm volatile("s_waitcnt vmcnt(" #n ") lgkmcnt(0)\n\ts_barrier" ::: "memory")
#define CFENCE() asm volatile("" ::: "memory")
DI s16x4 trd(lptr p) { return __builtin_amdgcn_ds_read_tr16_b64_v4i16((LAS s16x4*)p); }
DI bf16x8 trd2(lptr a, lptr b) { return __builtin_bit_cast(bf16x8, __builtin_shufflevector(trd(a), trd(b), 0, 1, 2, 3, 4, 5, 6, 7)); }
#define MFMA16(a, b, c) __builtin_amdgcn_mfma_f32_16x16x32_bf16((a), (b), (c), 0, 0, 0)
#define MFMA32(a, b, c) __builtin_amdgcn_mfma_f32_32x32x16_bf16((a), (b), (c), 0, 0, 0)
#define SMF(a, b, c) __builtin_amdgcn_smfmac_f32_32x32x32_bf16((a), (b), (c), (int)0xdddddddd, 0, 0)
template <int CTRL> DI unsigned dppu(unsigned x) { return (unsigned)__builtin_amdgcn_update_dpp((int)x, (int)x, CTRL, 0xf, 0xf, false); }
DI unsigned umax(unsigned a, unsigned b) { return a > b ? a : b; }
DI unsigned max8(unsigned x) { x = umax(x, dppu<0xB1>(x)); x = umax(x, dppu<0x4E>(x)); return umax(x, dppu<0x141>(x)); }
DI unsigned max16(unsigned x) { x = max8(x); return umax(x, dppu<0x140>(x)); }

#define STAMP(i)

DI f32x2 cmul(f32x2 a, f32x2 b) {
  const f32x2 t = (f32x2){a[0], a[0]} * b;
  return __builtin_elementwise_fma((f32x2){a[1], a[1]}, (f32x2){-b[1], b[0]}, t);
}
DI f32x2 cpow(float zr, float zi, float c) {
  const float e = __builtin_amdgcn_exp2f(zr * c), th = __builtin_amdgcn_fractf(zi * c);
  return e * (f32x2){__builtin_amdgcn_cosf(th), __builtin_amdgcn_sinf(th)};
}
constexpr int KG_ROW = 272, KG_TAB = 21 * KG_ROW;
struct kg_in { unsigned are, aim, bre, bim, cre, cim, ldt; };
DI void kgen_half(const kg_in& in, lptr tb, int lane, f32x4 (&d)[2][2]) {
  const float dt = rbf(expf(bflo(in.ldt)));
  const float zr = (dt * bflo(in.are)) * 1.4426950408889634f, zi = (dt * bflo(in.aim)) * 0.15915494309189535f;
  const f32x2 dB = {dt * bflo(in.bre), dt * bflo(in.bim)}, Cc = {bflo(in.cre), bflo(in.cim)};
  lptr tw = tb + lane * 4;
#define PUT(row, v) do { const f32x2 v_ = (v); *(LAS unsigned*)(tw + (row) * KG_ROW) = pk2(v_[0], v_[1]); } while (0)
  const f32x2 w1 = cpow(zr, -zi, 1.f), w8 = cpow(zr, zi, 8.f), w256 = cpow(zr, -zi, 256.f);
  const f32x2 w2 = cmul(w1, w1), w3 = cmul(w2, w1), w4 = cmul(w2, w2), w6 = cmul(w2, w4);
  *(LAS unsigned*)tw = 0x3f80u;
  PUT(1, w1); PUT(2, w2); PUT(3, w3); PUT(4, w4); PUT(5, cmul(w1, w4)); PUT(6, w6); PUT(7, cmul(w3, w4));
  PUT(8, w256); PUT(9, cmul(w2, w256)); PUT(10, cmul(w4, w256)); PUT(11, cmul(w6, w256));
  const f32x2 w512 = cmul(w256, w256), w768 = cmul(w512, w256), w1024 = cmul(w768, w256);
  PUT(12, w512); PUT(13, cmul(w512, w4)); PUT(14, w768); PUT(15, cmul(w768, w4));
  PUT(16, w1024); PUT(17, cmul(w256, w1024)); PUT(18, cmul(w512, w1024)); PUT(19, cmul(w768, w1024)); PUT(20, cmul(w1024, w1024));
  CFENCE();
  const int l15 = lane & 15, g = lane >> 4;
  lptr fb = tb + l15 * KG_ROW + g * 16, fb1 = tb + (16 + min(l15, 4)) * KG_ROW + g * 16;
  bf16x8 a0[4], a1[4];
#pragma unroll
  for (int kk = 0; kk < 4; ++kk) { a0[kk] = *(LAS const bf16x8*)(fb + kk * 64); a1[kk] = *(LAS const bf16x8*)(fb1 + kk * 64); }
  CFENCE();
  const f32x2 w16 = cmul(w8, w8), w32 = cmul(w16, w16), w64 = cmul(w32, w32);
  f32x2 bb[8];
  bb[0] = cmul(Cc, dB); bb[1] = cmul(bb[0], w8); bb[2] = cmul(bb[0], w16); bb[3] = cmul(bb[1], w16);
#pragma unroll
  for (int i = 0; i < 4; ++i) bb[4 + i] = cmul(bb[i], w32);
#pragma unroll
  for (int ct = 0; ct < 2; ++ct) {
#pragma unroll
    for (int i = 0; i < 2; ++i) {
#pragma unroll
      for (int j = 0; j < 8; ++j) PUT(8 * i + j, bb[j]);
      if (ct == 0 || i == 0) {
#pragma unroll
        for (int j = 0; j < 8; ++j) bb[j] = cmul(bb[j], w64);
      }
    }
    CFENCE();
    bf16x8 bf[4];
#pragma unroll
    for (int kk = 0; kk < 4; ++kk) bf[kk] = *(LAS const bf16x8*)(fb + kk * 64);
    CFENCE();
    d[ct][0] = (f32x4){0.f, 0.f, 0.f, 0.f}; d[ct][1] = d[ct][0];
#pragma unroll
    for (int kk = 0; kk < 4; ++kk) { d[ct][0] = MFMA16(a0[kk], bf[kk], d[ct][0]); d[ct][1] = MFMA16(a1[kk], bf[kk], d[ct][1]); }
  }
}
DI void kgen_finish(const f32x4 (&d)[2][2], lptr img, LAS unsigned* gate, lptr tb, int lane) {
  const int l15 = lane & 15, g = lane >> 4;
  {
    const int e0 = g < 2 ? 8 * l15 + 4 * g : (g == 2 ? 256 + 4 * l15 : 384 + 2 * l15), es = g < 2 ? 128 : (g == 2 ? 64 : 32), e1 = g == 3 ? 64 : 2;
    const int f0 = g == 0 ? 512 + l15 : (lane == 16 ? 640 : 700);
#pragma unroll
    for (int ct = 0; ct < 2; ++ct) {
      const lptr p = tb + (e0 + ct * es) * 2;
      *(LAS unsigned*)p = pk2(d[ct][0][0], d[ct][0][1]); *(LAS unsigned*)(p + e1 * 2) = pk2(d[ct][0][2], d[ct][0][3]);
      LAS u16* q = (LAS u16*)tb + f0 + ct * 16;
      const unsigned x0 = pk2(d[ct][1][0], d[ct][1][1]), x1 = pk2(d[ct][1][2], d[ct][1][3]);
      q[0] = (u16)x0; q[32] = (u16)(x0 >> 16); q[64] = (u16)x1; q[96] = (u16)(x1 >> 16);
    }
  }
  lds_wait(gate, 8u);
  LAS const u16* C = (LAS const u16*)tb;
  const int rl = 63 - lane;
#pragma unroll
  for (int r = 0; r < 2; ++r) {
    const unsigned a = C[512 + 64 * (1 - r) + rl], bb = C[513 + 64 * (1 - r) + rl];
    const unsigned aa = a | (a << 16), b2x = bb | (bb << 16);
    *(LAS u32x4*)(img + r * 1024 + lane * 16) = (u32x4){b2x, (rl & 1) ? b2x : (bb | (a << 16)), aa, aa};
  }
  {
    const unsigned ab = *(LAS const unsigned*)(C + 384 + 2 * rl), c = C[386 + 2 * rl];
    *(LAS u32x4*)(img + 2048 + lane * 16) = (u32x4){c | (c << 16), __builtin_amdgcn_perm(ab, ab, 0x03020302u), __builtin_amdgcn_perm(ab, ab, 0x01000302u),
                                                __builtin_amdgcn_perm(ab, ab, 0x01000100u)};
  }
  {
    const bool hi = lane < 32;
    const int e = hi ? 128 + 4 * rl : 8 * rl;
    const u32x2 w01 = *(LAS const u32x2*)(C + e), w23 = *(LAS const u32x2*)(C + e + 4);
    const unsigned o0 = hi ? __builtin_amdgcn_perm(w01[1], w23[0], 0x07060100u) : __builtin_amdgcn_perm(w23[1], w23[1], 0x01000302u);
    const unsigned o1 = __builtin_amdgcn_perm(w23[0], w01[1], hi ? 0x01000100u : 0x05040706u);
    const unsigned o2 = __builtin_amdgcn_perm(w01[1], w01[0], hi ? 0x03020504u : 0x05040706u);
    const unsigned o3 = __builtin_amdgcn_perm(w01[0], w01[0], hi ? 0x01000100u : 0x01000302u);
    *(LAS u32x4*)(img + 3072 + lane * 16) = (u32x4){o0, o1, o2, o3};
  }
  if (lane < 4) *(LAS u32x4*)(img + 4096 + lane * 16) = (u32x4){0u, 0u, 0u, 0u};
}

template <int NT> DI void qgemm(lptr lds, LAS unsigned* ctrl, const bf16x8 (&xa)[18], int mt, int lane) {
  const int l15 = lane & 15, g = lane >> 4;
  constexpr int nt0 = NT == 2 ? 3 : 0;
  f32x4 acc[NT];
#pragma unroll
  for (int i = 0; i < NT; ++i) acc[i] = (f32x4){0.f, 0.f, 0.f, 0.f};
  const lptr wq = lds + A_W + (g * 8 + (l15 >> 2) * 2) * 144 + (l15 & 3) * 8 + nt0 * 32;
  constexpr int PD = NT == 3 ? 4 : 6;
  bf16x8 wf[PD + 1][NT];
  unsigned stage = 0;
#define WNEED(n) do { while (stage < (n)) { stage = __builtin_amdgcn_readfirstlane(__hip_atomic_load(&ctrl[4], __ATOMIC_RELAXED, __HIP_MEMORY_SCOPE_WORKGROUP)); \
                                            if (stage < (n)) __builtin_amdgcn_s_sleep(1); } CFENCE(); } while (0)
#pragma unroll
  for (int kk = 0; kk < 18 + PD; ++kk) {
    if (kk >= PD) {
#pragma unroll
      for (int i = 0; i < NT; ++i) acc[i] = MFMA16(wf[(kk - PD) % (PD + 1)][i], xa[kk - PD], acc[i]);
    }
    __builtin_amdgcn_sched_barrier(0);
    if (kk < 18) {
      if ((kk & 1) == 0) WNEED((unsigned)(kk / 2 + 1));
#pragma unroll
      for (int i = 0; i < NT; ++i) wf[kk % (PD + 1)][i] = trd2(wq + kk * 4608 + i * 32, wq + kk * 4608 + 144 + i * 32);
    }
    __builtin_amdgcn_sched_barrier(0);
  }
  LAS unsigned* qw = (LAS unsigned*)(lds + A_Q + (mt * 16 + l15) * Q_ROW + nt0 * 32 + g * 8);
#pragma unroll
  for (int i = 0; i < NT; ++i)
    if (nt0 + i < 4 || g < 2) *(LAS u32x2*)(qw + 8 * i) = (u32x2){pk2(acc[i][0] * QSCALE, acc[i][1] * QSCALE), pk2(acc[i][2] * QSCALE, acc[i][3] * QSCALE)};
  CFENCE();
  if (lane == 0) __hip_atomic_fetch_add(&ctrl[5], 1u, __ATOMIC_RELAXED, __HIP_MEMORY_SCOPE_WORKGROUP);
}

DI f32x32 qk_tile(lptr kp, const bf16x8& q0, const bf16x8& q1, unsigned kt, u32x2 qa) {
  const bf16x8 k = *(LAS const bf16x8*)kp;
  f32x16 z;
#pragma unroll
  for (int j = 0; j < 16; ++j) z[j] = 0.f;
  const f32x16 a = MFMA32(k, q0, z), b = MFMA32(k, q1, z);
  f32x32 s = __builtin_shufflevector(a, b, 0, 1, 2, 3, 4, 5, 6, 7, 8, 9, 10, 11, 12, 13, 14, 15, 16, 17, 18, 19, 20, 21, 22, 23, 24, 25, 26, 27, 28, 29, 30, 31);
  return __builtin_amdgcn_mfma_f32_32x32x4bf16_1k(__builtin_bit_cast(s16x4, (u32x2){kt, 0u}), __builtin_bit_cast(s16x4, qa), s, 0, 0, 0);
}
DI bf16x16 sub8(const f32x32& s, int o) {
  f32x8 t = {s[o], s[o + 1], s[o + 2], s[o + 3], s[o + 4], s[o + 5], s[o + 6], s[o + 7]};
  return __builtin_bit_cast(bf16x16, t);
}

__global__ __launch_bounds__(768, 3) void fused_kernel(const u16* __restrict__ U, const u16* __restrict__ Are, const u16* __restrict__ Aim,
                                                       const u16* __restrict__ Bre, const u16* __restrict__ Bim, const u16* __restrict__ Cre,
                                                       const u16* __restrict__ Cim, const u16* __restrict__ logdt, const u16* __restrict__ Dskip,
                                                       const u16* __restrict__ X, const u16* __restrict__ Wq, const u16* __restrict__ Wk,
                                                       const u16* __restrict__ Wv, const u16* __restrict__ Wo, u16* __restrict__ out,
                                                       char* __restrict__ ws) {
  __shared__ __attribute__((aligned(16))) char lds_raw[LDS_BYTES];
  const lptr lds = (lptr)lds_raw;
  const unsigned lds0 = (unsigned)(uintptr_t)lds_raw;
  STAMP(0);
  const int tid = threadIdx.x, lane = tid & 63, wid = __builtin_amdgcn_readfirstlane(tid >> 6);
  const int l15 = lane & 15, g = lane >> 4, r32 = lane & 31, h = lane >> 5, q4 = l15 >> 2, p4 = l15 & 3;
  const int b = blockIdx.x, qb = (b & 7) * 4 + (b >> 6), hg = (b >> 3) & 7;
  const int ce = 512 + (b >> 2), cq = b & 3, mt = wid & 3;
  char* const myflag = ws + WS_F + (qb * 8 + hg) * 128;
  LAS unsigned* ctrl = (LAS unsigned*)(lds + A_CTRL);
  LAS unsigned* bctl = (LAS unsigned*)(lds + B_CTRL);
#define LADD(i) do { if (lane == 0) __hip_atomic_fetch_add(&ctrl[i], 1u, __ATOMIC_RELAXED, __HIP_MEMORY_SCOPE_WORKGROUP); } while (0)
#define LSET(i, v) do { if (lane == 0) __hip_atomic_store(&ctrl[i], (v), __ATOMIC_RELAXED, __HIP_MEMORY_SCOPE_WORKGROUP); } while (0)
  asm volatile("" :: "s"(U), "s"(Are), "s"(Aim), "s"(Bre), "s"(Bim), "s"(Cre), "s"(Cim), "s"(logdt), "s"(Dskip), "s"(X), "s"(Wq), "s"(Wk), "s"(Wv), "s"(Wo),
               "s"(out), "s"(ws));
  const int xoff = mt == 0 ? A_X0 : A_W + 9216 + mt * 18432;
  const unsigned xv0 = (lane >> 3) * ROWB + (((lane & 7) ^ (lane >> 4)) * 16), xv1 = (8 + (lane >> 3)) * ROWB + (((lane & 7) ^ (4 + (lane >> 4))) * 16);
  const char* xs = (const char*)X + (size_t)(qb * 64 + mt * 16) * ROWB;
  const bool kgw = mt != (wid < 4 ? 0 : 3) && (wid < 4 || wid >= 8);
  const int kc = wid < 4 ? wid - 1 : wid - 8, kch = kc < 2 ? 2 * b + kc : ce;
  int msc = kgw ? A_SCR + (2 * kc + (wid < 4 ? 1 : 0)) * KG_TAB + 49 * 16 : A_MISC + (wid == 11 ? 80 : (wid == 0 ? 64 : mt * 16));
  asm volatile("" : "+v"(msc)); msc = __builtin_amdgcn_readfirstlane(msc);
  if (wid >= 4 && wid < 8) {
    __builtin_amdgcn_s_setprio(3);
#pragma unroll
    for (int kb = 0; kb < 2; ++kb) { gldss(xv0, xs + kb * 128, lds0 + xoff + kb * 2048); gldss(xv1, xs + kb * 128, lds0 + xoff + kb * 2048 + 1024); }
  }
  {
    const int a6 = lane >> 3;
    const u16* pa = a6 < 2 ? (a6 == 0 ? Are : Aim) : (a6 < 4 ? (a6 == 2 ? Bre : Bim) : (a6 == 4 ? Cre : Cim));
    pa += kch * NS + (wid < 4 ? 64 : 0) + (lane & 7) * 8;
    if (lane == 48) pa = logdt + (kch & ~7);
    if (lane == 49) pa = (const u16*)(ws + WS_E);
    if (lane >= 50) pa = Dskip + ((lane == 50 ? 2 * b : ce) & ~7);
    if (lane >= (kgw ? 0 : 49) && lane < (wid >= 8 ? 52 : 50)) glds16c(pa, lds0 + msc - 49 * 16);
  }
  if (wid < 4 || wid >= 8) {
    const int j = b >> 3;
    const char* tb0; unsigned tv0;
    if (wid == 0) { tb0 = (const char*)X + (size_t)qb * 64 * ROWB + hg * 72 * 128; tv0 = lane * 144; }
    else if (wid < 4) { tb0 = (const char*)Wq + (j * 162 + (wid - 1) * 54) * 128; tv0 = min(lane, 53) * 128; }
    else if (wid < 10) { tb0 = (const char*)U + (size_t)qb * 64 * ROWB + hg * 144 + (wid - 8) * 140; tv0 = lane * ROWB; }
    else if (wid == 10) { tb0 = ws; tv0 = 1024 + (lane & 3) * (2u << 20); }
    else { tb0 = (const char*)(hg < 4 ? Wk : Wv); tv0 = min(lane * 4 + (j >> 3), 161) * 128; }
    touchs(tv0, tb0, lds0 + A_W - 256);
  }
  if (tid < 32) ctrl[tid] = 0u;
  STAMP(17);
  BAR();
  STAMP(18);
#define TAG() (*(LAS const unsigned*)(lds + msc) * 2654435761u + 0x9e3779b9u)
  auto kg_read = [&](kg_in& in) {
    LAS const u16* p = (LAS const u16*)(lds + msc - 49 * 16) + lane;
    in.are = p[0]; in.aim = p[64]; in.bre = p[128]; in.bim = p[192]; in.cre = p[256]; in.cim = p[320];
    in.ldt = ((LAS const u16*)(lds + msc - 16))[kch & 7];
    CFENCE();
  };
  bf16x8 wf[6][4];
  const int kt = wid >> 2;
  auto load_wf = [&]() {
    const int sw = 2 * (((q4 >> 1) & 1) | ((g & 1) << 1));
    const lptr wb = lds + kt * 24576 + (g * 8 + q4) * 128 + (p4 & 1) * 8;
#pragma unroll
    for (int nt = 0; nt < 4; ++nt) {
      const lptr p = wb + (((2 * nt + (p4 >> 1)) ^ sw) * 16);
#pragma unroll
      for (int kk = 0; kk < 6; ++kk) wf[kk][nt] = trd2(p + kk * 4096, p + kk * 4096 + 512);
    }
  };
  auto any_bad = [&]() {
    const u32x4 a = *(LAS const u32x4*)bctl, c = *(LAS const u32x4*)(bctl + 4);
    return __builtin_amdgcn_readfirstlane((int)(a[0] | a[1] | a[2] | a[3] | c[0] | c[1] | c[2] | c[3]));
  };

  if (wid < 8) {
    {
      const lptr xb0 = lds + xoff + l15 * 128 + ((g ^ (l15 >> 1)) * 16), xb1 = lds + xoff + l15 * 128 + (((4 + g) ^ (l15 >> 1)) * 16);
      bf16x8 xa[18];
      if (wid >= 4) {
#pragma unroll
        for (int kb = 2; kb < 9; ++kb) {
          gldss(xv0, xs + kb * 128, lds0 + xoff + kb * 2048); gldss(xv1, xs + kb * 128, lds0 + xoff + kb * 2048 + 1024);
          if (kb == 6) LADD(3);
        }
        STAMP(1);
        char* cbp = ws + WS_KV + (qb >> 2) * CH_BYTES;
        const int key0 = (qb & 3) * 64 + mt * 16;
        char* sp;
        if (hg < 4) {
          const int key = key0 + l15;
          sp = g == 0 ? cbp + (key >> 5) * 1024 + (hg >> 1) * 512 + (key & 31) * 16 + (hg & 1) * 8 : cbp + CH_TAIL + (key & 63) * 16 + (key >> 6) * 4;
        } else {
          const int key = key0 + 4 * g, dim = 4 * (hg - 4) + l15;
          const int vrow = l15 < 4 ? ((dim & 3) | ((dim & 4) << 1) | ((dim & 8) >> 1)) : 12 + l15;
          sp = cbp + CH_V + (key >> 3) * V_OCT + (key & 4) * 2 + vrow * 16;
        }
        PIN(sp);
        lds_wait(&ctrl[0], 1u);
        const lptr wb = lds + A_WKV + (g * 8 + q4) * 16 + (l15 & 1) * 8;
        bf16x8 wk[18];
#pragma unroll
        for (int i = 0; i < 18; ++i) wk[i] = trd2(wb + i * 512, wb + i * 512 + 64);
        STAMP(2);
        f32x4 acc = {0.f, 0.f, 0.f, 0.f}, acc2 = acc;
#define XGRP(kb0, n, KTYPE)                                                                                                    \
        { VMWAIT(n);                                                                                                           \
          if ((n) == 0) LSET(8 + mt, 1u);                                                                                      \
          _Pragma("unroll") for (int i = 0; i < 6; ++i) xa[2 * (kb0) + i] = *(LAS const bf16x8*)(((i & 1) ? xb1 : xb0) + ((kb0) + (i >> 1)) * 2048); \
          CFENCE();                                                                                                            \
          if ((n) == 0) LADD(12 + mt);                                                                                         \
          _Pragma("unroll") for (int i = 2 * (kb0); i < 2 * (kb0) + 6; i += 2) {                                                \
            if (KTYPE) { acc = MFMA16(wk[i], xa[i], acc); acc2 = MFMA16(wk[i + 1], xa[i + 1], acc2); }                          \
            else { acc = MFMA16(xa[i], wk[i], acc); acc2 = MFMA16(xa[i + 1], wk[i + 1], acc2); } } }
        if (hg < 4) { XGRP(0, 12, 1) XGRP(3, 6, 1) XGRP(6, 0, 1) } else { XGRP(0, 12, 0) XGRP(3, 6, 0) XGRP(6, 0, 0) }
        STAMP(3);
        acc += acc2;
        u32x2 v = {pk2(acc[0], acc[1]), pk2(acc[2], acc[3])};
        unsigned mv;
        if (hg < 4) {
          if (g == 0) st8c(sp, v);
          if (g == 1 && hg == 3) st4c(sp, v[0]);
          float n2 = bflo(v[0]) * bflo(v[0]) + bfhi(v[0]) * bfhi(v[0]);
          if (g == 0) n2 += bflo(v[1]) * bflo(v[1]) + bfhi(v[1]) * bfhi(v[1]);
          const unsigned m16 = max16(__float_as_uint(n2) & 0x7fffffffu);
          const float mq = __uint_as_float(__builtin_amdgcn_readlane(m16, 0)), mtl = __uint_as_float(__builtin_amdgcn_readlane(m16, 16));
          mv = __float_as_uint(hg == 3 ? mq + mtl : mq) & 0x7fffffffu;
        } else {
          unsigned am = umax(umax(v[0] << 16 & 0x7fffffffu, v[0] & 0x7fff0000u), umax(v[1] << 16 & 0x7fffffffu, v[1] & 0x7fff0000u));
          if (l15 >= 6) am = 0u;
          if (l15 == 6) v = (u32x2){0x3f803f80u, 0x3f803f80u};
          if (l15 == 7) v = (u32x2){0u, 0u};
          if (l15 < 4 || (hg == 7 && l15 < 8)) st8c(sp, v);
          am = max16(am);
          mv = umax(umax(__builtin_amdgcn_readlane(am, 0), __builtin_amdgcn_readlane(am, 16)), umax(__builtin_amdgcn_readlane(am, 32), __builtin_amdgcn_readlane(am, 48)));
        }
        if (lane == 0) __hip_atomic_fetch_max(&ctrl[2], mv, __ATOMIC_RELAXED, __HIP_MEMORY_SCOPE_WORKGROUP);
        STAMP(4);
      } else {
        if (wid == 0) {
          __builtin_amdgcn_s_setprio(3);
          {
            const char* wx = (const char*)(hg < 4 ? Wk : Wv) + 8 * (hg & 3);
#pragma unroll 1
            for (int i = 0; i < 8; ++i) gldss(lane * 36, wx + i * 64 * 36, lds0 + A_WKV + i * 1024);
            if (lane < 63) gldss(lane * 36, wx + 512 * 36, lds0 + A_WKV + 8192);
            if (lane == 0) {
              unsigned sv_;
              asm volatile("s_mov_b32 %0, m0\n\ts_mov_b32 m0, %3\n\ts_nop 0\n\tglobal_load_lds_dwordx3 %1, %2\n\ts_mov_b32 m0, %0"
                           : "=&s"(sv_) : "v"(575 * 36), "s"(wx), "s"(lds0 + A_WKV + 575 * 16) : "memory");
            }
          }
          __builtin_amdgcn_s_setprio(2);
          unsigned vo[9];
          slice_offsets(vo, lane);
          VMWAIT(0);
          LSET(0, 1u);
          STAMP(1);
          glds9(vo, (const char*)U + (size_t)qb * 64 * ROWB + hg * 144 - 4096, lds0 + A_UT);
          const lptr rp = lds + A_UT + (((4 * g + p4) & 7) * 8 + 2 * q4) * 144 + (g >> 1) * 8;
          char* up = ws + WS_UT + (size_t)(hg * 72 + (lane >> 5) * 4 + (lane & 3)) * UT_ROW + 128 + qb * 128 + ((lane >> 2) & 7) * 16;
          VMWAIT(0);
#pragma unroll
          for (int i = 0; i < 9; ++i) st16c(up + i * 8 * UT_ROW, __builtin_bit_cast(u32x4, trd2(rp + i * 16, rp + i * 16 + 144)));
          if (qb == 0) {
            st16c(ws + WS_UT + (size_t)(hg * 72 + lane) * UT_ROW + 112, (u32x4){0u, 0u, 0u, 0u});
            st16c(ws + WS_UT + (size_t)(hg * 72 + 64 + (lane & 7)) * UT_ROW + 112, (u32x4){0u, 0u, 0u, 0u});
          }
          STAMP(2);
        } else {
          __builtin_amdgcn_s_setprio(2);
          VMWAIT(1);
          kg_in in; kg_read(in);
          STAMP(1);
          f32x4 d[2][2];
          const lptr tb = lds + A_SCR + (2 * wid - 1) * KG_TAB;
          kgen_half(in, tb, lane, d);
#pragma unroll
          for (int i = 0; i < 4; ++i) *(LAS f32x4*)(tb + i * 1024 + lane * 16) = d[i >> 1][i & 1];
          CFENCE();
          LSET(15 + wid, 1u);
          VMWAIT(0);
          STAMP(2);
        }
        lds_wait(&ctrl[8 + mt], 1u);
#pragma unroll
        for (int kk = 0; kk < 18; ++kk) xa[kk] = *(LAS const bf16x8*)(((kk & 1) ? xb1 : xb0) + (kk >> 1) * 2048);
        CFENCE();
        LADD(12 + mt);
        STAMP(3);
        STAMP(4);
      }
      if (wid >= 4) {
        VMWAIT(0);
        if (lane == 0 && __hip_atomic_fetch_add(&ctrl[1], 1u, __ATOMIC_RELAXED, __HIP_MEMORY_SCOPE_WORKGROUP) == 3u)
          st8c(myflag, (u32x2){TAG(), __hip_atomic_load(&ctrl[2], __ATOMIC_RELAXED, __HIP_MEMORY_SCOPE_WORKGROUP)});
        __builtin_amdgcn_s_setprio(2);
        STAMP(5);
        qgemm<2>(lds, ctrl, xa, mt, lane);
      } else {
        VMWAIT(0);
        if (wid == 0 && lane == 0) st4c(myflag + 8, TAG());
        STAMP(5);
        qgemm<3>(lds, ctrl, xa, mt, lane);
      }
    }
    STAMP(8);
    const int kp = wid >> 2, head = wid & 3;
    f32x16 o0, o1;
#pragma unroll
    for (int j = 0; j < 16; ++j) { o0[j] = 0.f; o1[j] = 0.f; }
    const unsigned ka0 = lds0 + KV_LDS + kp * 1024 + lane * 16, ta0 = lds0 + KV_LDS + CH_TAIL + kp * 512 + r32 * 16,
                   va0 = lds0 + KV_LDS + CH_V + kp * 4 * V_OCT + h * V_OCT + (r32 < 19 ? r32 : 19) * 16;
    u32x4 q0, q1; unsigned qtl;
    lds_wait(&ctrl[5], 8u);
    {
      LAS const unsigned* qp = (LAS const unsigned*)(lds + A_Q + r32 * Q_ROW + head * 36 + h * 16);
#pragma unroll
      for (int j = 0; j < 4; ++j) { q0[j] = qp[j]; q1[j] = qp[32 * Q_ROW / 4 + j]; }
      qtl = *(LAS const unsigned*)(lds + A_Q + lane * Q_ROW + head * 36 + 32);
    }
    CFENCE();
    LADD(6);
    const u32x2 qa = {qtl, 0u}, qc = {0u, qtl};
    STAMP(9);
    lds_wait(&ctrl[7], 1u);
    STAMP(10);
    {
      int it = 8; unsigned ka = ka0, va = va0, ta = ta0;
      asm volatile(
      "ds_read_b128 v[128:131], %[ka]\n"
      "ds_read_b128 v[140:143], %[ta]\n"
      "v_mov_b32 v120, 0\n"
      "v_mov_b32 v121, 0\n"
      "v_mov_b32 v122, 0\n"
      "v_mov_b32 v123, 0\n"
      "v_mov_b32 v124, 0\n"
      "v_mov_b32 v125, 0\n"
      "v_mov_b32 v126, 0\n"
      "v_mov_b32 v127, 0\n"
      "v_mov_b32 v136, 0\n"
      "v_mov_b32 v137, 0\n"
      "v_mov_b32 v138, 0\n"
      "v_mov_b32 v139, 0\n"
      "s_waitcnt lgkmcnt(1)\n"
      "v_mfma_f32_32x32x16_bf16 v[64:79], v[128:131], %[q0], 0\n"
      "v_mfma_f32_32x32x16_bf16 v[80:95], v[128:131], %[q1], 0\n"
      "ds_read_b128 v[128:131], %[ka] offset:2048\n"
      "ds_read_b128 v[132:135], %[va]\n"
      "s_nop 7\n"
      "s_waitcnt lgkmcnt(2)\n"
      "v_mfma_f32_32x32x4_2b_bf16 v[64:95], v[140:141], %[qa], v[64:95]\n"
      "s_nop 15\n"
      "s_nop 1\n"
      "1:\n"
      "s_setprio 1\n"
      "s_waitcnt lgkmcnt(1)\n"
      "v_mfma_f32_32x32x16_bf16 v[96:111], v[128:131], %[q0], 0\n"
      "v_exp_f32 v64, v64\n"
      "v_exp_f32 v65, v65\n"
      "v_exp_f32 v66, v66\n"
      "v_smfmac_f32_32x32x32_bf16 %[o1], v[136:139], v[120:127], %[ix]\n"
      "ds_read_b128 v[136:139], %[va] offset:640\n"
      "v_exp_f32 v67, v67\n"
      "v_exp_f32 v68, v68\n"
      "v_exp_f32 v69, v69\n"
      "v_mfma_f32_32x32x16_bf16 v[112:127], v[128:131], %[q1], 0\n"
      "ds_read_b128 v[128:131], %[ka] offset:4096\n"
      "v_exp_f32 v70, v70\n"
      "v_exp_f32 v71, v71\n"
      "v_exp_f32 v72, v72\n"
      "v_exp_f32 v73, v73\n"
      "s_waitcnt lgkmcnt(2)\n"
      "v_smfmac_f32_32x32x32_bf16 %[o0], v[132:135], v[64:71], %[ix]\n"
      "v_exp_f32 v74, v74\n"
      "v_exp_f32 v75, v75\n"
      "v_exp_f32 v76, v76\n"
      "v_exp_f32 v77, v77\n"
      "v_exp_f32 v78, v78\n"
      "v_mfma_f32_32x32x4_2b_bf16 v[96:127], v[140:141], %[qc], v[96:127]\n"
      "v_exp_f32 v79, v79\n"
      "v_exp_f32 v80, v80\n"
      "v_exp_f32 v81, v81\n"
      "v_exp_f32 v82, v82\n"
      "v_exp_f32 v83, v83\n"
      "v_exp_f32 v84, v84\n"
      "v_exp_f32 v85, v85\n"
      "v_exp_f32 v86, v86\n"
      "s_waitcnt lgkmcnt(1)\n"
      "v_smfmac_f32_32x32x32_bf16 %[o0], v[136:139], v[72:79], %[ix]\n"
      "v_exp_f32 v87, v87\n"
      "v_exp_f32 v88, v88\n"
      "v_exp_f32 v89, v89\n"
      "v_exp_f32 v90, v90\n"
      "v_smfmac_f32_32x32x32_bf16 %[o1], v[132:135], v[80:87], %[ix]\n"
      "ds_read_b128 v[132:135], %[va] offset:2560\n"
      "v_exp_f32 v91, v91\n"
      "v_exp_f32 v92, v92\n"
      "v_exp_f32 v93, v93\n"
      "v_exp_f32 v94, v94\n"
      "v_exp_f32 v95, v95\n"
      "s_setprio 0\n"
      "s_waitcnt lgkmcnt(1)\n"
      "v_mfma_f32_32x32x16_bf16 v[64:79], v[128:131], %[q0], 0\n"
      "v_exp_f32 v96, v96\n"
      "v_exp_f32 v97, v97\n"
      "v_exp_f32 v98, v98\n"
      "v_smfmac_f32_32x32x32_bf16 %[o1], v[136:139], v[88:95], %[ix]\n"
      "ds_read_b128 v[136:139], %[va] offset:3200\n"
      "v_exp_f32 v99, v99\n"
      "v_exp_f32 v100, v100\n"
      "v_exp_f32 v101, v101\n"
      "v_mfma_f32_32x32x16_bf16 v[80:95], v[128:131], %[q1], 0\n"
      "ds_read_b128 v[128:131], %[ka] offset:6144\n"
      "v_exp_f32 v102, v102\n"
      "v_exp_f32 v103, v103\n"
      "v_exp_f32 v104, v104\n"
      "v_exp_f32 v105, v105\n"
      "s_waitcnt lgkmcnt(2)\n"
      "v_smfmac_f32_32x32x32_bf16 %[o0], v[132:135], v[96:103], %[ix]\n"
      "v_exp_f32 v106, v106\n"
      "v_exp_f32 v107, v107\n"
      "v_exp_f32 v108, v108\n"
      "v_exp_f32 v109, v109\n"
      "v_exp_f32 v110, v110\n"
      "v_mfma_f32_32x32x4_2b_bf16 v[64:95], v[142:143], %[qa], v[64:95]\n"
      "v_exp_f32 v111, v111\n"
      "v_exp_f32 v112, v112\n"
      "v_exp_f32 v113, v113\n"
      "v_exp_f32 v114, v114\n"
      "v_exp_f32 v115, v115\n"
      "v_exp_f32 v116, v116\n"
      "v_exp_f32 v117, v117\n"
      "v_exp_f32 v118, v118\n"
      "s_waitcnt lgkmcnt(1)\n"
      "v_smfmac_f32_32x32x32_bf16 %[o0], v[136:139], v[104:111], %[ix]\n"
      "v_exp_f32 v119, v119\n"
      "v_exp_f32 v120, v120\n"
      "v_exp_f32 v121, v121\n"
      "v_exp_f32 v122, v122\n"
      "v_smfmac_f32_32x32x32_bf16 %[o1], v[132:135], v[112:119], %[ix]\n"
      "ds_read_b128 v[132:135], %[va] offset:5120\n"
      "v_exp_f32 v123, v123\n"
      "v_exp_f32 v124, v124\n"
      "v_exp_f32 v125, v125\n"
      "v_exp_f32 v126, v126\n"
      "v_exp_f32 v127, v127\n"
      "s_setprio 3\n"
      "s_waitcnt lgkmcnt(1)\n"
      "v_mfma_f32_32x32x16_bf16 v[96:111], v[128:131], %[q0], 0\n"
      "v_exp_f32 v64, v64\n"
      "v_exp_f32 v65, v65\n"
      "v_exp_f32 v66, v66\n"
      "v_smfmac_f32_32x32x32_bf16 %[o1], v[136:139], v[120:127], %[ix]\n"
      "ds_read_b128 v[136:139], %[va] offset:5760\n"
      "v_exp_f32 v67, v67\n"
      "v_exp_f32 v68, v68\n"
      "v_exp_f32 v69, v69\n"
      "v_mfma_f32_32x32x16_bf16 v[112:127], v[128:131], %[q1], 0\n"
      "s_setprio 2\n"
      "s_waitcnt lgkmcnt(1)\n"
      "v_mfma_f32_32x32x16_bf16 v[64:79], v[128:131], %[q0], 0\n"
      "v_exp_f32 v96, v96\n"
      "v_exp_f32 v97, v97\n"
      "v_exp_f32 v98, v98\n"
      "v_smfmac_f32_32x32x32_bf16 %[o1], v[136:139], v[88:95], %[ix]\n"
      "ds_read_b128 v[136:139], %[va] offset:8320\n"
      "v_exp_f32 v99, v99\n"
      "v_exp_f32 v100, v100\n"
      "v_exp_f32 v101, v101\n"
      "v_mfma_f32_32x32x16_bf16 v[80:95], v[128:131], %[q1], 0\n"
      "ds_read_b128 v[128:131], %[ka] offset:21504\n"
      "v_exp_f32 v102, v102\n"
      "v_exp_f32 v103, v103\n"
      "v_exp_f32 v104, v104\n"
      "v_exp_f32 v105, v105\n"
      "s_waitcnt lgkmcnt(2)\n"
      "v_smfmac_f32_32x32x32_bf16 %[o0], v[132:135], v[96:103], %[ix]\n"
      "v_exp_f32 v106, v106\n"
      "v_exp_f32 v107, v107\n"
      "v_exp_f32 v108, v108\n"
      "v_exp_f32 v109, v109\n"
      "v_exp_f32 v110, v110\n"
      "v_mfma_f32_32x32x4_2b_bf16 v[64:95], v[140:141], %[qa], v[64:95]\n"
      "v_exp_f32 v111, v111\n"
      "v_exp_f32 v112, v112\n"
      "v_exp_f32 v113, v113\n"
      "v_exp_f32 v114, v114\n"
      "v_exp_f32 v115, v115\n"
      "v_exp_f32 v116, v116\n"
      "v_exp_f32 v117, v117\n"
      "v_exp_f32 v118, v118\n"
      "s_waitcnt lgkmcnt(1)\n"
      "v_smfmac_f32_32x32x32_bf16 %[o0], v[136:139], v[104:111], %[ix]\n"
      "v_exp_f32 v119, v119\n"
      "v_exp_f32 v120, v120\n"
      "v_exp_f32 v121, v121\n"
      "v_exp_f32 v122, v122\n"
      "v_smfmac_f32_32x32x32_bf16 %[o1], v[132:135], v[112:119], %[ix]\n"
      "ds_read_b128 v[132:135], %[va] offset:19456\n"
      "v_exp_f32 v123, v123\n"
      "v_exp_f32 v124, v124\n"
      "v_exp_f32 v125, v125\n"
      "v_exp_f32 v126, v126\n"
      "v_exp_f32 v127, v127\n"
      "v_add_u32 %[ka], 19456, %[ka]\n"
      "v_add_u32 %[va], 19456, %[va]\n"
      "v_add_u32 %[ta], 19456, %[ta]\n"
      "s_sub_u32 %[it], %[it], 1\n"
      "s_cmp_lg_u32 %[it], 0\n"
      "s_cbranch_scc1 1b\n"
      "v_smfmac_f32_32x32x32_bf16 %[o1], v[136:139], v[120:127], %[ix]\n"
      "s_setprio 0\n"
      "s_waitcnt lgkmcnt(0)\n"
      "s_nop 15\n"
          : [o0] "+v"(o0), [o1] "+v"(o1), [ka] "+v"(ka), [va] "+v"(va), [ta] "+v"(ta), [it] "+s"(it)
          : [q0] "v"(q0), [q1] "v"(q1), [qa] "v"(qa), [qc] "v"(qc), [ix] "v"(0xdddddddd)
          : "v64", "v65", "v66", "v67", "v68", "v69", "v70", "v71", "v72", "v73", "v74", "v75", "v76", "v77", "v78", "v79", "v80", "v81", "v82", "v83", "v84", "v85", "v86", "v87", "v88", "v89", "v90", "v91", "v92", "v93", "v94", "v95", "v96", "v97", "v98", "v99", "v100", "v101", "v102", "v103", "v104", "v105", "v106", "v107", "v108", "v109", "v110", "v111", "v112", "v113", "v114", "v115", "v116", "v117", "v118", "v119", "v120", "v121", "v122", "v123", "v124", "v125", "v126", "v127", "v128", "v129", "v130", "v131", "v132", "v133", "v134", "v135", "v136", "v137", "v138", "v139", "v140", "v141", "v142", "v143", "scc", "memory");
    }
    STAMP(13);
    const lptr xo = lds + C_OX + lane * 16;
    auto put_half = [&]() {
#pragma unroll
      for (int i = 0; i < 3; ++i) {
        const f32x4 a = {o0[4 * i], o0[4 * i + 1], o0[4 * i + 2], o0[4 * i + 3]}, c = {o1[4 * i], o1[4 * i + 1], o1[4 * i + 2], o1[4 * i + 3]};
        *(LAS f32x4*)(xo + (wid * 3 + i) * 1024) = kp ? a : c;
      }
    };
    put_half();
    {
#define DOT2(x, c) __builtin_amdgcn_fdot2_f32_bf16(__builtin_bit_cast(bf16x2, x), __builtin_bit_cast(bf16x2, x), (c), false)
      const float nt = DOT2(qtl, 0.f);
      float n0 = h ? 0.f : nt, n1 = h ? nt : 0.f;
#pragma unroll
      for (int j = 0; j < 4; ++j) { n0 = DOT2(q0[j], n0); n1 = DOT2(q1[j], n1); }
      const auto sw = __builtin_amdgcn_permlane32_swap(__float_as_uint(n0), __float_as_uint(n1), false, false);
      const float n2 = __uint_as_float(sw[0]) + __uint_as_float(sw[1]);
      const bool bad = __any(!(n2 * __uint_as_float(bctl[8]) <= 8000.f));
      if (lane == 0) bctl[wid] = bad ? 1u : 0u;
    }
    BAR();
    if (any_bad()) {
      BAR(); BAR();
      const bf16x8 qf0 = __builtin_bit_cast(bf16x8, q0), qf1 = __builtin_bit_cast(bf16x8, q1);
      float m0 = -INFINITY, m1 = -INFINITY;
#pragma unroll 1
      for (int i = 0; i < 32; ++i) {
        const int off = (i >> 2) * CH_BYTES;
        const f32x32 s = qk_tile((lptr)(uintptr_t)(ka0 + off + (i & 3) * 2048), qf0, qf1, *(LAS const unsigned*)(uintptr_t)(ta0 + off + (i & 3) * 4), qa);
#pragma unroll
        for (int j = 0; j < 16; ++j) { m0 = fmaxf(m0, s[j]); m1 = fmaxf(m1, s[16 + j]); }
      }
      m0 = fmaxf(m0, __shfl_xor(m0, 32)); m1 = fmaxf(m1, __shfl_xor(m1, 32));
      *(LAS f32x2*)(lds + SPARE + tid * 8) = (f32x2){m0, m1};
      BAR();
      const f32x2 mo = *(LAS const f32x2*)(lds + SPARE + (tid ^ 256) * 8);
      m0 = fmaxf(m0, mo[0]); m1 = fmaxf(m1, mo[1]);
#pragma unroll
      for (int j = 0; j < 16; ++j) { o0[j] = 0.f; o1[j] = 0.f; }
#pragma unroll 1
      for (int i = 0; i < 32; ++i) {
        const int off = (i >> 2) * CH_BYTES;
        f32x32 s = qk_tile((lptr)(uintptr_t)(ka0 + off + (i & 3) * 2048), qf0, qf1, *(LAS const unsigned*)(uintptr_t)(ta0 + off + (i & 3) * 4), qa);
#pragma unroll
        for (int j = 0; j < 16; ++j) { s[j] = __builtin_amdgcn_exp2f(s[j] - m0); s[16 + j] = __builtin_amdgcn_exp2f(s[16 + j] - m1); }
        const bf16x8 v0 = *(LAS const bf16x8*)(uintptr_t)(va0 + off + (i & 3) * 8 * V_OCT), v1 = *(LAS const bf16x8*)(uintptr_t)(va0 + off + ((i & 3) * 8 + 2) * V_OCT);
        o0 = SMF(v0, sub8(s, 0), o0); o0 = SMF(v1, sub8(s, 8), o0); o1 = SMF(v0, sub8(s, 16), o1); o1 = SMF(v1, sub8(s, 24), o1);
      }
      BAR();
      put_half();
      BAR();
    }
    float f[12];
#pragma unroll
    for (int i = 0; i < 3; ++i) {
      const f32x4 p = *(LAS const f32x4*)(xo + ((wid ^ 4) * 3 + i) * 1024);
#pragma unroll
      for (int j = 0; j < 4; ++j) f[4 * i + j] = (kp ? o1[4 * i + j] : o0[4 * i + j]) + p[j];
    }
    const auto sw = __builtin_amdgcn_permlane32_swap(__float_as_uint(f[10]), __float_as_uint(f[10]), false, false);
    const float inv = __builtin_amdgcn_rcpf(__uint_as_float(sw[0]));
    char* ob = ws + WS_O + (size_t)(qb * 4 + kp * 2 + (r32 >> 4)) * 18432 + l15 * 16;
    const int O = 9 * hg + 2 * head + h, Ot = 9 * hg + 8;
    st16c(ob + O * 256, (u32x4){nosent(pk2(f[0] * inv, f[1] * inv)), nosent(pk2(f[2] * inv, f[3] * inv)),
                                nosent(pk2(f[4] * inv, f[5] * inv)), nosent(pk2(f[6] * inv, f[7] * inv))});
    if (h == 0) st4c(ob + Ot * 256 + head * 4, nosent(pk2(f[8] * inv, f[9] * inv)));
    STAMP(11);
    load_wf();
    VMWAIT(0);
  } else {
    const int sv = wid - 8, chl = sv >> 1, half = sv & 1, rot = b & 7;
    const unsigned l16 = lane * 16;
    VMWAIT(1);
    const unsigned tag = __builtin_amdgcn_readfirstlane(TAG());
    const float dsk = bflo(__builtin_amdgcn_readfirstlane(((LAS const u16*)(lds + msc + 16))[(2 * b + chl) & 7])),
                dse = bflo(__builtin_amdgcn_readfirstlane(((LAS const u16*)(lds + msc + 32))[ce & 7]));
#define KVC(s0, s1) _Pragma("unroll 1") for (int i = 5 * (s0) + sv; i < 5 * (s1); i += 4) kv_group(ws, lds0, i / 5, rot, i % 5, l16)
    if (sv < 3) {
      __builtin_amdgcn_s_setprio(1);
      kg_in in; kg_read(in);
      STAMP(1);
      f32x4 d[2][2];
      const lptr tb = lds + A_SCR + 2 * sv * KG_TAB;
      kgen_half(in, tb, lane, d);
      VMWAIT(0);
      STAMP(2);
      lds_wait(&ctrl[16 + sv], 1u);
#pragma unroll
      for (int i = 0; i < 4; ++i) d[i >> 1][i & 1] += *(LAS const f32x4*)(tb + KG_TAB + i * 1024 + lane * 16);
      kgen_finish(d, lds + CVB + sv * R_BYTES, &ctrl[5], tb, lane);
      if (lane < 56) *(LAS u32x4*)(lds + CVB + CV_U + sv * CV_UCH + (lane < 28 ? lane * 16 : CV_U1 + (lane - 28) * 16)) = (u32x4){0u, 0u, 0u, 0u};
      STAMP(4);
      __builtin_amdgcn_s_setprio(3);
    } else {
      unsigned vo[9];
      slice_offsets(vo, lane);
      const char* wsrc = (const char*)Wq + hg * 144 - 4096;
      lds_wait(&ctrl[3], XLEAD);
      STAMP(1);
      __builtin_amdgcn_s_setprio(3);
#pragma unroll 1
      for (int a = 0; a < 9; ++a) {
        if (a >= 3 && (a & 1)) lds_wait(&ctrl[12 + (a >> 1)], 2u);
        glds9(vo, wsrc + a * 64 * ROWB, lds0 + A_W + a * 9216);
        if (a >= 2) { VMWAIT(18); LSET(4, (unsigned)(a - 1)); }
      }
      STAMP(3);
      VMWAIT(9); LSET(4, 8u);
      VMWAIT(0); LSET(4, 9u);
      STAMP(4);
      const unsigned tg = tag;
      const char* fp[4]; u32x4 fl[4], fu;
#pragma unroll
      for (int i = 0; i < 4; ++i) fp[i] = ws + WS_F + (((rot + 2 * i + h) & 7) * 32 + r32) * 128;
      const char* fup = ws + WS_F + (r32 * 8 + (h ? 7 : b / 36)) * 128;
      asm volatile("global_load_dwordx4 %0, %1, off sc1\n\ts_waitcnt vmcnt(0)" : "=&v"(fl[0]) : "v"(fp[0]) : "memory");
#pragma unroll 1
      for (int spin = 0; spin < SPIN_MAX; ++spin) {
        if (__all(h == 1 || fl[0][0] == tg)) break;
        __builtin_amdgcn_s_sleep(POLL0);
        if (h == 0 && fl[0][0] != tg) asm volatile("global_load_dwordx4 %0, %1, off sc1\n\ts_waitcnt vmcnt(0)" : "+v"(fl[0]) : "v"(fp[0]) : "memory");
      }
      STAMP(5);
      lds_wait(&ctrl[12], 2u);
#pragma unroll 1
      for (int i = 0; i < 5; ++i) kv_group(ws, lds0, 0, rot, i, l16);
      LD16(fu, fup, " sc1");
      VMWAIT(1);
      LSET(7, 1u);
      STAMP(7);
      VMWAIT(0); PIN(fu);
      flag_wait<0>(fp[0], tg, fl[0]);
      STAMP(9);
#pragma unroll 1
      for (int i = 0; i < 4; ++i) kv_group(ws, lds0, 1, rot, i, l16);
      flag_wait<2>(fup, tg, fu);
      lds_wait(&ctrl[5], 8u);
#pragma unroll 1
      for (int i = 0; i < 6; ++i)
        glds4c(l16, ws + WS_UT + (size_t)(i < 4 ? 2 * b + (i >> 1) : ce) * UT_ROW + 128 - ((i & 1) ? 0 : 2), lds0 + CVB + CV_U + (i >> 1) * CV_UCH + (i & 1) * CV_U1 + U_PADB);
      lds_wait(&ctrl[6], 8u);
      kv_group(ws, lds0, 1, rot, 4, l16);
      asm volatile("global_load_dwordx4 %0, %3, off sc1\n\tglobal_load_dwordx4 %1, %4, off sc1\n\tglobal_load_dwordx4 %2, %5, off sc1\n\ts_waitcnt vmcnt(0)"
                   : "=&v"(fl[1]), "=&v"(fl[2]), "=&v"(fl[3]) : "v"(fp[1]), "v"(fp[2]), "v"(fp[3]) : "memory");
#pragma unroll
      for (int i = 1; i < 4; ++i) flag_wait<0>(fp[i], tg, fl[i]);
      STAMP(10);
      {
        unsigned m = umax(umax(fl[0][1], fl[1][1]), umax(fl[2][1], fl[3][1]));
        m = umax(m, dppu<0x128>(m));
        { const auto t = __builtin_amdgcn_permlane16_swap(m, m, false, false); m = umax(t[0], t[1]); }
        { const auto t = __builtin_amdgcn_permlane32_swap(m, m, false, false); m = umax(t[0], t[1]); }
        const float k2 = __uint_as_float(__builtin_amdgcn_readlane(m, 0)) + __uint_as_float(__builtin_amdgcn_readlane(m, 1)) +
                         __uint_as_float(__builtin_amdgcn_readlane(m, 2)) + __uint_as_float(__builtin_amdgcn_readlane(m, 3));
        const unsigned vm = umax(umax(__builtin_amdgcn_readlane(m, 4), __builtin_amdgcn_readlane(m, 5)), umax(__builtin_amdgcn_readlane(m, 6), __builtin_amdgcn_readlane(m, 7)));
        const float vf = __uint_as_float(vm);
        if (lane == 0) bctl[8] = (vf <= 1048576.f && vf >= 9.5367431640625e-7f) ? __float_as_uint(k2) : 0x7fc00000u;
      }
    }
    const int rr = lane >> 3;
    const char* wo = (const char*)Wo + hg * 144;
    auto wo_groups = [&](int g0, int g1) {
      const int par = (g0 + sv) & 1;
      const unsigned wsw = (((lane & 7) ^ (2 * ((rr >> 1) & 1))) * 16) ^ (par * 64);
      const unsigned wv0 = rr * ROWB + wsw, wv1 = (18 * (rr >> 1) + 16 + (rr & 1)) * ROWB + wsw;
#pragma unroll 1
      for (int O = 9 * g0 + sv; O < 9 * g1; O += 4) {
        const int gq = O / 9, o = O - gq * 9;
        if (o < 8) gldss(wv0, wo + (gq * 72 + 18 * (o >> 1) + 8 * (o & 1)) * ROWB, lds0 + O * 1024);
        else gldss(wv1, wo + gq * 72 * ROWB, lds0 + O * 1024);
      }
    };
    auto wo_rem = [&]() {
#pragma unroll 1
      for (int a = sv; a < 9; a += 4) {
        const int O = 8 * a + rr, gq = O / 9, o = O - gq * 9, e8 = lane & 7;
        gldss((gq * 72 + (o < 8 ? 18 * (o >> 1) + 8 * (o & 1) + e8 : 18 * (e8 >> 1) + 16 + (e8 & 1))) * ROWB + 128, wo, lds0 + C_REM + a * 1024);
      }
    };
    const int rofs = CVB + (1023 - l15 + 4 * g) * 4, uofs = CVB + CV_U + (l15 & 1) * CV_U1 + U_PADB + (32 * (l15 >> 1) + 8 * g) * 2;
    f32x4 acc[8], ea = {0.f, 0.f, 0.f, 0.f}, eb = ea;
#pragma unroll
    for (int c = 0; c < 8; ++c) acc[c] = ea;
#pragma unroll 1
    for (int iv = 0; iv < 4; ++iv) {
      STAMP(16);
      BARVM(0);
      STAMP(24);
      if (iv < 2) { KVC(2 + 2 * iv, 4 + 2 * iv); }
      if (iv == 0) touch((const char*)Wo + ((b >> 3) * 162 + min(sv * 41 + lane, 161)) * 128, lds0 + B_CTRL - 256);
      if (iv >= 2) wo_groups(1 + iv, 2 * iv);
      if (iv == 2) {
        char* p = ws + WS_O + (qb * 4 + sv) * 18432 + 9 * hg * 256 + l16;
        st16c(p, (u32x4){SENT, SENT, SENT, SENT}); st16c(p + 1024, (u32x4){SENT, SENT, SENT, SENT});
        if (lane < 16) st16c(p + 2048, (u32x4){SENT, SENT, SENT, SENT});
      }
      {
        const int m0 = 4 * half + iv;
        const lptr rb = lds + chl * R_BYTES + rofs - 3584 - m0 * 64, ub = lds + chl * CV_UCH + uofs - m0 * 64;
        bf16x8 uf[8], kf[8];
#pragma unroll
        for (int z = 0; z < 8; ++z) {
          LAS const unsigned* rp = (LAS const unsigned*)(rb + 512 * (7 - z));
          kf[z] = __builtin_bit_cast(bf16x8, (u32x4){rp[0], rp[1], rp[2], rp[3]});
          uf[z] = *(LAS const bf16x8*)(ub + z * 512);
        }
        CFENCE();
#pragma unroll
        for (int c = 0; c < 8; ++c)
#pragma unroll
          for (int m1 = 0; m1 <= c; ++m1) acc[c] = MFMA16(uf[c - m1], kf[m1], acc[c]);
      }
      {
        const int m0 = 2 * sv + (iv >> 1), cB = 7 - cq;
        const lptr rb = lds + 2 * R_BYTES + rofs - m0 * 64, ub = lds + 2 * CV_UCH + uofs - m0 * 64;
        if ((iv & 1) == 0) {
#pragma unroll
          for (int i = 0; i < 5; ++i) {
            LAS const unsigned* rp = (LAS const unsigned*)(rb - 512 * i);
            eb = MFMA16(*(LAS const bf16x8*)(ub + 512 * (cB - i)), __builtin_bit_cast(bf16x8, (u32x4){rp[0], rp[1], rp[2], rp[3]}), eb);
          }
        } else {
          bf16x8 uf[4], kf[4];
#pragma unroll
          for (int i = 5; i < 9; ++i) {
            const bool isB = i <= cB;
            const int m1 = isB ? i : i - cB - 1, z = (isB ? cB : cq) - m1;
            LAS const unsigned* rp = (LAS const unsigned*)(rb - 512 * m1);
            kf[i - 5] = __builtin_bit_cast(bf16x8, (u32x4){rp[0], rp[1], rp[2], rp[3]});
            uf[i - 5] = *(LAS const bf16x8*)(ub + 512 * z);
          }
#pragma unroll
          for (int i = 5; i < 9; ++i) {
            if (i < 8 && i <= cB) eb = MFMA16(uf[i - 5], kf[i - 5], eb);
            else ea = MFMA16(uf[i - 5], kf[i - 5], ea);
          }
        }
      }
    }
    STAMP(6);
    unsigned us[8], ue[2];
    const int ec = chl ? 7 - cq : cq;
#pragma unroll
    for (int i = 0; i < 8; ++i) us[i] = *(LAS const unsigned*)(lds + CVB + CV_U + chl * CV_UCH + CV_U1 + U_PADB + (1024 * half + 256 * (i >> 1) + 64 * g + 32 * (i & 1) + 2 * l15) * 2);
#pragma unroll
    for (int i = 0; i < 2; ++i) ue[i] = *(LAS const unsigned*)(lds + CVB + CV_U + 2 * CV_UCH + CV_U1 + U_PADB + (256 * ec + 64 * g + 32 * i + 2 * l15) * 2);
    const lptr sc = lds + CV_SCR + l16;
    if (half) {
#pragma unroll
      for (int i = 0; i < 4; ++i) { *(LAS f32x4*)(sc + (sv * 4 + i) * 1024) = acc[i]; acc[i] = acc[4 + i]; }
    } else {
#pragma unroll
      for (int i = 0; i < 4; ++i) *(LAS f32x4*)(sc + (sv * 4 + i) * 1024) = acc[4 + i];
    }
    if (sv != 0) *(LAS f32x4*)(sc + 16384 + (sv - 1) * 1024) = ea;
    if (sv != 2) *(LAS f32x4*)(sc + 19456 + (sv > 2 ? 2 : sv) * 1024) = eb;
    STAMP(20);
    BAR();
    STAMP(27);
    KVC(6, 8);
#define FIN(x, y, uu, dd) pk2(rbf(x) + rbf(bflo(uu) * dd), rbf(y) + rbf(bfhi(uu) * dd))
#pragma unroll
    for (int pr = 0; pr < 2; ++pr) {
      const f32x4 f0 = acc[2 * pr] + *(LAS const f32x4*)(sc + ((sv ^ 1) * 4 + 2 * pr) * 1024), f1 = acc[2 * pr + 1] + *(LAS const f32x4*)(sc + ((sv ^ 1) * 4 + 2 * pr + 1) * 1024);
      unsigned a0 = FIN(f0[0], f0[1], us[4 * pr], dsk), a1 = FIN(f0[2], f0[3], us[4 * pr + 1], dsk), a2 = FIN(f1[0], f1[1], us[4 * pr + 2], dsk), a3 = FIN(f1[2], f1[3], us[4 * pr + 3], dsk);
      const bool o1b = lane & 1, o2b = lane & 2;
      unsigned r;
      r = dppu<0xB1>(o1b ? a0 : a1); if (o1b) a0 = r; else a1 = r;
      r = dppu<0xB1>(o1b ? a2 : a3); if (o1b) a2 = r; else a3 = r;
      r = dppu<0x4E>(o2b ? a0 : a2); if (o2b) a0 = r; else a2 = r;
      r = dppu<0x4E>(o2b ? a1 : a3); if (o2b) a1 = r; else a3 = r;
      st16c(ws + WS_YT + (2 * b + chl) * 4096 + (4 * half + 2 * pr + (p4 >> 1)) * 512 + g * 128 + (p4 & 1) * 64 + q4 * 16, (u32x4){a0, a1, a2, a3});
    }
    if (half == 0) {
      f32x4 e = chl ? eb : ea;
#pragma unroll
      for (int s = 0; s < 3; ++s) e += *(LAS const f32x4*)(sc + 16384 + chl * 3072 + s * 1024);
      char* p = ws + WS_YT + ce * 4096 + ec * 512 + g * 128 + l15 * 4;
      st4c(p, FIN(e[0], e[1], ue[0], dse)); st4c(p + 64, FIN(e[2], e[3], ue[1], dse));
    }
    STAMP(21);
    BARVM(0);
    STAMP(28);
    if (wid == 8 && lane == 0) st4c(myflag + 12, tag);
    wo_groups(6, 8); wo_groups(0, 1);
    const int tq = (qb >> 2) < 4 ? (qb >> 2) : 7 - (qb >> 2);
    const int pb0 = min(36 * hg + min(lane, 35), 255), pb1 = b == 0 ? sv * 64 + lane : 4 * lane + tq;
    const char* f2a = ws + WS_F + (lane >= 56 ? qb * 8 + (lane & 7) : ((pb0 & 7) * 4 + (pb0 >> 6)) * 8 + ((pb0 >> 3) & 7)) * 128;
    const char* f2b = ws + WS_F + (((pb1 & 7) * 4 + (pb1 >> 6)) * 8 + ((pb1 >> 3) & 7)) * 128;
    u32x4 fa, fb = {tag, tag, tag, tag};
    LD16(fa, f2a, " sc1");
    if (hg == 7 || b == 0) asm volatile("global_load_dwordx4 %0, %1, off sc1" : "+v"(fb) : "v"(f2b) : "memory");
    STAMP(22);
    BAR();
    STAMP(29);
    VMWAIT(0);
    PIN(fa); PIN(fb);
    flag_wait<3>(f2a, tag, fa); flag_wait<3>(f2b, tag, fb);
    {
      const char* yp = ws + WS_YT + hg * 72 * 4096 + qb * 128 + sv * 32;
      const unsigned yv = (lane >> 1) * 4096 + (lane & 1) * 16;
      gldssc(yv, yp, lds0 + C_YT + sv * 2304); gldssc(yv, yp + 32 * 4096, lds0 + C_YT + sv * 2304 + 1024);
      if (lane < 16) gldssc(yv, yp + 64 * 4096, lds0 + C_YT + sv * 2304 + 2048);
    }
    wo_groups(1, 3); wo_rem();
    STAMP(23);
    BARVM(0);
    STAMP(12);
    if (b == 0 && wid == 8 && lane == 0) st4c(ws + WS_E, tag);
#pragma unroll
    for (int n = 0; n < 3; ++n) {
      const int rit = min(16 * n + 4 * g + p4, 35), wit = 16 * n + (lane >> 2);
      const lptr rp = lds + C_YT + sv * 2304 + ((rit % 9) * 8 + q4) * 32 + (rit / 9) * 8;
      const u32x4 v = __builtin_bit_cast(u32x4, trd2(rp, rp + 128));
      if (wit < 36) *(u32x4*)(out + (size_t)(qb * 64 + sv * 16 + (wit / 9) * 4 + (lane & 3)) * OUTLD + hg * 72 + (wit % 9) * 8) = v;
    }
    __builtin_amdgcn_s_setprio(0);
    STAMP(13);
    BAR();
    if (any_bad()) { BAR(); KVC(0, 8); BARVM(0); BAR(); BAR(); wo_groups(0, 8); wo_rem(); BARVM(0); }
    load_wf();
    __builtin_amdgcn_s_sleep(OSLEEP);
  }
  {
    const lptr wr = lds + C_REM + kt * 3072 + (g * 8 + q4) * 16 + (p4 & 1) * 8;
    STAMP(14);
    const char* op = ws + WS_O + (size_t)((qb * 4 + mt) * 18 + kt * 6) * 1024 + lane * 16;
    u32x4 of[6];
    const char* op2 = op + 4096;
    asm volatile("global_load_dwordx4 %0, %6, off sc1\n\tglobal_load_dwordx4 %1, %6, off offset:1024 sc1\n\t"
                 "global_load_dwordx4 %2, %6, off offset:2048 sc1\n\tglobal_load_dwordx4 %3, %6, off offset:3072 sc1\n\t"
                 "global_load_dwordx4 %4, %7, off sc1\n\tglobal_load_dwordx4 %5, %7, off offset:1024 sc1\n\ts_waitcnt vmcnt(0)"
                 : "=&v"(of[0]), "=&v"(of[1]), "=&v"(of[2]), "=&v"(of[3]), "=&v"(of[4]), "=&v"(of[5]) : "v"(op), "v"(op2) : "memory");
#pragma unroll 1
    for (int spin = 0; spin < SPIN_MAX; ++spin) {
      bool miss = false;
#pragma unroll
      for (int kk = 0; kk < 6; ++kk) {
        if (umax(umax(of[kk][0], of[kk][1]), umax(of[kk][2], of[kk][3])) == SENT) {
          const char* p = op + kk * 1024;
          asm volatile("global_load_dwordx4 %0, %1, off sc1" : "+v"(of[kk]) : "v"(p) : "memory");
          miss = true;
        }
      }
      if (!__any(miss)) break;
      __builtin_amdgcn_s_sleep(ORETRY);
      asm volatile("s_waitcnt vmcnt(0)" : "+v"(of[0]), "+v"(of[1]), "+v"(of[2]), "+v"(of[3]), "+v"(of[4]), "+v"(of[5]) :: "memory");
    }
    STAMP(15);
    f32x4 acc[5];
#pragma unroll
    for (int nt = 0; nt < 5; ++nt) acc[nt] = (f32x4){0.f, 0.f, 0.f, 0.f};
#pragma unroll
    for (int kk = 0; kk < 6; ++kk) {
#pragma unroll
      for (int nt = 0; nt < 4; ++nt) acc[nt] = MFMA16(wf[kk][nt], __builtin_bit_cast(bf16x8, of[kk]), acc[nt]);
      acc[4] = MFMA16(trd2(wr + kk * 512, wr + kk * 512 + 64), __builtin_bit_cast(bf16x8, of[kk]), acc[4]);
    }
    const lptr rd = lds + C_RED2 + mt * 5120 + lane * 16, rd2 = lds + C_RED + mt * 5120 + lane * 16;
#pragma unroll
    for (int nt = 0; nt < 5; ++nt) {
      const int own = nt >> 1;
      if (kt != own) *(LAS f32x4*)(((kt + 1) % 3 == own ? rd : rd2) + nt * 1024) = acc[nt];
    }
    BAR();
    u16* cp = out + (size_t)(qb * 64 + mt * 16 + l15) * OUTLD + DM + hg * 72 + g * 4 + kt * 32;
    const f32x4 a0 = kt == 0 ? acc[0] : (kt == 1 ? acc[2] : acc[4]), a1 = kt == 0 ? acc[1] : acc[3];
    const f32x4 v = a0 + *(LAS const f32x4*)(rd + kt * 2048) + *(LAS const f32x4*)(rd2 + kt * 2048);
    if (kt < 2 || g < 2) *(u32x2*)cp = (u32x2){pk2(v[0], v[1]), pk2(v[2], v[3])};
    if (kt < 2) {
      const f32x4 w = a1 + *(LAS const f32x4*)(rd + kt * 2048 + 1024) + *(LAS const f32x4*)(rd2 + kt * 2048 + 1024);
      *(u32x2*)(cp + 16) = (u32x2){pk2(w[0], w[1]), pk2(w[2], w[3])};
    }
  }
}

extern "C" void kernel_launch(void* const* d_in, const int* in_sizes, int n_in, void* d_out, int out_size, void* d_ws, size_t ws_size,
                              hipStream_t stream) {
  fused_kernel<<<256, 768, 0, stream>>>((const u16*)d_in[0], (const u16*)d_in[1], (const u16*)d_in[2], (const u16*)d_in[3], (const u16*)d_in[4],
                                        (const u16*)d_in[5], (const u16*)d_in[6], (const u16*)d_in[7], (const u16*)d_in[8], (const u16*)d_in[9],
                                        (const u16*)d_in[10], (const u16*)d_in[11], (const u16*)d_in[12], (const u16*)d_in[13], (u16*)d_out,
                                        (char*)d_ws);
}
```

```cpp
#include <hip/hip_runtime.h>
#include <hip/hip_bf16.h>
#include <stdint.h>

typedef __bf16 bf16x8 __attribute__((ext_vector_type(8)));
typedef __bf16 bf16x16 __attribute__((ext_vector_type(16)));
typedef __bf16 bf16x2 __attribute__((ext_vector_type(2)));
typedef short s16x4 __attribute__((ext_vector_type(4)));
typedef short s16x8 __attribute__((ext_vector_type(8)));
typedef float f32x2 __attribute__((ext_vector_type(2)));
typedef float f32x4 __attribute__((ext_vector_type(4)));
typedef float f32x8 __attribute__((ext_vector_type(8)));
typedef float f32x16 __attribute__((ext_vector_type(16)));
typedef float f32x32 __attribute__((ext_vector_type(32)));
typedef unsigned u32x2 __attribute__((ext_vector_type(2)));
typedef unsigned u32x4 __attribute__((ext_vector_type(4)));
typedef unsigned short u16;
#define LAS __attribute__((address_space(3)))
#define DI __device__ __forceinline__
typedef LAS char* lptr;

constexpr int SEQ = 2048, DM = 576, NS = 128, OUTLD = 2 * DM, ROWB = DM * 2;
constexpr int CH_BYTES = 19456, KV_BYTES = 8 * CH_BYTES, CH_TAIL = 8192, CH_V = 9216, V_OCT = 320;
constexpr int R_BYTES = 4160, CV_U = 13312, CV_UCH = 4640 + 4544, CV_U1 = 4640, U_PADB = 448, UT_ROW = 4224;
constexpr size_t WS_E = 0, WS_F = 4096, WS_KV = WS_F + 256 * 128;
constexpr size_t WS_UT = WS_KV + KV_BYTES;
constexpr size_t WS_YT = WS_UT + (size_t)DM * UT_ROW;
constexpr size_t WS_O = WS_YT + (size_t)DM * SEQ * 2;
constexpr size_t WS_DBG = 64u << 20;
constexpr int LDS_BYTES = 163840, KV_LDS = 0, SPARE = KV_BYTES, CVB = 6 * CH_BYTES;
constexpr int A_UT = 0, A_WKV = 9216, A_X0 = 18432, A_Q = 36864, Q_ROW = 144, A_CTRL = A_Q + 64 * Q_ROW, A_MISC = A_CTRL + 128, A_SCR = A_MISC + 144, A_W = 80896, B_CTRL = LDS_BYTES - 64;
constexpr int CV_SCR = 0, C_REM = 73728, C_YT = 82944, C_RED = C_YT + 9216, C_OX = C_RED + 20480, C_RED2 = C_OX + 24576;
static_assert(A_SCR + 6 * 5712 <= A_W - 256 && A_SCR % 16 == 0 && A_W + 82944 == LDS_BYTES && CVB + CV_U + 3 * CV_UCH <= B_CTRL - 256 && C_OX + 24576 <= 7 * CH_BYTES + 1024 &&
              C_RED2 + 20480 <= B_CTRL, "LDS map");
constexpr float QSCALE = 0.23570226039551584f * 1.4426950408889634f;
constexpr unsigned SENT = 0xffffffffu;
constexpr int SPIN_MAX = 1 << 16;
#define POLL1 12
#define POLL0 8
#define XLEAD 4u
#define OSLEEP 20
#define ORETRY 8

DI float bf2f(u16 x) { return __uint_as_float((unsigned)x << 16); }
DI float bflo(unsigned x) { return __uint_as_float(x << 16); }
DI float bfhi(unsigned x) { return __uint_as_float(x & 0xffff0000u); }
DI unsigned pk2(float lo, float hi) { f32x2 v = {lo, hi}; return __builtin_bit_cast(unsigned, __builtin_convertvector(v, bf16x2)); }
DI float rbf(float x) { return bfhi(pk2(x, x)); }
DI unsigned nosent(unsigned x) { return x < 0xfffffffeu ? x : 0xfffffffeu; }
#define GLDS(SC) "s_mov_b32 %0, m0\n\ts_mov_b32 m0, %2\n\ts_nop 0\n\tglobal_load_lds_dwordx4 %1, off" SC "\n\ts_mov_b32 m0, %0"
DI void glds16(const void* g, unsigned l) { unsigned sv; asm volatile(GLDS("") : "=&s"(sv) : "v"(g), "s"(l) : "memory"); }
DI void glds16c(const void* g, unsigned l) { unsigned sv; asm volatile(GLDS(" sc1") : "=&s"(sv) : "v"(g), "s"(l) : "memory"); }
#define GLDSS(SC) "s_mov_b32 %0, m0\n\ts_mov_b32 m0, %3\n\ts_nop 0\n\tglobal_load_lds_dwordx4 %1, %2" SC "\n\ts_mov_b32 m0, %0"
DI void gldss(unsigned voff, const void* sbase, unsigned l) { unsigned sv; asm volatile(GLDSS("") : "=&s"(sv) : "v"(voff), "s"(sbase), "s"(l) : "memory"); }
DI void gldssc(unsigned voff, const void* sbase, unsigned l) { unsigned sv; asm volatile(GLDSS(" sc1") : "=&s"(sv) : "v"(voff), "s"(sbase), "s"(l) : "memory"); }
DI void glds9(const unsigned (&vo)[9], const void* sbase, unsigned l) {
  unsigned sv;
  asm volatile("s_mov_b32 %0, m0\n\ts_mov_b32 m0, %11\n\ts_nop 0\n\t"
               "global_load_lds_dwordx4 %1, %10\n\tglobal_load_lds_dwordx4 %2, %10 offset:1024\n\t"
               "global_load_lds_dwordx4 %3, %10 offset:2048\n\tglobal_load_lds_dwordx4 %4, %10 offset:3072\n\t"
               "s_add_u32 m0, m0, 0x1000\n\ts_nop 0\n\t"
               "global_load_lds_dwordx4 %5, %10\n\tglobal_load_lds_dwordx4 %6, %10 offset:1024\n\t"
               "global_load_lds_dwordx4 %7, %10 offset:2048\n\tglobal_load_lds_dwordx4 %8, %10 offset:3072\n\t"
               "s_add_u32 m0, m0, 0x1000\n\ts_nop 0\n\tglobal_load_lds_dwordx4 %9, %10\n\ts_mov_b32 m0, %0"
               : "=&s"(sv) : "v"(vo[0]), "v"(vo[1]), "v"(vo[2]), "v"(vo[3]), "v"(vo[4]), "v"(vo[5]), "v"(vo[6]), "v"(vo[7]), "v"(vo[8]),
                 "s"(sbase), "s"(l) : "memory", "scc");
}
DI void glds4c(unsigned voff, const void* sbase, unsigned l) {
  unsigned sv;
  asm volatile("s_mov_b32 %0, m0\n\ts_mov_b32 m0, %3\n\ts_nop 0\n\t"
               "global_load_lds_dwordx4 %1, %2 sc1\n\tglobal_load_lds_dwordx4 %1, %2 offset:1024 sc1\n\t"
               "global_load_lds_dwordx4 %1, %2 offset:2048 sc1\n\tglobal_load_lds_dwordx4 %1, %2 offset:3072 sc1\n\ts_mov_b32 m0, %0"
               : "=&s"(sv) : "v"(voff), "s"(sbase), "s"(l) : "memory");
}
DI void touch(const void* g, unsigned junk) {
  unsigned sv;
  asm volatile("s_mov_b32 %0, m0\n\ts_mov_b32 m0, %2\n\ts_nop 0\n\tglobal_load_lds_dword %1, off sc1\n\ts_mov_b32 m0, %0" : "=&s"(sv) : "v"(g), "s"(junk) : "memory");
}
template <int W> DI void flag_wait(const char* p, unsigned tag, u32x4& v) {
#pragma unroll 1
  for (int spin = 0; spin < SPIN_MAX; ++spin) {
    if (__all(v[W] == tag)) break;
    __builtin_amdgcn_s_sleep(POLL1);
    if (v[W] != tag) asm volatile("global_load_dwordx4 %0, %1, off sc1\n\ts_waitcnt vmcnt(0)" : "+v"(v) : "v"(p) : "memory");
  }
}
DI void touchs(unsigned voff, const void* sbase, unsigned junk) {
  unsigned sv;
  asm volatile("s_mov_b32 %0, m0\n\ts_mov_b32 m0, %3\n\ts_nop 0\n\tglobal_load_lds_dword %1, %2 sc1\n\ts_mov_b32 m0, %0" : "=&s"(sv) : "v"(voff), "s"(sbase), "s"(junk) : "memory");
}
DI void slice_offsets(unsigned (&vo)[9], int lane) {
#pragma unroll
  for (int r = 0; r < 9; ++r) {
    const int c = r * 64 + lane, row = c / 9, src = (row & ~7) + 4 * (row & 1) + ((row >> 1) & 3);
    vo[r] = src * ROWB + (c - row * 9) * 16 + 4096 - (r & 3) * 1024;
  }
}
DI void kv_group(const char* ws, unsigned lds0, int slot, int rot, int grp, unsigned l16) {
  const int off = grp < 4 ? grp * 4096 : 15360;
  glds4c(l16, ws + WS_KV + ((slot + rot) & 7) * CH_BYTES + off, lds0 + KV_LDS + slot * CH_BYTES + off);
}
DI void lds_wait(LAS unsigned* p, unsigned n) {
  while (__hip_atomic_load(p, __ATOMIC_RELAXED, __HIP_MEMORY_SCOPE_WORKGROUP) < n) __builtin_amdgcn_s_sleep(1);
  asm volatile("" ::: "memory");
}
DI void st16c(void* p, u32x4 v) { asm volatile("global_store_dwordx4 %0, %1, off sc1\n\ts_nop 1" ::"v"(p), "v"(v) : "memory"); }
DI void st8c(void* p, u32x2 v) { asm volatile("global_store_dwordx2 %0, %1, off sc1" ::"v"(p), "v"(v) : "memory"); }
DI void st4c(void* p, unsigned v) { asm volatile("global_store_dword %0, %1, off sc1" ::"v"(p), "v"(v) : "memory"); }
#define LD4(d, p, SC) asm volatile("global_load_dword %0, %1, off" SC : "=v"(d) : "v"(p) : "memory")
#define LD8(d, p, SC) asm volatile("global_load_dwordx2 %0, %1, off" SC : "=v"(d) : "v"(p) : "memory")
#define LD16(d, p, SC) asm volatile("global_load_dwordx4 %0, %1, off" SC : "=v"(d) : "v"(p) : "memory")
#define PIN(x) asm volatile("" : "+v"(x))
#define VMWAIT(n) do { asm volatile("s_waitcnt vmcnt(" #n ")" ::: "memory"); __builtin_amdgcn_sched_barrier(0); } while (0)
#define BAR() asm volatile("s_waitcnt lgkmcnt(0)\n\ts_barrier" ::: "memory")
#define BARVM(n) asm volatile("s_waitcnt vmcnt(" #n ") lgkmcnt(0)\n\ts_barrier" ::: "memory")
#define CFENCE() asm volatile("" ::: "memory")
DI s16x4 trd(lptr p) { return __builtin_amdgcn_ds_read_tr16_b64_v4i16((LAS s16x4*)p); }
DI bf16x8 trd2(lptr a, lptr b) { return __builtin_bit_cast(bf16x8, __builtin_shufflevector(trd(a), trd(b), 0, 1, 2, 3, 4, 5, 6, 7)); }
#define MFMA16(a, b, c) __builtin_amdgcn_mfma_f32_16x16x32_bf16((a), (b), (c), 0, 0, 0)
#define MFMA32(a, b, c) __builtin_amdgcn_mfma_f32_32x32x16_bf16((a), (b), (c), 0, 0, 0)
#define SMF(a, b, c) __builtin_amdgcn_smfmac_f32_32x32x32_bf16((a), (b), (c), (int)0xdddddddd, 0, 0)
template <int CTRL> DI unsigned dppu(unsigned x) { return (unsigned)__builtin_amdgcn_update_dpp((int)x, (int)x, CTRL, 0xf, 0xf, false); }
DI unsigned umax(unsigned a, unsigned b) { return a > b ? a : b; }
DI unsigned max8(unsigned x) { x = umax(x, dppu<0xB1>(x)); x = umax(x, dppu<0x4E>(x)); return umax(x, dppu<0x141>(x)); }
DI unsigned max16(unsigned x) { x = max8(x); return umax(x, dppu<0x140>(x)); }

#define STAMP(i)

DI f32x2 cmul(f32x2 a, f32x2 b) {
  const f32x2 t = (f32x2){a[0], a[0]} * b;
  return __builtin_elementwise_fma((f32x2){a[1], a[1]}, (f32x2){-b[1], b[0]}, t);
}
DI f32x2 cpow(float zr, float zi, float c) {
  const float e = __builtin_amdgcn_exp2f(zr * c), th = __builtin_amdgcn_fractf(zi * c);
  return e * (f32x2){__builtin_amdgcn_cosf(th), __builtin_amdgcn_sinf(th)};
}
constexpr int KG_ROW = 272, KG_TAB = 21 * KG_ROW;
struct kg_in { unsigned are, aim, bre, bim, cre, cim, ldt; };
DI void kgen_half(const kg_in& in, lptr tb, int lane, f32x4 (&d)[2][2]) {
  const float dt = rbf(expf(bflo(in.ldt)));
  const float zr = (dt * bflo(in.are)) * 1.4426950408889634f, zi = (dt * bflo(in.aim)) * 0.15915494309189535f;
  const f32x2 dB = {dt * bflo(in.bre), dt * bflo(in.bim)}, Cc = {bflo(in.cre), bflo(in.cim)};
  lptr tw = tb + lane * 4;
#define PUT(row, v) do { const f32x2 v_ = (v); *(LAS unsigned*)(tw + (row) * KG_ROW) = pk2(v_[0], v_[1]); } while (0)
  const f32x2 w1 = cpow(zr, -zi, 1.f), w8 = cpow(zr, zi, 8.f), w256 = cpow(zr, -zi, 256.f);
  const f32x2 w2 = cmul(w1, w1), w3 = cmul(w2, w1), w4 = cmul(w2, w2), w6 = cmul(w2, w4);
  *(LAS unsigned*)tw = 0x3f80u;
  PUT(1, w1); PUT(2, w2); PUT(3, w3); PUT(4, w4); PUT(5, cmul(w1, w4)); PUT(6, w6); PUT(7, cmul(w3, w4));
  PUT(8, w256); PUT(9, cmul(w2, w256)); PUT(10, cmul(w4, w256)); PUT(11, cmul(w6, w256));
  const f32x2 w512 = cmul(w256, w256), w768 = cmul(w512, w256), w1024 = cmul(w768, w256);
  PUT(12, w512); PUT(13, cmul(w512, w4)); PUT(14, w768); PUT(15, cmul(w768, w4));
  PUT(16, w1024); PUT(17, cmul(w256, w1024)); PUT(18, cmul(w512, w1024)); PUT(19, cmul(w768, w1024)); PUT(20, cmul(w1024, w1024));
  CFENCE();
  const int l15 = lane & 15, g = lane >> 4;
  lptr fb = tb + l15 * KG_ROW + g * 16, fb1 = tb + (16 + min(l15, 4)) * KG_ROW + g * 16;
  bf16x8 a0[4], a1[4];
#pragma unroll
  for (int kk = 0; kk < 4; ++kk) { a0[kk] = *(LAS const bf16x8*)(fb + kk * 64); a1[kk] = *(LAS const bf16x8*)(fb1 + kk * 64); }
  CFENCE();
  const f32x2 w16 = cmul(w8, w8), w32 = cmul(w16, w16), w64 = cmul(w32, w32);
  f32x2 bb[8];
  bb[0] = cmul(Cc, dB); bb[1] = cmul(bb[0], w8); bb[2] = cmul(bb[0], w16); bb[3] = cmul(bb[1], w16);
#pragma unroll
  for (int i = 0; i < 4; ++i) bb[4 + i] = cmul(bb[i], w32);
#pragma unroll
  for (int ct = 0; ct < 2; ++ct) {
#pragma unroll
    for (int i = 0; i < 2; ++i) {
#pragma unroll
      for (int j = 0; j < 8; ++j) PUT(8 * i + j, bb[j]);
      if (ct == 0 || i == 0) {
#pragma unroll
        for (int j = 0; j < 8; ++j) bb[j] = cmul(bb[j], w64);
      }
    }
    CFENCE();
    bf16x8 bf[4];
#pragma unroll
    for (int kk = 0; kk < 4; ++kk) bf[kk] = *(LAS const bf16x8*)(fb + kk * 64);
    CFENCE();
    d[ct][0] = (f32x4){0.f, 0.f, 0.f, 0.f}; d[ct][1] = d[ct][0];
#pragma unroll
    for (int kk = 0; kk < 4; ++kk) { d[ct][0] = MFMA16(a0[kk], bf[kk], d[ct][0]); d[ct][1] = MFMA16(a1[kk], bf[kk], d[ct][1]); }
  }
}
DI void kgen_finish(const f32x4 (&d)[2][2], lptr img, LAS unsigned* gate, lptr tb, int lane) {
  const int l15 = lane & 15, g = lane >> 4;
  {
    const int e0 = g < 2 ? 8 * l15 + 4 * g : (g == 2 ? 256 + 4 * l15 : 384 + 2 * l15), es = g < 2 ? 128 : (g == 2 ? 64 : 32), e1 = g == 3 ? 64 : 2;
    const int f0 = g == 0 ? 512 + l15 : (lane == 16 ? 640 : 700);
#pragma unroll
    for (int ct = 0; ct < 2; ++ct) {
      const lptr p = tb + (e0 + ct * es) * 2;
      *(LAS unsigned*)p = pk2(d[ct][0][0], d[ct][0][1]); *(LAS unsigned*)(p + e1 * 2) = pk2(d[ct][0][2], d[ct][0][3]);
      LAS u16* q = (LAS u16*)tb + f0 + ct * 16;
      const unsigned x0 = pk2(d[ct][1][0], d[ct][1][1]), x1 = pk2(d[ct][1][2], d[ct][1][3]);
      q[0] = (u16)x0; q[32] = (u16)(x0 >> 16); q[64] = (u16)x1; q[96] = (u16)(x1 >> 16);
    }
  }
  lds_wait(gate, 8u);
  LAS const u16* C = (LAS const u16*)tb;
  const int rl = 63 - lane;
#pragma unroll
  for (int r = 0; r < 2; ++r) {
    const unsigned a = C[512 + 64 * (1 - r) + rl], bb = C[513 + 64 * (1 - r) + rl];
    const unsigned aa = a | (a << 16), b2x = bb | (bb << 16);
    *(LAS u32x4*)(img + r * 1024 + lane * 16) = (u32x4){b2x, (rl & 1) ? b2x : (bb | (a << 16)), aa, aa};
  }
  {
    const unsigned ab = *(LAS const unsigned*)(C + 384 + 2 * rl), c = C[386 + 2 * rl];
    *(LAS u32x4*)(img + 2048 + lane * 16) = (u32x4){c | (c << 16), __builtin_amdgcn_perm(ab, ab, 0x03020302u), __builtin_amdgcn_perm(ab, ab, 0x01000302u),
                                                __builtin_amdgcn_perm(ab, ab, 0x01000100u)};
  }
  {
    const bool hi = lane < 32;
    const int e = hi ? 128 + 4 * rl : 8 * rl;
    const u32x2 w01 = *(LAS const u32x2*)(C + e), w23 = *(LAS const u32x2*)(C + e + 4);
    const unsigned o0 = hi ? __builtin_amdgcn_perm(w01[1], w23[0], 0x07060100u) : __builtin_amdgcn_perm(w23[1], w23[1], 0x01000302u);
    const unsigned o1 = __builtin_amdgcn_perm(w23[0], w01[1], hi ? 0x01000100u : 0x05040706u);
    const unsigned o2 = __builtin_amdgcn_perm(w01[1], w01[0], hi ? 0x03020504u : 0x05040706u);
    const unsigned o3 = __builtin_amdgcn_perm(w01[0], w01[0], hi ? 0x01000100u : 0x01000302u);
    *(LAS u32x4*)(img + 3072 + lane * 16) = (u32x4){o0, o1, o2, o3};
  }
  if (lane < 4) *(LAS u32x4*)(img + 4096 + lane * 16) = (u32x4){0u, 0u, 0u, 0u};
}

template <int NT> DI void qgemm(lptr lds, LAS unsigned* ctrl, const bf16x8 (&xa)[18], int mt, int lane) {
  const int l15 = lane & 15, g = lane >> 4;
  constexpr int nt0 = NT == 2 ? 3 : 0;
  f32x4 acc[NT];
#pragma unroll
  for (int i = 0; i < NT; ++i) acc[i] = (f32x4){0.f, 0.f, 0.f, 0.f};
  const lptr wq = lds + A_W + (g * 8 + (l15 >> 2) * 2) * 144 + (l15 & 3) * 8 + nt0 * 32;
  constexpr int PD = NT == 3 ? 4 : 6;
  bf16x8 wf[PD + 1][NT];
  unsigned stage = 0;
#define WNEED(n) do { while (stage < (n)) { stage = __builtin_amdgcn_readfirstlane(__hip_atomic_load(&ctrl[4], __ATOMIC_RELAXED, __HIP_MEMORY_SCOPE_WORKGROUP)); \
                                            if (stage < (n)) __builtin_amdgcn_s_sleep(1); } CFENCE(); } while (0)
#pragma unroll
  for (int kk = 0; kk < 18 + PD; ++kk) {
    if (kk >= PD) {
#pragma unroll
      for (int i = 0; i < NT; ++i) acc[i] = MFMA16(wf[(kk - PD) % (PD + 1)][i], xa[kk - PD], acc[i]);
    }
    __builtin_amdgcn_sched_barrier(0);
    if (kk < 18) {
      if ((kk & 1) == 0) WNEED((unsigned)(kk / 2 + 1));
#pragma unroll
      for (int i = 0; i < NT; ++i) wf[kk % (PD + 1)][i] = trd2(wq + kk * 4608 + i * 32, wq + kk * 4608 + 144 + i * 32);
    }
    __builtin_amdgcn_sched_barrier(0);
  }
  LAS unsigned* qw = (LAS unsigned*)(lds + A_Q + (mt * 16 + l15) * Q_ROW + nt0 * 32 + g * 8);
#pragma unroll
  for (int i = 0; i < NT; ++i)
    if (nt0 + i < 4 || g < 2) *(LAS u32x2*)(qw + 8 * i) = (u32x2){pk2(acc[i][0] * QSCALE, acc[i][1] * QSCALE), pk2(acc[i][2] * QSCALE, acc[i][3] * QSCALE)};
  CFENCE();
  if (lane == 0) __hip_atomic_fetch_add(&ctrl[5], 1u, __ATOMIC_RELAXED, __HIP_MEMORY_SCOPE_WORKGROUP);
}

DI f32x32 qk_tile(lptr kp, const bf16x8& q0, const bf16x8& q1, unsigned kt, u32x2 qa) {
  const bf16x8 k = *(LAS const bf16x8*)kp;
  f32x16 z;
#pragma unroll
  for (int j = 0; j < 16; ++j) z[j] = 0.f;
  const f32x16 a = MFMA32(k, q0, z), b = MFMA32(k, q1, z);
  f32x32 s = __builtin_shufflevector(a, b, 0, 1, 2, 3, 4, 5, 6, 7, 8, 9, 10, 11, 12, 13, 14, 15, 16, 17, 18, 19, 20, 21, 22, 23, 24, 25, 26, 27, 28, 29, 30, 31);
  return __builtin_amdgcn_mfma_f32_32x32x4bf16_1k(__builtin_bit_cast(s16x4, (u32x2){kt, 0u}), __builtin_bit_cast(s16x4, qa), s, 0, 0, 0);
}
DI bf16x16 sub8(const f32x32& s, int o) {
  f32x8 t = {s[o], s[o + 1], s[o + 2], s[o + 3], s[o + 4], s[o + 5], s[o + 6], s[o + 7]};
  return __builtin_bit_cast(bf16x16, t);
}

__global__ __launch_bounds__(768, 3) void fused_kernel(const u16* __restrict__ U, const u16* __restrict__ Are, const u16* __restrict__ Aim,
                                                       const u16* __restrict__ Bre, const u16* __restrict__ Bim, const u16* __restrict__ Cre,
                                                       const u16* __restrict__ Cim, const u16* __restrict__ logdt, const u16* __restrict__ Dskip,
                                                       const u16* __restrict__ X, const u16* __restrict__ Wq, const u16* __restrict__ Wk,
                                                       const u16* __restrict__ Wv, const u16* __restrict__ Wo, u16* __restrict__ out,
                                                       char* __restrict__ ws) {
  __shared__ __attribute__((aligned(16))) char lds_raw[LDS_BYTES];
  const lptr lds = (lptr)lds_raw;
  const unsigned lds0 = (unsigned)(uintptr_t)lds_raw;
  STAMP(0);
  const int tid = threadIdx.x, lane = tid & 63, wid = __builtin_amdgcn_readfirstlane(tid >> 6);
  const int l15 = lane & 15, g = lane >> 4, r32 = lane & 31, h = lane >> 5, q4 = l15 >> 2, p4 = l15 & 3;
  const int b = blockIdx.x, qb = (b & 7) * 4 + (b >> 6), hg = (b >> 3) & 7;
  const int ce = 512 + (b >> 2), cq = b & 3, mt = wid & 3;
  char* const myflag = ws + WS_F + (qb * 8 + hg) * 128;
  LAS unsigned* ctrl = (LAS unsigned*)(lds + A_CTRL);
  LAS unsigned* bctl = (LAS unsigned*)(lds + B_CTRL);
#define LADD(i) do { if (lane == 0) __hip_atomic_fetch_add(&ctrl[i], 1u, __ATOMIC_RELAXED, __HIP_MEMORY_SCOPE_WORKGROUP); } while (0)
#define LSET(i, v) do { if (lane == 0) __hip_atomic_store(&ctrl[i], (v), __ATOMIC_RELAXED, __HIP_MEMORY_SCOPE_WORKGROUP); } while (0)
  asm volatile("" :: "s"(U), "s"(Are), "s"(Aim), "s"(Bre), "s"(Bim), "s"(Cre), "s"(Cim), "s"(logdt), "s"(Dskip), "s"(X), "s"(Wq), "s"(Wk), "s"(Wv), "s"(Wo),
               "s"(out), "s"(ws));
  const int xoff = mt == 0 ? A_X0 : A_W + 9216 + mt * 18432;
  const unsigned xv0 = (lane >> 3) * ROWB + (((lane & 7) ^ (lane >> 4)) * 16), xv1 = (8 + (lane >> 3)) * ROWB + (((lane & 7) ^ (4 + (lane >> 4))) * 16);
  const char* xs = (const char*)X + (size_t)(qb * 64 + mt * 16) * ROWB;
  const bool kgw = mt != (wid < 4 ? 0 : 3) && (wid < 4 || wid >= 8);
  const int kc = wid < 4 ? wid - 1 : wid - 8, kch = kc < 2 ? 2 * b + kc : ce;
  int msc = kgw ? A_SCR + (2 * kc + (wid < 4 ? 1 : 0)) * KG_TAB + 49 * 16 : A_MISC + (wid == 11 ? 80 : (wid == 0 ? 64 : mt * 16));
  asm volatile("" : "+v"(msc)); msc = __builtin_amdgcn_readfirstlane(msc);
  if (wid >= 4 && wid < 8) {
    __builtin_amdgcn_s_setprio(3);
#pragma unroll
    for (int kb = 0; kb < 2; ++kb) { gldss(xv0, xs + kb * 128, lds0 + xoff + kb * 2048); gldss(xv1, xs + kb * 128, lds0 + xoff + kb * 2048 + 1024); }
  }
  {
    const int a6 = lane >> 3;
    const u16* pa = a6 < 2 ? (a6 == 0 ? Are : Aim) : (a6 < 4 ? (a6 == 2 ? Bre : Bim) : (a6 == 4 ? Cre : Cim));
    pa += kch * NS + (wid < 4 ? 64 : 0) + (lane & 7) * 8;
    if (lane == 48) pa = logdt + (kch & ~7);
    if (lane == 49) pa = (const u16*)(ws + WS_E);
    if (lane >= 50) pa = Dskip + ((lane == 50 ? 2 * b : ce) & ~7);
    if (lane >= (kgw ? 0 : 49) && lane < (wid >= 8 ? 52 : 50)) glds16c(pa, lds0 + msc - 49 * 16);
  }
  if (wid < 4 || wid >= 8) {
    const int j = b >> 3;
    const char* tb0; unsigned tv0;
    if (wid == 0) { tb0 = (const char*)X + (size_t)qb * 64 * ROWB + hg * 72 * 128; tv0 = lane * 144; }
    else if (wid < 4) { tb0 = (const char*)Wq + (j * 162 + (wid - 1) * 54) * 128; tv0 = min(lane, 53) * 128; }
    else if (wid < 10) { tb0 = (const char*)U + (size_t)qb * 64 * ROWB + hg * 144 + (wid - 8) * 140; tv0 = lane * ROWB; }
    else if (wid == 10) { tb0 = ws; tv0 = 1024 + (lane & 3) * (2u << 20); }
    else { tb0 = (const char*)(hg < 4 ? Wk : Wv); tv0 = min(lane * 4 + (j >> 3), 161) * 128; }
    touchs(tv0, tb0, lds0 + A_W - 256);
  }
  if (tid < 32) ctrl[tid] = 0u;
  STAMP(17);
  BAR();
  STAMP(18);
#define TAG() (*(LAS const unsigned*)(lds + msc) * 2654435761u + 0x9e3779b9u)
  auto kg_read = [&](kg_in& in) {
    LAS const u16* p = (LAS const u16*)(lds + msc - 49 * 16) + lane;
    in.are = p[0]; in.aim = p[64]; in.bre = p[128]; in.bim = p[192]; in.cre = p[256]; in.cim = p[320];
    in.ldt = ((LAS const u16*)(lds + msc - 16))[kch & 7];
    CFENCE();
  };
#define FRESH_LANE() int tid_ = threadIdx.x; asm volatile("" : "+v"(tid_));                                                          \
  const int lane = tid_ & 63, l15 = lane & 15, g = lane >> 4, r32 = lane & 31, h = lane >> 5, q4 = l15 >> 2, p4 = l15 & 3;             \
  (void)r32; (void)h; (void)q4; (void)p4; (void)g
  auto any_bad = [&]() {
    const u32x4 a = *(LAS const u32x4*)bctl, c = *(LAS const u32x4*)(bctl + 4);
    return __builtin_amdgcn_readfirstlane((int)(a[0] | a[1] | a[2] | a[3] | c[0] | c[1] | c[2] | c[3]));
  };

  if (wid < 8) {
    {
      const lptr xb0 = lds + xoff + l15 * 128 + ((g ^ (l15 >> 1)) * 16), xb1 = lds + xoff + l15 * 128 + (((4 + g) ^ (l15 >> 1)) * 16);
      bf16x8 xa[18];
      if (wid >= 4) {
#pragma unroll
        for (int kb = 2; kb < 9; ++kb) {
          gldss(xv0, xs + kb * 128, lds0 + xoff + kb * 2048); gldss(xv1, xs + kb * 128, lds0 + xoff + kb * 2048 + 1024);
          if (kb == 6) LADD(3);
        }
        STAMP(1);
        char* cbp = ws + WS_KV + (qb >> 2) * CH_BYTES;
        const int key0 = (qb & 3) * 64 + mt * 16;
        char* sp;
        if (hg < 4) {
          const int key = key0 + l15;
          sp = g == 0 ? cbp + (key >> 5) * 1024 + (hg >> 1) * 512 + (key & 31) * 16 + (hg & 1) * 8 : cbp + CH_TAIL + (key & 63) * 16 + (key >> 6) * 4;
        } else {
          const int key = key0 + 4 * g, dim = 4 * (hg - 4) + l15;
          const int vrow = l15 < 4 ? ((dim & 3) | ((dim & 4) << 1) | ((dim & 8) >> 1)) : 12 + l15;
          sp = cbp + CH_V + (key >> 3) * V_OCT + (key & 4) * 2 + vrow * 16;
        }
        PIN(sp);
        lds_wait(&ctrl[0], 1u);
        const lptr wb = lds + A_WKV + (g * 8 + q4) * 16 + (l15 & 1) * 8;
        bf16x8 wk[18];
#pragma unroll
        for (int i = 0; i < 18; ++i) wk[i] = trd2(wb + i * 512, wb + i * 512 + 64);
        STAMP(2);
        f32x4 acc = {0.f, 0.f, 0.f, 0.f}, acc2 = acc;
#define XGRP(kb0, n, KTYPE)                                                                                                    \
        { VMWAIT(n);                                                                                                           \
          if ((n) == 0) LSET(8 + mt, 1u);                                                                                      \
          _Pragma("unroll") for (int i = 0; i < 6; ++i) xa[2 * (kb0) + i] = *(LAS const bf16x8*)(((i & 1) ? xb1 : xb0) + ((kb0) + (i >> 1)) * 2048); \
          CFENCE();                                                                                                            \
          if ((n) == 0) LADD(12 + mt);                                                                                         \
          _Pragma("unroll") for (int i = 2 * (kb0); i < 2 * (kb0) + 6; i += 2) {                                                \
            if (KTYPE) { acc = MFMA16(wk[i], xa[i], acc); acc2 = MFMA16(wk[i + 1], xa[i + 1], acc2); }                          \
            else { acc = MFMA16(xa[i], wk[i], acc); acc2 = MFMA16(xa[i + 1], wk[i + 1], acc2); } } }
        if (hg < 4) { XGRP(0, 12, 1) XGRP(3, 6, 1) XGRP(6, 0, 1) } else { XGRP(0, 12, 0) XGRP(3, 6, 0) XGRP(6, 0, 0) }
        STAMP(3);
        acc += acc2;
        u32x2 v = {pk2(acc[0], acc[1]), pk2(acc[2], acc[3])};
        unsigned mv;
        if (hg < 4) {
          if (g == 0) st8c(sp, v);
          if (g == 1 && hg == 3) st4c(sp, v[0]);
          float n2 = bflo(v[0]) * bflo(v[0]) + bfhi(v[0]) * bfhi(v[0]);
          if (g == 0) n2 += bflo(v[1]) * bflo(v[1]) + bfhi(v[1]) * bfhi(v[1]);
          const unsigned m16 = max16(__float_as_uint(n2) & 0x7fffffffu);
          const float mq = __uint_as_float(__builtin_amdgcn_readlane(m16, 0)), mtl = __uint_as_float(__builtin_amdgcn_readlane(m16, 16));
          mv = __float_as_uint(hg == 3 ? mq + mtl : mq) & 0x7fffffffu;
        } else {
          unsigned am = umax(umax(v[0] << 16 & 0x7fffffffu, v[0] & 0x7fff0000u), umax(v[1] << 16 & 0x7fffffffu, v[1] & 0x7fff0000u));
          if (l15 >= 6) am = 0u;
          if (l15 == 6) v = (u32x2){0x3f803f80u, 0x3f803f80u};
          if (l15 == 7) v = (u32x2){0u, 0u};
          if (l15 < 4 || (hg == 7 && l15 < 8)) st8c(sp, v);
          am = max16(am);
          mv = umax(umax(__builtin_amdgcn_readlane(am, 0), __builtin_amdgcn_readlane(am, 16)), umax(__builtin_amdgcn_readlane(am, 32), __builtin_amdgcn_readlane(am, 48)));
        }
        if (lane == 0) __hip_atomic_fetch_max(&ctrl[2], mv, __ATOMIC_RELAXED, __HIP_MEMORY_SCOPE_WORKGROUP);
        STAMP(4);
      } else {
        if (wid == 0) {
          __builtin_amdgcn_s_setprio(3);
          {
            const char* wx = (const char*)(hg < 4 ? Wk : Wv) + 8 * (hg & 3);
#pragma unroll 1
            for (int i = 0; i < 8; ++i) gldss(lane * 36, wx + i * 64 * 36, lds0 + A_WKV + i * 1024);
            if (lane < 63) gldss(lane * 36, wx + 512 * 36, lds0 + A_WKV + 8192);
            if (lane == 0) {
              unsigned sv_;
              asm volatile("s_mov_b32 %0, m0\n\ts_mov_b32 m0, %3\n\ts_nop 0\n\tglobal_load_lds_dwordx3 %1, %2\n\ts_mov_b32 m0, %0"
                           : "=&s"(sv_) : "v"(575 * 36), "s"(wx), "s"(lds0 + A_WKV + 575 * 16) : "memory");
            }
          }
          __builtin_amdgcn_s_setprio(2);
          unsigned vo[9];
          slice_offsets(vo, lane);
          VMWAIT(0);
          LSET(0, 1u);
          STAMP(1);
          glds9(vo, (const char*)U + (size_t)qb * 64 * ROWB + hg * 144 - 4096, lds0 + A_UT);
          const lptr rp = lds + A_UT + (((4 * g + p4) & 7) * 8 + 2 * q4) * 144 + (g >> 1) * 8;
          char* up = ws + WS_UT + (size_t)(hg * 72 + (lane >> 5) * 4 + (lane & 3)) * UT_ROW + 128 + qb * 128 + ((lane >> 2) & 7) * 16;
          VMWAIT(0);
#pragma unroll
          for (int i = 0; i < 9; ++i) st16c(up + i * 8 * UT_ROW, __builtin_bit_cast(u32x4, trd2(rp + i * 16, rp + i * 16 + 144)));
          if (qb == 0) {
            st16c(ws + WS_UT + (size_t)(hg * 72 + lane) * UT_ROW + 112, (u32x4){0u, 0u, 0u, 0u});
            st16c(ws + WS_UT + (size_t)(hg * 72 + 64 + (lane & 7)) * UT_ROW + 112, (u32x4){0u, 0u, 0u, 0u});
          }
          STAMP(2);
        } else {
          __builtin_amdgcn_s_setprio(2);
          VMWAIT(1);
          kg_in in; kg_read(in);
          STAMP(1);
          f32x4 d[2][2];
          const lptr tb = lds + A_SCR + (2 * wid - 1) * KG_TAB;
          kgen_half(in, tb, lane, d);
#pragma unroll
          for (int i = 0; i < 4; ++i) *(LAS f32x4*)(tb + i * 1024 + lane * 16) = d[i >> 1][i & 1];
          CFENCE();
          LSET(15 + wid, 1u);
          VMWAIT(0);
          STAMP(2);
        }
        lds_wait(&ctrl[8 + mt], 1u);
#pragma unroll
        for (int kk = 0; kk < 18; ++kk) xa[kk] = *(LAS const bf16x8*)(((kk & 1) ? xb1 : xb0) + (kk >> 1) * 2048);
        CFENCE();
        LADD(12 + mt);
        STAMP(3);
        STAMP(4);
      }
      if (wid >= 4) {
        VMWAIT(0);
        if (lane == 0 && __hip_atomic_fetch_add(&ctrl[1], 1u, __ATOMIC_RELAXED, __HIP_MEMORY_SCOPE_WORKGROUP) == 3u)
          st8c(myflag, (u32x2){TAG(), __hip_atomic_load(&ctrl[2], __ATOMIC_RELAXED, __HIP_MEMORY_SCOPE_WORKGROUP)});
        __builtin_amdgcn_s_setprio(2);
        STAMP(5);
        qgemm<2>(lds, ctrl, xa, mt, lane);
      } else {
        VMWAIT(0);
        if (wid == 0 && lane == 0) st4c(myflag + 8, TAG());
        STAMP(5);
        qgemm<3>(lds, ctrl, xa, mt, lane);
      }
    }
    STAMP(8);
    FRESH_LANE();
    const int kp = wid >> 2, head = wid & 3;
    f32x16 o0, o1;
#pragma unroll
    for (int j = 0; j < 16; ++j) { o0[j] = 0.f; o1[j] = 0.f; }
    const unsigned ka0 = lds0 + KV_LDS + kp * 1024 + lane * 16, ta0 = lds0 + KV_LDS + CH_TAIL + kp * 512 + r32 * 16,
                   va0 = lds0 + KV_LDS + CH_V + kp * 4 * V_OCT + h * V_OCT + (r32 < 19 ? r32 : 19) * 16;
    u32x4 q0, q1; unsigned qtl;
    lds_wait(&ctrl[5], 8u);
    {
      LAS const unsigned* qp = (LAS const unsigned*)(lds + A_Q + r32 * Q_ROW + head * 36 + h * 16);
#pragma unroll
      for (int j = 0; j < 4; ++j) { q0[j] = qp[j]; q1[j] = qp[32 * Q_ROW / 4 + j]; }
      qtl = *(LAS const unsigned*)(lds + A_Q + lane * Q_ROW + head * 36 + 32);
    }
    CFENCE();
    LADD(6);
    const u32x2 qa = {qtl, 0u}, qc = {0u, qtl};
    STAMP(9);
    lds_wait(&ctrl[7], 1u);
    STAMP(10);
    {
      int it = 8; unsigned ka = ka0, va = va0, ta = ta0;
      asm volatile(
      "ds_read_b128 v[128:131], %[ka]\n"
      "ds_read_b128 v[140:143], %[ta]\n"
      "v_mov_b32 v120, 0\n"
      "v_mov_b32 v121, 0\n"
      "v_mov_b32 v122, 0\n"
      "v_mov_b32 v123, 0\n"
      "v_mov_b32 v124, 0\n"
      "v_mov_b32 v125, 0\n"
      "v_mov_b32 v126, 0\n"
      "v_mov_b32 v127, 0\n"
      "v_mov_b32 v136, 0\n"
      "v_mov_b32 v137, 0\n"
      "v_mov_b32 v138, 0\n"
      "v_mov_b32 v139, 0\n"
      "s_waitcnt lgkmcnt(1)\n"
      "v_mfma_f32_32x32x16_bf16 v[64:79], v[128:131], %[q0], 0\n"
      "v_mfma_f32_32x32x16_bf16 v[80:95], v[128:131], %[q1], 0\n"
      "ds_read_b128 v[128:131], %[ka] offset:2048\n"
      "ds_read_b128 v[132:135], %[va]\n"
      "s_nop 7\n"
      "s_waitcnt lgkmcnt(2)\n"
      "v_mfma_f32_32x32x4_2b_bf16 v[64:95], v[140:141], %[qa], v[64:95]\n"
      "s_nop 15\n"
      "s_nop 1\n"
      "1:\n"
      "s_setprio 1\n"
      "s_waitcnt lgkmcnt(1)\n"
      "v_mfma_f32_32x32x16_bf16 v[96:111], v[128:131], %[q0], 0\n"
      "v_exp_f32 v64, v64\n"
      "v_exp_f32 v65, v65\n"
      "v_exp_f32 v66, v66\n"
      "v_smfmac_f32_32x32x32_bf16 %[o1], v[136:139], v[120:127], %[ix]\n"
      "ds_read_b128 v[136:139], %[va] offset:640\n"
      "v_exp_f32 v67, v67\n"
      "v_exp_f32 v68, v68\n"
      "v_exp_f32 v69, v69\n"
      "v_mfma_f32_32x32x16_bf16 v[112:127], v[128:131], %[q1], 0\n"
      "ds_read_b128 v[128:131], %[ka] offset:4096\n"
      "v_exp_f32 v70, v70\n"
      "v_exp_f32 v71, v71\n"
      "v_exp_f32 v72, v72\n"
      "v_exp_f32 v73, v73\n"
      "s_waitcnt lgkmcnt(2)\n"
      "v_smfmac_f32_32x32x32_bf16 %[o0], v[132:135], v[64:71], %[ix]\n"
      "v_exp_f32 v74, v74\n"
      "v_exp_f32 v75, v75\n"
      "v_exp_f32 v76, v76\n"
      "v_exp_f32 v77, v77\n"
      "v_exp_f32 v78, v78\n"
      "v_mfma_f32_32x32x4_2b_bf16 v[96:127], v[140:141], %[qc], v[96:127]\n"
      "v_exp_f32 v79, v79\n"
      "v_exp_f32 v80, v80\n"
      "v_exp_f32 v81, v81\n"
      "v_exp_f32 v82, v82\n"
      "v_exp_f32 v83, v83\n"
      "v_exp_f32 v84, v84\n"
      "v_exp_f32 v85, v85\n"
      "v_exp_f32 v86, v86\n"
      "s_waitcnt lgkmcnt(1)\n"
      "v_smfmac_f32_32x32x32_bf16 %[o0], v[136:139], v[72:79], %[ix]\n"
      "v_exp_f32 v87, v87\n"
      "v_exp_f32 v88, v88\n"
      "v_exp_f32 v89, v89\n"
      "v_exp_f32 v90, v90\n"
      "v_smfmac_f32_32x32x32_bf16 %[o1], v[132:135], v[80:87], %[ix]\n"
      "ds_read_b128 v[132:135], %[va] offset:2560\n"
      "v_exp_f32 v91, v91\n"
      "v_exp_f32 v92, v92\n"
      "v_exp_f32 v93, v93\n"
      "v_exp_f32 v94, v94\n"
      "v_exp_f32 v95, v95\n"
      "s_setprio 0\n"
      "s_waitcnt lgkmcnt(1)\n"
      "v_mfma_f32_32x32x16_bf16 v[64:79], v[128:131], %[q0], 0\n"
      "v_exp_f32 v96, v96\n"
      "v_exp_f32 v97, v97\n"
      "v_exp_f32 v98, v98\n"
      "v_smfmac_f32_32x32x32_bf16 %[o1], v[136:139], v[88:95], %[ix]\n"
      "ds_read_b128 v[136:139], %[va] offset:3200\n"
      "v_exp_f32 v99, v99\n"
      "v_exp_f32 v100, v100\n"
      "v_exp_f32 v101, v101\n"
      "v_mfma_f32_32x32x16_bf16 v[80:95], v[128:131], %[q1], 0\n"
      "ds_read_b128 v[128:131], %[ka] offset:6144\n"
      "v_exp_f32 v102, v102\n"
      "v_exp_f32 v103, v103\n"
      "v_exp_f32 v104, v104\n"
      "v_exp_f32 v105, v105\n"
      "s_waitcnt lgkmcnt(2)\n"
      "v_smfmac_f32_32x32x32_bf16 %[o0], v[132:135], v[96:103], %[ix]\n"
      "v_exp_f32 v106, v106\n"
      "v_exp_f32 v107, v107\n"
      "v_exp_f32 v108, v108\n"
      "v_exp_f32 v109, v109\n"
      "v_exp_f32 v110, v110\n"
      "v_mfma_f32_32x32x4_2b_bf16 v[64:95], v[142:143], %[qa], v[64:95]\n"
      "v_exp_f32 v111, v111\n"
      "v_exp_f32 v112, v112\n"
      "v_exp_f32 v113, v113\n"
      "v_exp_f32 v114, v114\n"
      "v_exp_f32 v115, v115\n"
      "v_exp_f32 v116, v116\n"
      "v_exp_f32 v117, v117\n"
      "v_exp_f32 v118, v118\n"
      "s_waitcnt lgkmcnt(1)\n"
      "v_smfmac_f32_32x32x32_bf16 %[o0], v[136:139], v[104:111], %[ix]\n"
      "v_exp_f32 v119, v119\n"
      "v_exp_f32 v120, v120\n"
      "v_exp_f32 v121, v121\n"
      "v_exp_f32 v122, v122\n"
      "v_smfmac_f32_32x32x32_bf16 %[o1], v[132:135], v[112:119], %[ix]\n"
      "ds_read_b128 v[132:135], %[va] offset:5120\n"
      "v_exp_f32 v123, v123\n"
      "v_exp_f32 v124, v124\n"
      "v_exp_f32 v125, v125\n"
      "v_exp_f32 v126, v126\n"
      "v_exp_f32 v127, v127\n"
      "s_setprio 3\n"
      "s_waitcnt lgkmcnt(1)\n"
      "v_mfma_f32_32x32x16_bf16 v[96:111], v[128:131], %[q0], 0\n"
      "v_exp_f32 v64, v64\n"
      "v_exp_f32 v65, v65\n"
      "v_exp_f32 v66, v66\n"
      "v_smfmac_f32_32x32x32_bf16 %[o1], v[136:139], v[120:127], %[ix]\n"
      "ds_read_b128 v[136:139], %[va] offset:5760\n"
      "v_exp_f32 v67, v67\n"
      "v_exp_f32 v68, v68\n"
      "v_exp_f32 v69, v69\n"
      "v_mfma_f32_32x32x16_bf16 v[112:127], v[128:131], %[q1], 0\n"
      "s_setprio 2\n"
      "s_waitcnt lgkmcnt(1)\n"
      "v_mfma_f32_32x32x16_bf16 v[64:79], v[128:131], %[q0], 0\n"
      "v_exp_f32 v96, v96\n"
      "v_exp_f32 v97, v97\n"
      "v_exp_f32 v98, v98\n"
      "v_smfmac_f32_32x32x32_bf16 %[o1], v[136:139], v[88:95], %[ix]\n"
      "ds_read_b128 v[136:139], %[va] offset:8320\n"
      "v_exp_f32 v99, v99\n"
      "v_exp_f32 v100, v100\n"
      "v_exp_f32 v101, v101\n"
      "v_mfma_f32_32x32x16_bf16 v[80:95], v[128:131], %[q1], 0\n"
      "ds_read_b128 v[128:131], %[ka] offset:21504\n"
      "v_exp_f32 v102, v102\n"
      "v_exp_f32 v103, v103\n"
      "v_exp_f32 v104, v104\n"
      "v_exp_f32 v105, v105\n"
      "s_waitcnt lgkmcnt(2)\n"
      "v_smfmac_f32_32x32x32_bf16 %[o0], v[132:135], v[96:103], %[ix]\n"
      "v_exp_f32 v106, v106\n"
      "v_exp_f32 v107, v107\n"
      "v_exp_f32 v108, v108\n"
      "v_exp_f32 v109, v109\n"
      "v_exp_f32 v110, v110\n"
      "v_mfma_f32_32x32x4_2b_bf16 v[64:95], v[140:141], %[qa], v[64:95]\n"
      "v_exp_f32 v111, v111\n"
      "v_exp_f32 v112, v112\n"
      "v_exp_f32 v113, v113\n"
      "v_exp_f32 v114, v114\n"
      "v_exp_f32 v115, v115\n"
      "v_exp_f32 v116, v116\n"
      "v_exp_f32 v117, v117\n"
      "v_exp_f32 v118, v118\n"
      "s_waitcnt lgkmcnt(1)\n"
      "v_smfmac_f32_32x32x32_bf16 %[o0], v[136:139], v[104:111], %[ix]\n"
      "v_exp_f32 v119, v119\n"
      "v_exp_f32 v120, v120\n"
      "v_exp_f32 v121, v121\n"
      "v_exp_f32 v122, v122\n"
      "v_smfmac_f32_32x32x32_bf16 %[o1], v[132:135], v[112:119], %[ix]\n"
      "ds_read_b128 v[132:135], %[va] offset:19456\n"
      "v_exp_f32 v123, v123\n"
      "v_exp_f32 v124, v124\n"
      "v_exp_f32 v125, v125\n"
      "v_exp_f32 v126, v126\n"
      "v_exp_f32 v127, v127\n"
      "v_add_u32 %[ka], 19456, %[ka]\n"
      "v_add_u32 %[va], 19456, %[va]\n"
      "v_add_u32 %[ta], 19456, %[ta]\n"
      "s_sub_u32 %[it], %[it], 1\n"
      "s_cmp_lg_u32 %[it], 0\n"
      "s_cbranch_scc1 1b\n"
      "v_smfmac_f32_32x32x32_bf16 %[o1], v[136:139], v[120:127], %[ix]\n"
      "s_setprio 0\n"
      "s_waitcnt lgkmcnt(0)\n"
      "s_nop 15\n"
          : [o0] "+v"(o0), [o1] "+v"(o1), [ka] "+v"(ka), [va] "+v"(va), [ta] "+v"(ta), [it] "+s"(it)
          : [q0] "v"(q0), [q1] "v"(q1), [qa] "v"(qa), [qc] "v"(qc), [ix] "v"(0xdddddddd)
          : "v64", "v65", "v66", "v67", "v68", "v69", "v70", "v71", "v72", "v73", "v74", "v75", "v76", "v77", "v78", "v79", "v80", "v81", "v82", "v83", "v84", "v85", "v86", "v87", "v88", "v89", "v90", "v91", "v92", "v93", "v94", "v95", "v96", "v97", "v98", "v99", "v100", "v101", "v102", "v103", "v104", "v105", "v106", "v107", "v108", "v109", "v110", "v111", "v112", "v113", "v114", "v115", "v116", "v117", "v118", "v119", "v120", "v121", "v122", "v123", "v124", "v125", "v126", "v127", "v128", "v129", "v130", "v131", "v132", "v133", "v134", "v135", "v136", "v137", "v138", "v139", "v140", "v141", "v142", "v143", "scc", "memory");
    }
    STAMP(13);
    const lptr xo = lds + C_OX + lane * 16;
    auto put_half = [&]() {
#pragma unroll
      for (int i = 0; i < 3; ++i) {
        const f32x4 a = {o0[4 * i], o0[4 * i + 1], o0[4 * i + 2], o0[4 * i + 3]}, c = {o1[4 * i], o1[4 * i + 1], o1[4 * i + 2], o1[4 * i + 3]};
        *(LAS f32x4*)(xo + (wid * 3 + i) * 1024) = kp ? a : c;
      }
    };
    put_half();
    {
#define DOT2(x, c) __builtin_amdgcn_fdot2_f32_bf16(__builtin_bit_cast(bf16x2, x), __builtin_bit_cast(bf16x2, x), (c), false)
      const float nt = DOT2(qtl, 0.f);
      float n0 = h ? 0.f : nt, n1 = h ? nt : 0.f;
#pragma unroll
      for (int j = 0; j < 4; ++j) { n0 = DOT2(q0[j], n0); n1 = DOT2(q1[j], n1); }
      const auto sw = __builtin_amdgcn_permlane32_swap(__float_as_uint(n0), __float_as_uint(n1), false, false);
      const float n2 = __uint_as_float(sw[0]) + __uint_as_float(sw[1]);
      const bool bad = __any(!(n2 * __uint_as_float(bctl[8]) <= 8000.f));
      if (lane == 0) bctl[wid] = bad ? 1u : 0u;
    }
    BAR();
    if (any_bad()) {
      BAR(); BAR();
      const bf16x8 qf0 = __builtin_bit_cast(bf16x8, q0), qf1 = __builtin_bit_cast(bf16x8, q1);
      float m0 = -INFINITY, m1 = -INFINITY;
#pragma unroll 1
      for (int i = 0; i < 32; ++i) {
        const int off = (i >> 2) * CH_BYTES;
        const f32x32 s = qk_tile((lptr)(uintptr_t)(ka0 + off + (i & 3) * 2048), qf0, qf1, *(LAS const unsigned*)(uintptr_t)(ta0 + off + (i & 3) * 4), qa);
#pragma unroll
        for (int j = 0; j < 16; ++j) { m0 = fmaxf(m0, s[j]); m1 = fmaxf(m1, s[16 + j]); }
      }
      m0 = fmaxf(m0, __shfl_xor(m0, 32)); m1 = fmaxf(m1, __shfl_xor(m1, 32));
      *(LAS f32x2*)(lds + SPARE + tid * 8) = (f32x2){m0, m1};
      BAR();
      const f32x2 mo = *(LAS const f32x2*)(lds + SPARE + (tid ^ 256) * 8);
      m0 = fmaxf(m0, mo[0]); m1 = fmaxf(m1, mo[1]);
#pragma unroll
      for (int j = 0; j < 16; ++j) { o0[j] = 0.f; o1[j] = 0.f; }
#pragma unroll 1
      for (int i = 0; i < 32; ++i) {
        const int off = (i >> 2) * CH_BYTES;
        f32x32 s = qk_tile((lptr)(uintptr_t)(ka0 + off + (i & 3) * 2048), qf0, qf1, *(LAS const unsigned*)(uintptr_t)(ta0 + off + (i & 3) * 4), qa);
#pragma unroll
        for (int j = 0; j < 16; ++j) { s[j] = __builtin_amdgcn_exp2f(s[j] - m0); s[16 + j] = __builtin_amdgcn_exp2f(s[16 + j] - m1); }
        const bf16x8 v0 = *(LAS const bf16x8*)(uintptr_t)(va0 + off + (i & 3) * 8 * V_OCT), v1 = *(LAS const bf16x8*)(uintptr_t)(va0 + off + ((i & 3) * 8 + 2) * V_OCT);
        o0 = SMF(v0, sub8(s, 0), o0); o0 = SMF(v1, sub8(s, 8), o0); o1 = SMF(v0, sub8(s, 16), o1); o1 = SMF(v1, sub8(s, 24), o1);
      }
      BAR();
      put_half();
      BAR();
    }
    float f[12];
#pragma unroll
    for (int i = 0; i < 3; ++i) {
      const f32x4 p = *(LAS const f32x4*)(xo + ((wid ^ 4) * 3 + i) * 1024);
#pragma unroll
      for (int j = 0; j < 4; ++j) f[4 * i + j] = (kp ? o1[4 * i + j] : o0[4 * i + j]) + p[j];
    }
    const auto sw = __builtin_amdgcn_permlane32_swap(__float_as_uint(f[10]), __float_as_uint(f[10]), false, false);
    const float inv = __builtin_amdgcn_rcpf(__uint_as_float(sw[0]));
    char* ob = ws + WS_O + (size_t)(qb * 4 + kp * 2 + (r32 >> 4)) * 18432 + l15 * 16;
    const int O = 9 * hg + 2 * head + h, Ot = 9 * hg + 8;
    st16c(ob + O * 256, (u32x4){nosent(pk2(f[0] * inv, f[1] * inv)), nosent(pk2(f[2] * inv, f[3] * inv)),
                                nosent(pk2(f[4] * inv, f[5] * inv)), nosent(pk2(f[6] * inv, f[7] * inv))});
    if (h == 0) st4c(ob + Ot * 256 + head * 4, nosent(pk2(f[8] * inv, f[9] * inv)));
    STAMP(11);
  } else {
    const int sv = wid - 8, chl = sv >> 1, half = sv & 1, rot = b & 7;
    const unsigned l16 = lane * 16;
    VMWAIT(1);
    const unsigned tag = __builtin_amdgcn_readfirstlane(TAG());
    const float dsk = bflo(__builtin_amdgcn_readfirstlane(((LAS const u16*)(lds + msc + 16))[(2 * b + chl) & 7])),
                dse = bflo(__builtin_amdgcn_readfirstlane(((LAS const u16*)(lds + msc + 32))[ce & 7]));
#define KVC(s0, s1) _Pragma("unroll 1") for (int i = 5 * (s0) + sv; i < 5 * (s1); i += 4) kv_group(ws, lds0, i / 5, rot, i % 5, l16)
    if (sv < 3) {
      __builtin_amdgcn_s_setprio(1);
      kg_in in; kg_read(in);
      STAMP(1);
      f32x4 d[2][2];
      const lptr tb = lds + A_SCR + 2 * sv * KG_TAB;
      kgen_half(in, tb, lane, d);
      VMWAIT(0);
      STAMP(2);
      lds_wait(&ctrl[16 + sv], 1u);
#pragma unroll
      for (int i = 0; i < 4; ++i) d[i >> 1][i & 1] += *(LAS const f32x4*)(tb + KG_TAB + i * 1024 + lane * 16);
      kgen_finish(d, lds + CVB + sv * R_BYTES, &ctrl[5], tb, lane);
      if (lane < 56) *(LAS u32x4*)(lds + CVB + CV_U + sv * CV_UCH + (lane < 28 ? lane * 16 : CV_U1 + (lane - 28) * 16)) = (u32x4){0u, 0u, 0u, 0u};
      STAMP(4);
      __builtin_amdgcn_s_setprio(3);
    } else {
      unsigned vo[9];
      slice_offsets(vo, lane);
      const char* wsrc = (const char*)Wq + hg * 144 - 4096;
      lds_wait(&ctrl[3], XLEAD);
      STAMP(1);
      __builtin_amdgcn_s_setprio(3);
#pragma unroll 1
      for (int a = 0; a < 9; ++a) {
        if (a >= 3 && (a & 1)) lds_wait(&ctrl[12 + (a >> 1)], 2u);
        glds9(vo, wsrc + a * 64 * ROWB, lds0 + A_W + a * 9216);
        if (a >= 2) { VMWAIT(18); LSET(4, (unsigned)(a - 1)); }
      }
      STAMP(3);
      VMWAIT(9); LSET(4, 8u);
      VMWAIT(0); LSET(4, 9u);
      STAMP(4);
      const unsigned tg = tag;
      const char* fp[4]; u32x4 fl0, fl1, fl2, fl3, fu;
#pragma unroll
      for (int i = 0; i < 4; ++i) fp[i] = ws + WS_F + (((rot + 2 * i + h) & 7) * 32 + r32) * 128;
      const char* fup = ws + WS_F + (r32 * 8 + (h ? 7 : b / 36)) * 128;
      asm volatile("global_load_dwordx4 %0, %1, off sc1\n\ts_waitcnt vmcnt(0)" : "=&v"(fl0) : "v"(fp[0]) : "memory");
#pragma unroll 1
      for (int spin = 0; spin < SPIN_MAX; ++spin) {
        if (__all(h == 1 || fl0[0] == tg)) break;
        __builtin_amdgcn_s_sleep(POLL0);
        if (h == 0 && fl0[0] != tg) asm volatile("global_load_dwordx4 %0, %1, off sc1\n\ts_waitcnt vmcnt(0)" : "+v"(fl0) : "v"(fp[0]) : "memory");
      }
      STAMP(5);
      lds_wait(&ctrl[12], 2u);
#pragma unroll 1
      for (int i = 0; i < 5; ++i) kv_group(ws, lds0, 0, rot, i, l16);
      LD16(fu, fup, " sc1");
      VMWAIT(1);
      LSET(7, 1u);
      STAMP(7);
      VMWAIT(0); PIN(fu);
      flag_wait<0>(fp[0], tg, fl0);
      STAMP(9);
#pragma unroll 1
      for (int i = 0; i < 4; ++i) kv_group(ws, lds0, 1, rot, i, l16);
      flag_wait<2>(fup, tg, fu);
      lds_wait(&ctrl[5], 8u);
#pragma unroll 1
      for (int i = 0; i < 6; ++i)
        glds4c(l16, ws + WS_UT + (size_t)(i < 4 ? 2 * b + (i >> 1) : ce) * UT_ROW + 128 - ((i & 1) ? 0 : 2), lds0 + CVB + CV_U + (i >> 1) * CV_UCH + (i & 1) * CV_U1 + U_PADB);
      lds_wait(&ctrl[6], 8u);
      kv_group(ws, lds0, 1, rot, 4, l16);
      asm volatile("global_load_dwordx4 %0, %3, off sc1\n\tglobal_load_dwordx4 %1, %4, off sc1\n\tglobal_load_dwordx4 %2, %5, off sc1\n\ts_waitcnt vmcnt(0)"
                   : "=&v"(fl1), "=&v"(fl2), "=&v"(fl3) : "v"(fp[1]), "v"(fp[2]), "v"(fp[3]) : "memory");
      flag_wait<0>(fp[1], tg, fl1); flag_wait<0>(fp[2], tg, fl2); flag_wait<0>(fp[3], tg, fl3);
      STAMP(10);
      {
        unsigned m = umax(umax(fl0[1], fl1[1]), umax(fl2[1], fl3[1]));
        m = umax(m, dppu<0x128>(m));
        { const auto t = __builtin_amdgcn_permlane16_swap(m, m, false, false); m = umax(t[0], t[1]); }
        { const auto t = __builtin_amdgcn_permlane32_swap(m, m, false, false); m = umax(t[0], t[1]); }
        const float k2 = __uint_as_float(__builtin_amdgcn_readlane(m, 0)) + __uint_as_float(__builtin_amdgcn_readlane(m, 1)) +
                         __uint_as_float(__builtin_amdgcn_readlane(m, 2)) + __uint_as_float(__builtin_amdgcn_readlane(m, 3));
        const unsigned vm = umax(umax(__builtin_amdgcn_readlane(m, 4), __builtin_amdgcn_readlane(m, 5)), umax(__builtin_amdgcn_readlane(m, 6), __builtin_amdgcn_readlane(m, 7)));
        const float vf = __uint_as_float(vm);
        if (lane == 0) bctl[8] = (vf <= 1048576.f && vf >= 9.5367431640625e-7f) ? __float_as_uint(k2) : 0x7fc00000u;
      }
    }
    const int rr = lane >> 3;
    const char* wo = (const char*)Wo + hg * 144;
    auto wo_groups = [&](int g0, int g1) {
      const int par = (g0 + sv) & 1;
      const unsigned wsw = (((lane & 7) ^ (2 * ((rr >> 1) & 1))) * 16) ^ (par * 64);
      const unsigned wv0 = rr * ROWB + wsw, wv1 = (18 * (rr >> 1) + 16 + (rr & 1)) * ROWB + wsw;
#pragma unroll 1
      for (int O = 9 * g0 + sv; O < 9 * g1; O += 4) {
        const int gq = O / 9, o = O - gq * 9;
        if (o < 8) gldss(wv0, wo + (gq * 72 + 18 * (o >> 1) + 8 * (o & 1)) * ROWB, lds0 + O * 1024);
        else gldss(wv1, wo + gq * 72 * ROWB, lds0 + O * 1024);
      }
    };
    auto wo_rem = [&]() {
#pragma unroll 1
      for (int a = sv; a < 9; a += 4) {
        const int O = 8 * a + rr, gq = O / 9, o = O - gq * 9, e8 = lane & 7;
        gldss((gq * 72 + (o < 8 ? 18 * (o >> 1) + 8 * (o & 1) + e8 : 18 * (e8 >> 1) + 16 + (e8 & 1))) * ROWB + 128, wo, lds0 + C_REM + a * 1024);
      }
    };
    const int rofs = CVB + (1023 - l15 + 4 * g) * 4, uofs = CVB + CV_U + (l15 & 1) * CV_U1 + U_PADB + (32 * (l15 >> 1) + 8 * g) * 2;
    f32x4 acc[8], ea = {0.f, 0.f, 0.f, 0.f}, eb = ea;
#pragma unroll
    for (int c = 0; c < 8; ++c) acc[c] = ea;
#pragma unroll 1
    for (int iv = 0; iv < 4; ++iv) {
      STAMP(16);
      BARVM(0);
      STAMP(24);
      if (iv < 2) { KVC(2 + 2 * iv, 4 + 2 * iv); }
      if (iv == 0) touch((const char*)Wo + ((b >> 3) * 162 + min(sv * 41 + lane, 161)) * 128, lds0 + B_CTRL - 256);
      if (iv >= 2) wo_groups(1 + iv, 2 * iv);
      if (iv == 2) {
        char* p = ws + WS_O + (qb * 4 + sv) * 18432 + 9 * hg * 256 + l16;
        st16c(p, (u32x4){SENT, SENT, SENT, SENT}); st16c(p + 1024, (u32x4){SENT, SENT, SENT, SENT});
        if (lane < 16) st16c(p + 2048, (u32x4){SENT, SENT, SENT, SENT});
      }
      {
        const int m0 = 4 * half + iv;
        const lptr rb = lds + chl * R_BYTES + rofs - 3584 - m0 * 64, ub = lds + chl * CV_UCH + uofs - m0 * 64;
        bf16x8 uf[8], kf[8];
#pragma unroll
        for (int z = 0; z < 8; ++z) {
          LAS const unsigned* rp = (LAS const unsigned*)(rb + 512 * (7 - z));
          kf[z] = __builtin_bit_cast(bf16x8, (u32x4){rp[0], rp[1], rp[2], rp[3]});
          uf[z] = *(LAS const bf16x8*)(ub + z * 512);
        }
        CFENCE();
#pragma unroll
        for (int c = 0; c < 8; ++c)
#pragma unroll
          for (int m1 = 0; m1 <= c; ++m1) acc[c] = MFMA16(uf[c - m1], kf[m1], acc[c]);
      }
      {
        const int m0 = 2 * sv + (iv >> 1), cB = 7 - cq;
        const lptr rb = lds + 2 * R_BYTES + rofs - m0 * 64, ub = lds + 2 * CV_UCH + uofs - m0 * 64;
        if ((iv & 1) == 0) {
#pragma unroll
          for (int i = 0; i < 5; ++i) {
            LAS const unsigned* rp = (LAS const unsigned*)(rb - 512 * i);
            eb = MFMA16(*(LAS const bf16x8*)(ub + 512 * (cB - i)), __builtin_bit_cast(bf16x8, (u32x4){rp[0], rp[1], rp[2], rp[3]}), eb);
          }
        } else {
          bf16x8 uf[4], kf[4];
#pragma unroll
          for (int i = 5; i < 9; ++i) {
            const bool isB = i <= cB;
            const int m1 = isB ? i : i - cB - 1, z = (isB ? cB : cq) - m1;
            LAS const unsigned* rp = (LAS const unsigned*)(rb - 512 * m1);
            kf[i - 5] = __builtin_bit_cast(bf16x8, (u32x4){rp[0], rp[1], rp[2], rp[3]});
            uf[i - 5] = *(LAS const bf16x8*)(ub + 512 * z);
          }
#pragma unroll
          for (int i = 5; i < 9; ++i) {
            if (i < 8 && i <= cB) eb = MFMA16(uf[i - 5], kf[i - 5], eb);
            else ea = MFMA16(uf[i - 5], kf[i - 5], ea);
          }
        }
      }
    }
    STAMP(6);
    unsigned us[8], ue[2];
    const int ec = chl ? 7 - cq : cq;
#pragma unroll
    for (int i = 0; i < 8; ++i) us[i] = *(LAS const unsigned*)(lds + CVB + CV_U + chl * CV_UCH + CV_U1 + U_PADB + (1024 * half + 256 * (i >> 1) + 64 * g + 32 * (i & 1) + 2 * l15) * 2);
#pragma unroll
    for (int i = 0; i < 2; ++i) ue[i] = *(LAS const unsigned*)(lds + CVB + CV_U + 2 * CV_UCH + CV_U1 + U_PADB + (256 * ec + 64 * g + 32 * i + 2 * l15) * 2);
    const lptr sc = lds + CV_SCR + l16;
    if (half) {
#pragma unroll
      for (int i = 0; i < 4; ++i) { *(LAS f32x4*)(sc + (sv * 4 + i) * 1024) = acc[i]; acc[i] = acc[4 + i]; }
    } else {
#pragma unroll
      for (int i = 0; i < 4; ++i) *(LAS f32x4*)(sc + (sv * 4 + i) * 1024) = acc[4 + i];
    }
    if (sv != 0) *(LAS f32x4*)(sc + 16384 + (sv - 1) * 1024) = ea;
    if (sv != 2) *(LAS f32x4*)(sc + 19456 + (sv > 2 ? 2 : sv) * 1024) = eb;
    STAMP(20);
    BAR();
    STAMP(27);
    KVC(6, 8);
#define FIN(x, y, uu, dd) pk2(rbf(x) + rbf(bflo(uu) * dd), rbf(y) + rbf(bfhi(uu) * dd))
#pragma unroll
    for (int pr = 0; pr < 2; ++pr) {
      const f32x4 f0 = acc[2 * pr] + *(LAS const f32x4*)(sc + ((sv ^ 1) * 4 + 2 * pr) * 1024), f1 = acc[2 * pr + 1] + *(LAS const f32x4*)(sc + ((sv ^ 1) * 4 + 2 * pr + 1) * 1024);
      unsigned a0 = FIN(f0[0], f0[1], us[4 * pr], dsk), a1 = FIN(f0[2], f0[3], us[4 * pr + 1], dsk), a2 = FIN(f1[0], f1[1], us[4 * pr + 2], dsk), a3 = FIN(f1[2], f1[3], us[4 * pr + 3], dsk);
      const bool o1b = lane & 1, o2b = lane & 2;
      unsigned r;
      r = dppu<0xB1>(o1b ? a0 : a1); if (o1b) a0 = r; else a1 = r;
      r = dppu<0xB1>(o1b ? a2 : a3); if (o1b) a2 = r; else a3 = r;
      r = dppu<0x4E>(o2b ? a0 : a2); if (o2b) a0 = r; else a2 = r;
      r = dppu<0x4E>(o2b ? a1 : a3); if (o2b) a1 = r; else a3 = r;
      st16c(ws + WS_YT + (2 * b + chl) * 4096 + (4 * half + 2 * pr + (p4 >> 1)) * 512 + g * 128 + (p4 & 1) * 64 + q4 * 16, (u32x4){a0, a1, a2, a3});
    }
    if (half == 0) {
      f32x4 e = chl ? eb : ea;
#pragma unroll
      for (int s = 0; s < 3; ++s) e += *(LAS const f32x4*)(sc + 16384 + chl * 3072 + s * 1024);
      char* p = ws + WS_YT + ce * 4096 + ec * 512 + g * 128 + l15 * 4;
      st4c(p, FIN(e[0], e[1], ue[0], dse)); st4c(p + 64, FIN(e[2], e[3], ue[1], dse));
    }
    STAMP(21);
    BARVM(0);
    STAMP(28);
    if (wid == 8 && lane == 0) st4c(myflag + 12, tag);
    wo_groups(6, 8); wo_groups(0, 1);
    const int tq = (qb >> 2) < 4 ? (qb >> 2) : 7 - (qb >> 2);
    const int pb0 = min(36 * hg + min(lane, 35), 255), pb1 = b == 0 ? sv * 64 + lane : 4 * lane + tq;
    const char* f2a = ws + WS_F + (lane >= 56 ? qb * 8 + (lane & 7) : ((pb0 & 7) * 4 + (pb0 >> 6)) * 8 + ((pb0 >> 3) & 7)) * 128;
    const char* f2b = ws + WS_F + (((pb1 & 7) * 4 + (pb1 >> 6)) * 8 + ((pb1 >> 3) & 7)) * 128;
    u32x4 fa, fb = {tag, tag, tag, tag};
    LD16(fa, f2a, " sc1");
    if (hg == 7 || b == 0) asm volatile("global_load_dwordx4 %0, %1, off sc1" : "+v"(fb) : "v"(f2b) : "memory");
    STAMP(22);
    BAR();
    STAMP(29);
    VMWAIT(0);
    PIN(fa); PIN(fb);
    flag_wait<3>(f2a, tag, fa); flag_wait<3>(f2b, tag, fb);
    {
      const char* yp = ws + WS_YT + hg * 72 * 4096 + qb * 128 + sv * 32;
      const unsigned yv = (lane >> 1) * 4096 + (lane & 1) * 16;
      gldssc(yv, yp, lds0 + C_YT + sv * 2304); gldssc(yv, yp + 32 * 4096, lds0 + C_YT + sv * 2304 + 1024);
      if (lane < 16) gldssc(yv, yp + 64 * 4096, lds0 + C_YT + sv * 2304 + 2048);
    }
    wo_groups(1, 3); wo_rem();
    STAMP(23);
    BARVM(0);
    STAMP(12);
    if (b == 0 && wid == 8 && lane == 0) st4c(ws + WS_E, tag);
#pragma unroll
    for (int n = 0; n < 3; ++n) {
      const int rit = min(16 * n + 4 * g + p4, 35), wit = 16 * n + (lane >> 2);
      const lptr rp = lds + C_YT + sv * 2304 + ((rit % 9) * 8 + q4) * 32 + (rit / 9) * 8;
      const u32x4 v = __builtin_bit_cast(u32x4, trd2(rp, rp + 128));
      if (wit < 36) *(u32x4*)(out + (size_t)(qb * 64 + sv * 16 + (wit / 9) * 4 + (lane & 3)) * OUTLD + hg * 72 + (wit % 9) * 8) = v;
    }
    __builtin_amdgcn_s_setprio(0);
    STAMP(13);
    BAR();
    if (any_bad()) { BAR(); KVC(0, 8); BARVM(0); BAR(); BAR(); wo_groups(0, 8); wo_rem(); BARVM(0); }
    __builtin_amdgcn_s_sleep(OSLEEP);
  }
  {
    FRESH_LANE();
    const int kt = wid >> 2;
    bf16x8 wf[6][4];
    {
      const int sw = 2 * (((q4 >> 1) & 1) | ((g & 1) << 1));
      const lptr wb = lds + kt * 24576 + (g * 8 + q4) * 128 + (p4 & 1) * 8;
#pragma unroll
      for (int nt = 0; nt < 4; ++nt) {
        const lptr p = wb + (((2 * nt + (p4 >> 1)) ^ sw) * 16);
#pragma unroll
        for (int kk = 0; kk < 6; ++kk) wf[kk][nt] = trd2(p + kk * 4096, p + kk * 4096 + 512);
      }
    }
    VMWAIT(0);
    const lptr wr = lds + C_REM + kt * 3072 + (g * 8 + q4) * 16 + (p4 & 1) * 8;
    STAMP(14);
    const char* op = ws + WS_O + (size_t)((qb * 4 + mt) * 18 + kt * 6) * 1024 + lane * 16;
    u32x4 of0, of1, of2, of3, of4, of5;
    const char* op2 = op + 3072;
    asm volatile("global_load_dwordx4 %0, %6, off sc1\n\tglobal_load_dwordx4 %1, %6, off offset:1024 sc1\n\t"
                 "global_load_dwordx4 %2, %6, off offset:2048 sc1\n\tglobal_load_dwordx4 %3, %7, off sc1\n\t"
                 "global_load_dwordx4 %4, %7, off offset:1024 sc1\n\tglobal_load_dwordx4 %5, %7, off offset:2048 sc1\n\ts_waitcnt vmcnt(0)"
                 : "=&v"(of0), "=&v"(of1), "=&v"(of2), "=&v"(of3), "=&v"(of4), "=&v"(of5) : "v"(op), "v"(op2) : "memory");
#define MX4(x) umax(umax(x[0], x[1]), umax(x[2], x[3]))
#pragma unroll 1
    for (int spin = 0; spin < SPIN_MAX; ++spin) {
      const bool m0 = umax(umax(MX4(of0), MX4(of1)), MX4(of2)) == SENT, m1 = umax(umax(MX4(of3), MX4(of4)), MX4(of5)) == SENT;
      if (!__any(m0 || m1)) break;
      __builtin_amdgcn_s_sleep(ORETRY);
      if (m0) asm volatile("global_load_dwordx4 %0, %3, off sc1\n\tglobal_load_dwordx4 %1, %3, off offset:1024 sc1\n\t"
                           "global_load_dwordx4 %2, %3, off offset:2048 sc1\n\ts_waitcnt vmcnt(0)" : "+v"(of0), "+v"(of1), "+v"(of2) : "v"(op) : "memory");
      if (m1) asm volatile("global_load_dwordx4 %0, %3, off sc1\n\tglobal_load_dwordx4 %1, %3, off offset:1024 sc1\n\t"
                           "global_load_dwordx4 %2, %3, off offset:2048 sc1\n\ts_waitcnt vmcnt(0)" : "+v"(of3), "+v"(of4), "+v"(of5) : "v"(op2) : "memory");
    }
    const u32x4 of[6] = {of0, of1, of2, of3, of4, of5};
    STAMP(15);
    f32x4 acc[5];
#pragma unroll
    for (int nt = 0; nt < 5; ++nt) acc[nt] = (f32x4){0.f, 0.f, 0.f, 0.f};
#pragma unroll
    for (int kk = 0; kk < 6; ++kk) {
#pragma unroll
      for (int nt = 0; nt < 4; ++nt) acc[nt] = MFMA16(wf[kk][nt], __builtin_bit_cast(bf16x8, of[kk]), acc[nt]);
      acc[4] = MFMA16(trd2(wr + kk * 512, wr + kk * 512 + 64), __builtin_bit_cast(bf16x8, of[kk]), acc[4]);
    }
    const lptr rd = lds + C_RED2 + mt * 5120 + lane * 16, rd2 = lds + C_RED + mt * 5120 + lane * 16;
#pragma unroll
    for (int nt = 0; nt < 5; ++nt) {
      const int own = nt >> 1;
      if (kt != own) *(LAS f32x4*)(((kt + 1) % 3 == own ? rd : rd2) + nt * 1024) = acc[nt];
    }
    BAR();
    u16* cp = out + (size_t)(qb * 64 + mt * 16 + l15) * OUTLD + DM + hg * 72 + g * 4 + kt * 32;
    const f32x4 a0 = kt == 0 ? acc[0] : (kt == 1 ? acc[2] : acc[4]), a1 = kt == 0 ? acc[1] : acc[3];
    const f32x4 v = a0 + *(LAS const f32x4*)(rd + kt * 2048) + *(LAS const f32x4*)(rd2 + kt * 2048);
    if (kt < 2 || g < 2) *(u32x2*)cp = (u32x2){pk2(v[0], v[1]), pk2(v[2], v[3])};
    if (kt < 2) {
      const f32x4 w = a1 + *(LAS const f32x4*)(rd + kt * 2048 + 1024) + *(LAS const f32x4*)(rd2 + kt * 2048 + 1024);
      *(u32x2*)(cp + 16) = (u32x2){pk2(w[0], w[1]), pk2(w[2], w[3])};
    }
  }
}

extern "C" void kernel_launch(void* const* d_in, const int* in_sizes, int n_in, void* d_out, int out_size, void* d_ws, size_t ws_size,
                              hipStream_t stream) {
  fused_kernel<<<256, 768, 0, stream>>>((const u16*)d_in[0], (const u16*)d_in[1], (const u16*)d_in[2], (const u16*)d_in[3], (const u16*)d_in[4],
                                        (const u16*)d_in[5], (const u16*)d_in[6], (const u16*)d_in[7], (const u16*)d_in[8], (const u16*)d_in[9],
                                        (const u16*)d_in[10], (const u16*)d_in[11], (const u16*)d_in[12], (const u16*)d_in[13], (u16*)d_out,
                                        (char*)d_ws);
}
```

```cpp
#include <hip/hip_runtime.h>
#include <hip/hip_bf16.h>
#include <stdint.h>

typedef __bf16 bf16x8 __attribute__((ext_vector_type(8)));
typedef __bf16 bf16x16 __attribute__((ext_vector_type(16)));
typedef __bf16 bf16x2 __attribute__((ext_vector_type(2)));
typedef short s16x4 __attribute__((ext_vector_type(4)));
typedef short s16x8 __attribute__((ext_vector_type(8)));
typedef float f32x2 __attribute__((ext_vector_type(2)));
typedef float f32x4 __attribute__((ext_vector_type(4)));
typedef float f32x8 __attribute__((ext_vector_type(8)));
typedef float f32x16 __attribute__((ext_vector_type(16)));
typedef float f32x32 __attribute__((ext_vector_type(32)));
typedef unsigned u32x2 __attribute__((ext_vector_type(2)));
typedef unsigned u32x4 __attribute__((ext_vector_type(4)));
typedef unsigned short u16;
#define LAS __attribute__((address_space(3)))
#define DI __device__ __forceinline__
typedef LAS char* lptr;

constexpr int SEQ = 2048, DM = 576, NS = 128, OUTLD = 2 * DM, ROWB = DM * 2;
constexpr int CH_BYTES = 19456, KV_BYTES = 8 * CH_BYTES, CH_TAIL = 8192, CH_V = 9216, V_OCT = 320;
constexpr int R_BYTES = 4160, CV_U = 13312, CV_UCH = 4640 + 4544, CV_U1 = 4640, U_PADB = 448, UT_ROW = 4224;
constexpr size_t WS_E = 0, WS_F = 4096, WS_KV = WS_F + 256 * 128;
constexpr size_t WS_UT = WS_KV + KV_BYTES;
constexpr size_t WS_YT = WS_UT + (size_t)DM * UT_ROW;
constexpr size_t WS_O = WS_YT + (size_t)DM * SEQ * 2;
constexpr size_t WS_DBG = 64u << 20;
constexpr int LDS_BYTES = 163840, KV_LDS = 0, SPARE = KV_BYTES, CVB = 6 * CH_BYTES;
constexpr int A_UT = 0, A_WKV = 9216, A_X0 = 18432, A_Q = 36864, Q_ROW = 144, A_CTRL = A_Q + 64 * Q_ROW, A_MISC = A_CTRL + 128, A_SCR = A_MISC + 144, A_W = 80896, B_CTRL = LDS_BYTES - 64;
constexpr int CV_SCR = 0, C_REM = 73728, C_YT = 82944, C_RED = C_YT + 9216, C_OX = C_RED + 20480, C_RED2 = C_OX + 24576;
static_assert(A_SCR + 6 * 5712 <= A_W - 256 && A_SCR % 16 == 0 && A_W + 82944 == LDS_BYTES && CVB + CV_U + 3 * CV_UCH <= B_CTRL - 384 && C_OX + 24576 <= 7 * CH_BYTES + 1024 &&
              C_RED2 + 20480 <= B_CTRL - 384, "LDS map");
constexpr float QSCALE = 0.23570226039551584f * 1.4426950408889634f;
constexpr unsigned SENT = 0xffffffffu;
constexpr int SPIN_MAX = 1 << 16;
#define POLL1 12
#define POLL0 8
#define XLEAD 0u
#define CSLEEP 8
#define OSLEEP 30
#define ORETRY 8

DI float bf2f(u16 x) { return __uint_as_float((unsigned)x << 16); }
DI float bflo(unsigned x) { return __uint_as_float(x << 16); }
DI float bfhi(unsigned x) { return __uint_as_float(x & 0xffff0000u); }
DI unsigned pk2(float lo, float hi) { f32x2 v = {lo, hi}; return __builtin_bit_cast(unsigned, __builtin_convertvector(v, bf16x2)); }
DI float rbf(float x) { return bfhi(pk2(x, x)); }
DI unsigned nosent(unsigned x) { return x < 0xfffffffeu ? x : 0xfffffffeu; }
#define GLDS(SC) "s_mov_b32 %0, m0\n\ts_mov_b32 m0, %2\n\ts_nop 0\n\tglobal_load_lds_dwordx4 %1, off" SC "\n\ts_mov_b32 m0, %0"
DI void glds16(const void* g, unsigned l) { unsigned sv; asm volatile(GLDS("") : "=&s"(sv) : "v"(g), "s"(l) : "memory"); }
DI void glds16c(const void* g, unsigned l) { unsigned sv; asm volatile(GLDS(" sc1") : "=&s"(sv) : "v"(g), "s"(l) : "memory"); }
#define GLDSS(SC) "s_mov_b32 %0, m0\n\ts_mov_b32 m0, %3\n\ts_nop 0\n\tglobal_load_lds_dwordx4 %1, %2" SC "\n\ts_mov_b32 m0, %0"
DI void gldss(unsigned voff, const void* sbase, unsigned l) { unsigned sv; asm volatile(GLDSS("") : "=&s"(sv) : "v"(voff), "s"(sbase), "s"(l) : "memory"); }
DI void gldssc(unsigned voff, const void* sbase, unsigned l) { unsigned sv; asm volatile(GLDSS(" sc1") : "=&s"(sv) : "v"(voff), "s"(sbase), "s"(l) : "memory"); }
DI void glds9(const unsigned (&vo)[9], const void* sbase, unsigned l) {
  unsigned sv;
  asm volatile("s_mov_b32 %0, m0\n\ts_mov_b32 m0, %11\n\ts_nop 0\n\t"
               "global_load_lds_dwordx4 %1, %10\n\tglobal_load_lds_dwordx4 %2, %10 offset:1024\n\t"
               "global_load_lds_dwordx4 %3, %10 offset:2048\n\tglobal_load_lds_dwordx4 %4, %10 offset:3072\n\t"
               "s_add_u32 m0, m0, 0x1000\n\ts_nop 0\n\t"
               "global_load_lds_dwordx4 %5, %10\n\tglobal_load_lds_dwordx4 %6, %10 offset:1024\n\t"
               "global_load_lds_dwordx4 %7, %10 offset:2048\n\tglobal_load_lds_dwordx4 %8, %10 offset:3072\n\t"
               "s_add_u32 m0, m0, 0x1000\n\ts_nop 0\n\tglobal_load_lds_dwordx4 %9, %10\n\ts_mov_b32 m0, %0"
               : "=&s"(sv) : "v"(vo[0]), "v"(vo[1]), "v"(vo[2]), "v"(vo[3]), "v"(vo[4]), "v"(vo[5]), "v"(vo[6]), "v"(vo[7]), "v"(vo[8]),
                 "s"(sbase), "s"(l) : "memory", "scc");
}
DI void glds4c(unsigned voff, const void* sbase, unsigned l) {
  unsigned sv;
  asm volatile("s_mov_b32 %0, m0\n\ts_mov_b32 m0, %3\n\ts_nop 0\n\t"
               "global_load_lds_dwordx4 %1, %2 sc1\n\tglobal_load_lds_dwordx4 %1, %2 offset:1024 sc1\n\t"
               "global_load_lds_dwordx4 %1, %2 offset:2048 sc1\n\tglobal_load_lds_dwordx4 %1, %2 offset:3072 sc1\n\ts_mov_b32 m0, %0"
               : "=&s"(sv) : "v"(voff), "s"(sbase), "s"(l) : "memory");
}
DI void touch(const void* g, unsigned junk) {
  unsigned sv;
  asm volatile("s_mov_b32 %0, m0\n\ts_mov_b32 m0, %2\n\ts_nop 0\n\tglobal_load_lds_dword %1, off sc1\n\ts_mov_b32 m0, %0" : "=&s"(sv) : "v"(g), "s"(junk) : "memory");
}
template <int W> DI void flag_wait(const char* p, unsigned tag, u32x4& v) {
#pragma unroll 1
  for (int spin = 0; spin < SPIN_MAX; ++spin) {
    if (__all(v[W] == tag)) break;
    __builtin_amdgcn_s_sleep(POLL1);
    if (v[W] != tag) asm volatile("global_load_dwordx4 %0, %1, off sc1\n\ts_waitcnt vmcnt(0)" : "+v"(v) : "v"(p) : "memory");
  }
}
DI void touchs(unsigned voff, const void* sbase, unsigned junk) {
  unsigned sv;
  asm volatile("s_mov_b32 %0, m0\n\ts_mov_b32 m0, %3\n\ts_nop 0\n\tglobal_load_lds_dword %1, %2 sc1\n\ts_mov_b32 m0, %0" : "=&s"(sv) : "v"(voff), "s"(sbase), "s"(junk) : "memory");
}
DI void slice_offsets(unsigned (&vo)[9], int lane) {
#pragma unroll
  for (int r = 0; r < 9; ++r) {
    const int c = r * 64 + lane, row = c / 9, src = (row & ~7) + 4 * (row & 1) + ((row >> 1) & 3);
    vo[r] = src * ROWB + (c - row * 9) * 16 + 4096 - (r & 3) * 1024;
  }
}
DI void kv_group(const char* ws, unsigned lds0, int slot, int rot, int grp, unsigned l16) {
  const int off = grp < 4 ? grp * 4096 : 15360;
  glds4c(l16, ws + WS_KV + ((slot + rot) & 7) * CH_BYTES + off, lds0 + KV_LDS + slot * CH_BYTES + off);
}
DI void lds_wait(LAS unsigned* p, unsigned n) {
  while (__hip_atomic_load(p, __ATOMIC_RELAXED, __HIP_MEMORY_SCOPE_WORKGROUP) < n) __builtin_amdgcn_s_sleep(1);
  asm volatile("" ::: "memory");
}
DI void st16c(void* p, u32x4 v) { asm volatile("global_store_dwordx4 %0, %1, off sc1\n\ts_nop 1" ::"v"(p), "v"(v) : "memory"); }
DI void st8c(void* p, u32x2 v) { asm volatile("global_store_dwordx2 %0, %1, off sc1" ::"v"(p), "v"(v) : "memory"); }
DI void st4c(void* p, unsigned v) { asm volatile("global_store_dword %0, %1, off sc1" ::"v"(p), "v"(v) : "memory"); }
#define LD4(d, p, SC) asm volatile("global_load_dword %0, %1, off" SC : "=v"(d) : "v"(p) : "memory")
#define LD8(d, p, SC) asm volatile("global_load_dwordx2 %0, %1, off" SC : "=v"(d) : "v"(p) : "memory")
#define LD16(d, p, SC) asm volatile("global_load_dwordx4 %0, %1, off" SC : "=v"(d) : "v"(p) : "memory")
#define PIN(x) asm volatile("" : "+v"(x))
#define VMWAIT(n) do { asm volatile("s_waitcnt vmcnt(" #n ")" ::: "memory"); __builtin_amdgcn_sched_barrier(0); } while (0)
#define BAR() asm volatile("s_waitcnt lgkmcnt(0)\n\ts_barrier" ::: "memory")
#define BARVM(n) asm volatile("s_waitcnt vmcnt(" #n ") lgkmcnt(0)\n\ts_barrier" ::: "memory")
#define CFENCE() asm volatile("" ::: "memory")
DI s16x4 trd(lptr p) { return __builtin_amdgcn_ds_read_tr16_b64_v4i16((LAS s16x4*)p); }
DI bf16x8 trd2(lptr a, lptr b) { return __builtin_bit_cast(bf16x8, __builtin_shufflevector(trd(a), trd(b), 0, 1, 2, 3, 4, 5, 6, 7)); }
#define MFMA16(a, b, c) __builtin_amdgcn_mfma_f32_16x16x32_bf16((a), (b), (c), 0, 0, 0)
#define MFMA32(a, b, c) __builtin_amdgcn_mfma_f32_32x32x16_bf16((a), (b), (c), 0, 0, 0)
#define SMF(a, b, c) __builtin_amdgcn_smfmac_f32_32x32x32_bf16((a), (b), (c), (int)0xdddddddd, 0, 0)
template <int CTRL> DI unsigned dppu(unsigned x) { return (unsigned)__builtin_amdgcn_update_dpp((int)x, (int)x, CTRL, 0xf, 0xf, false); }
DI unsigned umax(unsigned a, unsigned b) { return a > b ? a : b; }
DI unsigned max8(unsigned x) { x = umax(x, dppu<0xB1>(x)); x = umax(x, dppu<0x4E>(x)); return umax(x, dppu<0x141>(x)); }
DI unsigned max16(unsigned x) { x = max8(x); return umax(x, dppu<0x140>(x)); }

#define STAMP(i)

DI f32x2 cmul(f32x2 a, f32x2 b) {
  const f32x2 t = (f32x2){a[0], a[0]} * b;
  return __builtin_elementwise_fma((f32x2){a[1], a[1]}, (f32x2){-b[1], b[0]}, t);
}
DI f32x2 cpow(float zr, float zi, float c) {
  const float e = __builtin_amdgcn_exp2f(zr * c), th = __builtin_amdgcn_fractf(zi * c);
  return e * (f32x2){__builtin_amdgcn_cosf(th), __builtin_amdgcn_sinf(th)};
}
constexpr int KG_ROW = 272, KG_TAB = 21 * KG_ROW;
struct kg_in { unsigned are, aim, bre, bim, cre, cim, ldt; };
DI void kgen_half(const kg_in& in, lptr tb, int lane, f32x4 (&d)[2][2]) {
  const float dt = rbf(expf(bflo(in.ldt)));
  const float zr = (dt * bflo(in.are)) * 1.4426950408889634f, zi = (dt * bflo(in.aim)) * 0.15915494309189535f;
  const f32x2 dB = {dt * bflo(in.bre), dt * bflo(in.bim)}, Cc = {bflo(in.cre), bflo(in.cim)};
  lptr tw = tb + lane * 4;
#define PUT(row, v) do { const f32x2 v_ = (v); *(LAS unsigned*)(tw + (row) * KG_ROW) = pk2(v_[0], v_[1]); } while (0)
  const f32x2 w1 = cpow(zr, -zi, 1.f), w8 = cpow(zr, zi, 8.f), w256 = cpow(zr, -zi, 256.f);
  const f32x2 w2 = cmul(w1, w1), w3 = cmul(w2, w1), w4 = cmul(w2, w2), w6 = cmul(w2, w4);
  *(LAS unsigned*)tw = 0x3f80u;
  PUT(1, w1); PUT(2, w2); PUT(3, w3); PUT(4, w4); PUT(5, cmul(w1, w4)); PUT(6, w6); PUT(7, cmul(w3, w4));
  PUT(8, w256); PUT(9, cmul(w2, w256)); PUT(10, cmul(w4, w256)); PUT(11, cmul(w6, w256));
  const f32x2 w512 = cmul(w256, w256), w768 = cmul(w512, w256), w1024 = cmul(w768, w256);
  PUT(12, w512); PUT(13, cmul(w512, w4)); PUT(14, w768); PUT(15, cmul(w768, w4));
  PUT(16, w1024); PUT(17, cmul(w256, w1024)); PUT(18, cmul(w512, w1024)); PUT(19, cmul(w768, w1024)); PUT(20, cmul(w1024, w1024));
  CFENCE();
  const int l15 = lane & 15, g = lane >> 4;
  lptr fb = tb + l15 * KG_ROW + g * 16, fb1 = tb + (16 + min(l15, 4)) * KG_ROW + g * 16;
  bf16x8 a0[4], a1[4];
#pragma unroll
  for (int kk = 0; kk < 4; ++kk) { a0[kk] = *(LAS const bf16x8*)(fb + kk * 64); a1[kk] = *(LAS const bf16x8*)(fb1 + kk * 64); }
  CFENCE();
  const f32x2 w16 = cmul(w8, w8), w32 = cmul(w16, w16), w64 = cmul(w32, w32);
  f32x2 bb[8];
  bb[0] = cmul(Cc, dB); bb[1] = cmul(bb[0], w8); bb[2] = cmul(bb[0], w16); bb[3] = cmul(bb[1], w16);
#pragma unroll
  for (int i = 0; i < 4; ++i) bb[4 + i] = cmul(bb[i], w32);
#pragma unroll
  for (int ct = 0; ct < 2; ++ct) {
#pragma unroll
    for (int i = 0; i < 2; ++i) {
#pragma unroll
      for (int j = 0; j < 8; ++j) PUT(8 * i + j, bb[j]);
      if (ct == 0 || i == 0) {
#pragma unroll
        for (int j = 0; j < 8; ++j) bb[j] = cmul(bb[j], w64);
      }
    }
    CFENCE();
    bf16x8 bf[4];
#pragma unroll
    for (int kk = 0; kk < 4; ++kk) bf[kk] = *(LAS const bf16x8*)(fb + kk * 64);
    CFENCE();
    d[ct][0] = (f32x4){0.f, 0.f, 0.f, 0.f}; d[ct][1] = d[ct][0];
#pragma unroll
    for (int kk = 0; kk < 4; ++kk) { d[ct][0] = MFMA16(a0[kk], bf[kk], d[ct][0]); d[ct][1] = MFMA16(a1[kk], bf[kk], d[ct][1]); }
  }
}
DI void kgen_finish(const f32x4 (&d)[2][2], lptr img, LAS unsigned* gate, lptr tb, int lane) {
  const int l15 = lane & 15, g = lane >> 4;
  {
    const int e0 = g < 2 ? 8 * l15 + 4 * g : (g == 2 ? 256 + 4 * l15 : 384 + 2 * l15), es = g < 2 ? 128 : (g == 2 ? 64 : 32), e1 = g == 3 ? 64 : 2;
    const int f0 = g == 0 ? 512 + l15 : (lane == 16 ? 640 : 700);
#pragma unroll
    for (int ct = 0; ct < 2; ++ct) {
      const lptr p = tb + (e0 + ct * es) * 2;
      *(LAS unsigned*)p = pk2(d[ct][0][0], d[ct][0][1]); *(LAS unsigned*)(p + e1 * 2) = pk2(d[ct][0][2], d[ct][0][3]);
      LAS u16* q = (LAS u16*)tb + f0 + ct * 16;
      const unsigned x0 = pk2(d[ct][1][0], d[ct][1][1]), x1 = pk2(d[ct][1][2], d[ct][1][3]);
      q[0] = (u16)x0; q[32] = (u16)(x0 >> 16); q[64] = (u16)x1; q[96] = (u16)(x1 >> 16);
    }
  }
  lds_wait(gate, 8u);
  LAS const u16* C = (LAS const u16*)tb;
  const int rl = 63 - lane;
#pragma unroll
  for (int r = 0; r < 2; ++r) {
    const unsigned a = C[512 + 64 * (1 - r) + rl], bb = C[513 + 64 * (1 - r) + rl];
    const unsigned aa = a | (a << 16), b2x = bb | (bb << 16);
    *(LAS u32x4*)(img + r * 1024 + lane * 16) = (u32x4){b2x, (rl & 1) ? b2x : (bb | (a << 16)), aa, aa};
  }
  {
    const unsigned ab = *(LAS const unsigned*)(C + 384 + 2 * rl), c = C[386 + 2 * rl];
    *(LAS u32x4*)(img + 2048 + lane * 16) = (u32x4){c | (c << 16), __builtin_amdgcn_perm(ab, ab, 0x03020302u), __builtin_amdgcn_perm(ab, ab, 0x01000302u),
                                                __builtin_amdgcn_perm(ab, ab, 0x01000100u)};
  }
  {
    const bool hi = lane < 32;
    const int e = hi ? 128 + 4 * rl : 8 * rl;
    const u32x2 w01 = *(LAS const u32x2*)(C + e), w23 = *(LAS const u32x2*)(C + e + 4);
    const unsigned o0 = hi ? __builtin_amdgcn_perm(w01[1], w23[0], 0x07060100u) : __builtin_amdgcn_perm(w23[1], w23[1], 0x01000302u);
    const unsigned o1 = __builtin_amdgcn_perm(w23[0], w01[1], hi ? 0x01000100u : 0x05040706u);
    const unsigned o2 = __builtin_amdgcn_perm(w01[1], w01[0], hi ? 0x03020504u : 0x05040706u);
    const unsigned o3 = __builtin_amdgcn_perm(w01[0], w01[0], hi ? 0x01000100u : 0x01000302u);
    *(LAS u32x4*)(img + 3072 + lane * 16) = (u32x4){o0, o1, o2, o3};
  }
  if (lane < 4) *(LAS u32x4*)(img + 4096 + lane * 16) = (u32x4){0u, 0u, 0u, 0u};
}

template <int NT> DI void qgemm(lptr lds, LAS unsigned* ctrl, const bf16x8 (&xa)[18], int mt, int lane) {
  const int l15 = lane & 15, g = lane >> 4;
  constexpr int nt0 = NT == 2 ? 3 : 0;
  f32x4 acc[NT];
#pragma unroll
  for (int i = 0; i < NT; ++i) acc[i] = (f32x4){0.f, 0.f, 0.f, 0.f};
  const lptr wq = lds + A_W + (g * 8 + (l15 >> 2) * 2) * 144 + (l15 & 3) * 8 + nt0 * 32;
  constexpr int PD = NT == 3 ? 4 : 6;
  bf16x8 wf[PD + 1][NT];
  unsigned stage = 0;
#define WNEED(n) do { while (stage < (n)) { stage = __builtin_amdgcn_readfirstlane(__hip_atomic_load(&ctrl[4], __ATOMIC_RELAXED, __HIP_MEMORY_SCOPE_WORKGROUP)); \
                                            if (stage < (n)) __builtin_amdgcn_s_sleep(1); } CFENCE(); } while (0)
#pragma unroll
  for (int kk = 0; kk < 18 + PD; ++kk) {
    if (kk >= PD) {
#pragma unroll
      for (int i = 0; i < NT; ++i) acc[i] = MFMA16(wf[(kk - PD) % (PD + 1)][i], xa[kk - PD], acc[i]);
    }
    __builtin_amdgcn_sched_barrier(0);
    if (kk < 18) {
      if ((kk & 1) == 0) WNEED((unsigned)(kk / 2 + 1));
#pragma unroll
      for (int i = 0; i < NT; ++i) wf[kk % (PD + 1)][i] = trd2(wq + kk * 4608 + i * 32, wq + kk * 4608 + 144 + i * 32);
    }
    __builtin_amdgcn_sched_barrier(0);
  }
  LAS unsigned* qw = (LAS unsigned*)(lds + A_Q + (mt * 16 + l15) * Q_ROW + nt0 * 32 + g * 8);
#pragma unroll
  for (int i = 0; i < NT; ++i)
    if (nt0 + i < 4 || g < 2) *(LAS u32x2*)(qw + 8 * i) = (u32x2){pk2(acc[i][0] * QSCALE, acc[i][1] * QSCALE), pk2(acc[i][2] * QSCALE, acc[i][3] * QSCALE)};
  CFENCE();
  if (lane == 0) __hip_atomic_fetch_add(&ctrl[5], 1u, __ATOMIC_RELAXED, __HIP_MEMORY_SCOPE_WORKGROUP);
}

DI f32x32 qk_tile(lptr kp, const bf16x8& q0, const bf16x8& q1, unsigned kt, u32x2 qa) {
  const bf16x8 k = *(LAS const bf16x8*)kp;
  f32x16 z;
#pragma unroll
  for (int j = 0; j < 16; ++j) z[j] = 0.f;
  const f32x16 a = MFMA32(k, q0, z), b = MFMA32(k, q1, z);
  f32x32 s = __builtin_shufflevector(a, b, 0, 1, 2, 3, 4, 5, 6, 7, 8, 9, 10, 11, 12, 13, 14, 15, 16, 17, 18, 19, 20, 21, 22, 23, 24, 25, 26, 27, 28, 29, 30, 31);
  return __builtin_amdgcn_mfma_f32_32x32x4bf16_1k(__builtin_bit_cast(s16x4, (u32x2){kt, 0u}), __builtin_bit_cast(s16x4, qa), s, 0, 0, 0);
}
DI bf16x16 sub8(const f32x32& s, int o) {
  f32x8 t = {s[o], s[o + 1], s[o + 2], s[o + 3], s[o + 4], s[o + 5], s[o + 6], s[o + 7]};
  return __builtin_bit_cast(bf16x16, t);
}

__global__ __launch_bounds__(768, 3) void fused_kernel(const u16* __restrict__ U, const u16* __restrict__ Are, const u16* __restrict__ Aim,
                                                       const u16* __restrict__ Bre, const u16* __restrict__ Bim, const u16* __restrict__ Cre,
                                                       const u16* __restrict__ Cim, const u16* __restrict__ logdt, const u16* __restrict__ Dskip,
                                                       const u16* __restrict__ X, const u16* __restrict__ Wq, const u16* __restrict__ Wk,
                                                       const u16* __restrict__ Wv, const u16* __restrict__ Wo, u16* __restrict__ out,
                                                       char* __restrict__ ws) {
  __shared__ __attribute__((aligned(16))) char lds_raw[LDS_BYTES];
  const lptr lds = (lptr)lds_raw;
  const unsigned lds0 = (unsigned)(uintptr_t)lds_raw;
  STAMP(0);
  const int tid = threadIdx.x, lane = tid & 63, wid = __builtin_amdgcn_readfirstlane(tid >> 6);
  const int l15 = lane & 15, g = lane >> 4, r32 = lane & 31, h = lane >> 5, q4 = l15 >> 2, p4 = l15 & 3;
  const int b = blockIdx.x, qb = (b & 7) * 4 + (b >> 6), hg = (b >> 3) & 7;
  const int ce = 512 + (b >> 2), cq = b & 3, mt = wid & 3;
  char* const myflag = ws + WS_F + (qb * 8 + hg) * 128;
  LAS unsigned* ctrl = (LAS unsigned*)(lds + A_CTRL);
  LAS unsigned* bctl = (LAS unsigned*)(lds + B_CTRL);
#define LADD(i) do { if (lane == 0) __hip_atomic_fetch_add(&ctrl[i], 1u, __ATOMIC_RELAXED, __HIP_MEMORY_SCOPE_WORKGROUP); } while (0)
#define LSET(i, v) do { if (lane == 0) __hip_atomic_store(&ctrl[i], (v), __ATOMIC_RELAXED, __HIP_MEMORY_SCOPE_WORKGROUP); } while (0)
  {
    const int arg = wid >= 6 ? wid - 5 : (mt == 0 ? 9 : (wid < 4 ? 10 : 11 + (hg >> 2)));
    const char* base = ((const char* const __attribute__((address_space(4)))*)__builtin_amdgcn_kernarg_segment_ptr())[arg];
    const int o0 = wid >= 6 ? b * 512 : (mt == 0 ? qb * 73728 + hg * 9216 + wid * 1152 : (wid < 4 ? ((b >> 3) * 3 + wid - 1) * 6912 : (b >> 6) * 5184));
    const int st = wid >= 6 ? 16 : (mt == 0 ? 72 : (wid < 4 ? 108 : 80));
    unsigned vo = lane * st + o0;
    if (wid >= 6 && lane >= 32) vo = ce * 256 + r32 * 8;
    touchs(vo, base, lds0 + A_W - 256);
  }
  if (tid < 32) ctrl[tid] = 0u;
  BAR();
  asm volatile("" :: "s"(U), "s"(Are), "s"(Aim), "s"(Bre), "s"(Bim), "s"(Cre), "s"(Cim), "s"(logdt), "s"(Dskip), "s"(X), "s"(Wq), "s"(Wk), "s"(Wv), "s"(Wo),
               "s"(out), "s"(ws));
  const int xoff = mt == 0 ? A_X0 : A_W + 9216 + mt * 18432;
  const unsigned xv0 = (lane >> 3) * ROWB + (((lane & 7) ^ (lane >> 4)) * 16), xv1 = (8 + (lane >> 3)) * ROWB + (((lane & 7) ^ (4 + (lane >> 4))) * 16);
  const char* xs = (const char*)X + (size_t)(qb * 64 + mt * 16) * ROWB;
  const bool kgw = mt != (wid < 4 ? 0 : 3) && (wid < 4 || wid >= 8);
  const int kc = wid < 4 ? wid - 1 : wid - 8, kch = kc < 2 ? 2 * b + kc : ce;
  int msc = kgw ? A_SCR + (2 * kc + (wid < 4 ? 1 : 0)) * KG_TAB + 49 * 16 : A_MISC + (wid == 11 ? 80 : (wid == 0 ? 64 : mt * 16));
  asm volatile("" : "+v"(msc)); msc = __builtin_amdgcn_readfirstlane(msc);
  if (wid >= 4 && wid < 8) {
    __builtin_amdgcn_s_setprio(3);
#pragma unroll
    for (int kb = 0; kb < 2; ++kb) { gldss(xv0, xs + kb * 128, lds0 + xoff + kb * 2048); gldss(xv1, xs + kb * 128, lds0 + xoff + kb * 2048 + 1024); }
  } else if (wid == 0) {
    __builtin_amdgcn_s_setprio(3);
    const char* wx = (const char*)(hg < 4 ? Wk : Wv) + 8 * (hg & 3);
#pragma unroll 1
    for (int i = 0; i < 8; ++i) gldss(lane * 36, wx + i * 64 * 36, lds0 + A_WKV + i * 1024);
    if (lane < 63) gldss(lane * 36, wx + 512 * 36, lds0 + A_WKV + 8192);
    if (lane == 0) {
      unsigned sv_;
      asm volatile("s_mov_b32 %0, m0\n\ts_mov_b32 m0, %3\n\ts_nop 0\n\tglobal_load_lds_dwordx3 %1, %2\n\ts_mov_b32 m0, %0"
                   : "=&s"(sv_) : "v"(575 * 36), "s"(wx), "s"(lds0 + A_WKV + 575 * 16) : "memory");
    }
  }
  {
    const int a6 = lane >> 3;
    const u16* pa = a6 < 2 ? (a6 == 0 ? Are : Aim) : (a6 < 4 ? (a6 == 2 ? Bre : Bim) : (a6 == 4 ? Cre : Cim));
    pa += kch * NS + (wid < 4 ? 64 : 0) + (lane & 7) * 8;
    if (lane == 48) pa = logdt + (kch & ~7);
    if (lane == 49) pa = (const u16*)(ws + WS_E);
    if (lane >= 50) pa = Dskip + (kch & ~7);
    if (lane >= (kgw ? 0 : 49) && lane < (kgw ? 51 : 50)) glds16c(pa, lds0 + msc - 49 * 16);
  }
  STAMP(17);
#define TAG() (*(LAS const unsigned*)(lds + msc) * 2654435761u + 0x9e3779b9u)
  auto kg_read = [&](kg_in& in) {
    LAS const u16* p = (LAS const u16*)(lds + msc - 49 * 16) + lane;
    in.are = p[0]; in.aim = p[64]; in.bre = p[128]; in.bim = p[192]; in.cre = p[256]; in.cim = p[320];
    in.ldt = ((LAS const u16*)(lds + msc - 16))[kch & 7];
    CFENCE();
  };
#define FRESH_LANE() int tid_ = threadIdx.x; asm volatile("" : "+v"(tid_));                                                          \
  const int lane = tid_ & 63, l15 = lane & 15, g = lane >> 4, r32 = lane & 31, h = lane >> 5, q4 = l15 >> 2, p4 = l15 & 3;             \
  (void)r32; (void)h; (void)q4; (void)p4; (void)g
  auto any_bad = [&]() {
    const u32x4 a = *(LAS const u32x4*)bctl, c = *(LAS const u32x4*)(bctl + 4);
    return __builtin_amdgcn_readfirstlane((int)(a[0] | a[1] | a[2] | a[3] | c[0] | c[1] | c[2] | c[3]));
  };

  if (wid < 8) {
    {
      const lptr xb0 = lds + xoff + l15 * 128 + ((g ^ (l15 >> 1)) * 16), xb1 = lds + xoff + l15 * 128 + (((4 + g) ^ (l15 >> 1)) * 16);
      bf16x8 xa[18];
      if (wid >= 4) {
#pragma unroll
        for (int kb = 2; kb < 9; ++kb) {
          gldss(xv0, xs + kb * 128, lds0 + xoff + kb * 2048); gldss(xv1, xs + kb * 128, lds0 + xoff + kb * 2048 + 1024);
          if (kb == 6) LADD(3);
        }
        STAMP(1);
        char* cbp = ws + WS_KV + (qb >> 2) * CH_BYTES;
        const int key0 = (qb & 3) * 64 + mt * 16;
        char* sp;
        if (hg < 4) {
          const int key = key0 + l15;
          sp = g == 0 ? cbp + (key >> 5) * 1024 + (hg >> 1) * 512 + (key & 31) * 16 + (hg & 1) * 8 : cbp + CH_TAIL + (key & 63) * 16 + (key >> 6) * 4;
        } else {
          const int key = key0 + 4 * g, dim = 4 * (hg - 4) + l15;
          const int vrow = l15 < 4 ? ((dim & 3) | ((dim & 4) << 1) | ((dim & 8) >> 1)) : 12 + l15;
          sp = cbp + CH_V + (key >> 3) * V_OCT + (key & 4) * 2 + vrow * 16;
        }
        PIN(sp);
        lds_wait(&ctrl[0], 1u);
        const lptr wb = lds + A_WKV + (g * 8 + q4) * 16 + (l15 & 1) * 8;
        bf16x8 wk[18];
#pragma unroll
        for (int i = 0; i < 18; ++i) wk[i] = trd2(wb + i * 512, wb + i * 512 + 64);
        STAMP(2);
        f32x4 acc = {0.f, 0.f, 0.f, 0.f}, acc2 = acc;
#define XGRP(kb0, n, KTYPE)                                                                                                    \
        { VMWAIT(n);                                                                                                           \
          if ((n) == 0) LSET(8 + mt, 1u);                                                                                      \
          _Pragma("unroll") for (int i = 0; i < 6; ++i) xa[2 * (kb0) + i] = *(LAS const bf16x8*)(((i & 1) ? xb1 : xb0) + ((kb0) + (i >> 1)) * 2048); \
          CFENCE();                                                                                                            \
          if ((n) == 0) LADD(12 + mt);                                                                                         \
          _Pragma("unroll") for (int i = 2 * (kb0); i < 2 * (kb0) + 6; i += 2) {                                                \
            if (KTYPE) { acc = MFMA16(wk[i], xa[i], acc); acc2 = MFMA16(wk[i + 1], xa[i + 1], acc2); }                          \
            else { acc = MFMA16(xa[i], wk[i], acc); acc2 = MFMA16(xa[i + 1], wk[i + 1], acc2); } } }
        if (hg < 4) { XGRP(0, 12, 1) XGRP(3, 6, 1) XGRP(6, 0, 1) } else { XGRP(0, 12, 0) XGRP(3, 6, 0) XGRP(6, 0, 0) }
        STAMP(3);
        acc += acc2;
        u32x2 v = {pk2(acc[0], acc[1]), pk2(acc[2], acc[3])};
        unsigned mv;
        if (hg < 4) {
          if (g == 0) st8c(sp, v);
          if (g == 1 && hg == 3) st4c(sp, v[0]);
          float n2 = bflo(v[0]) * bflo(v[0]) + bfhi(v[0]) * bfhi(v[0]);
          if (g == 0) n2 += bflo(v[1]) * bflo(v[1]) + bfhi(v[1]) * bfhi(v[1]);
          const unsigned m16 = max16(__float_as_uint(n2) & 0x7fffffffu);
          const float mq = __uint_as_float(__builtin_amdgcn_readlane(m16, 0)), mtl = __uint_as_float(__builtin_amdgcn_readlane(m16, 16));
          mv = __float_as_uint(hg == 3 ? mq + mtl : mq) & 0x7fffffffu;
        } else {
          unsigned am = umax(umax(v[0] << 16 & 0x7fffffffu, v[0] & 0x7fff0000u), umax(v[1] << 16 & 0x7fffffffu, v[1] & 0x7fff0000u));
          if (l15 >= 6) am = 0u;
          if (l15 == 6) v = (u32x2){0x3f803f80u, 0x3f803f80u};
          if (l15 == 7) v = (u32x2){0u, 0u};
          if (l15 < 4 || (hg == 7 && l15 < 8)) st8c(sp, v);
          am = max16(am);
          mv = umax(umax(__builtin_amdgcn_readlane(am, 0), __builtin_amdgcn_readlane(am, 16)), umax(__builtin_amdgcn_readlane(am, 32), __builtin_amdgcn_readlane(am, 48)));
        }
        if (lane == 0) __hip_atomic_fetch_max(&ctrl[2], mv, __ATOMIC_RELAXED, __HIP_MEMORY_SCOPE_WORKGROUP);
        STAMP(4);
      } else {
        if (wid == 0) {
          __builtin_amdgcn_s_setprio(2);
          unsigned vo[9];
          slice_offsets(vo, lane);
          VMWAIT(1);
          LSET(0, 1u);
          STAMP(1);
          glds9(vo, (const char*)U + (size_t)qb * 64 * ROWB + hg * 144 - 4096, lds0 + A_UT);
          const lptr rp = lds + A_UT + (((4 * g + p4) & 7) * 8 + 2 * q4) * 144 + (g >> 1) * 8;
          char* up = ws + WS_UT + (size_t)(hg * 72 + (lane >> 5) * 4 + (lane & 3)) * UT_ROW + 128 + qb * 128 + ((lane >> 2) & 7) * 16;
          VMWAIT(0);
#pragma unroll
          for (int i = 0; i < 9; ++i) st16c(up + i * 8 * UT_ROW, __builtin_bit_cast(u32x4, trd2(rp + i * 16, rp + i * 16 + 144)));
          if (qb == 0) {
            st16c(ws + WS_UT + (size_t)(hg * 72 + lane) * UT_ROW + 112, (u32x4){0u, 0u, 0u, 0u});
            st16c(ws + WS_UT + (size_t)(hg * 72 + 64 + (lane & 7)) * UT_ROW + 112, (u32x4){0u, 0u, 0u, 0u});
          }
          STAMP(2);
        } else {
          __builtin_amdgcn_s_setprio(2);
          VMWAIT(0);
          kg_in in; kg_read(in);
          STAMP(1);
          f32x4 d[2][2];
          const lptr tb = lds + A_SCR + (2 * wid - 1) * KG_TAB;
          kgen_half(in, tb, lane, d);
#pragma unroll
          for (int i = 0; i < 4; ++i) *(LAS f32x4*)(tb + i * 1024 + lane * 16) = d[i >> 1][i & 1];
          CFENCE();
          LSET(15 + wid, 1u);
          VMWAIT(0);
          STAMP(2);
        }
        lds_wait(&ctrl[8 + mt], 1u);
#pragma unroll
        for (int kk = 0; kk < 18; ++kk) xa[kk] = *(LAS const bf16x8*)(((kk & 1) ? xb1 : xb0) + (kk >> 1) * 2048);
        CFENCE();
        LADD(12 + mt);
        STAMP(3);
        STAMP(4);
      }
      if (wid >= 4) {
        VMWAIT(0);
        if (lane == 0 && __hip_atomic_fetch_add(&ctrl[1], 1u, __ATOMIC_RELAXED, __HIP_MEMORY_SCOPE_WORKGROUP) == 3u)
          st8c(myflag, (u32x2){TAG(), __hip_atomic_load(&ctrl[2], __ATOMIC_RELAXED, __HIP_MEMORY_SCOPE_WORKGROUP)});
        __builtin_amdgcn_s_setprio(2);
        STAMP(5);
        qgemm<2>(lds, ctrl, xa, mt, lane);
      } else {
        VMWAIT(0);
        if (wid == 0 && lane == 0) st4c(myflag + 8, TAG());
        STAMP(5);
        qgemm<3>(lds, ctrl, xa, mt, lane);
      }
    }
    STAMP(8);
    FRESH_LANE();
    const int kp = wid >> 2, head = wid & 3;
    f32x16 o0, o1;
#pragma unroll
    for (int j = 0; j < 16; ++j) { o0[j] = 0.f; o1[j] = 0.f; }
    const unsigned ka0 = lds0 + KV_LDS + kp * 1024 + lane * 16, ta0 = lds0 + KV_LDS + CH_TAIL + kp * 512 + r32 * 16,
                   va0 = lds0 + KV_LDS + CH_V + kp * 4 * V_OCT + h * V_OCT + (r32 < 19 ? r32 : 19) * 16;
    unsigned oo = (r32 >> 4) * 18432 + l15 * 16 + h * 256;
    PIN(oo);
    u32x4 q0, q1; unsigned qtl;
    lds_wait(&ctrl[5], 8u);
    {
      LAS const unsigned* qp = (LAS const unsigned*)(lds + A_Q + r32 * Q_ROW + head * 36 + h * 16);
#pragma unroll
      for (int j = 0; j < 4; ++j) { q0[j] = qp[j]; q1[j] = qp[32 * Q_ROW / 4 + j]; }
      qtl = *(LAS const unsigned*)(lds + A_Q + lane * Q_ROW + head * 36 + 32);
    }
    CFENCE();
    LADD(6);
    const u32x2 qa = {qtl, 0u}, qc = {0u, qtl};
    float qn2;
    {
#define DOT2(x, c) __builtin_amdgcn_fdot2_f32_bf16(__builtin_bit_cast(bf16x2, x), __builtin_bit_cast(bf16x2, x), (c), false)
      const float nt = DOT2(qtl, 0.f);
      float n0 = h ? 0.f : nt, n1 = h ? nt : 0.f;
#pragma unroll
      for (int j = 0; j < 4; ++j) { n0 = DOT2(q0[j], n0); n1 = DOT2(q1[j], n1); }
      const auto sw = __builtin_amdgcn_permlane32_swap(__float_as_uint(n0), __float_as_uint(n1), false, false);
      qn2 = __uint_as_float(sw[0]) + __uint_as_float(sw[1]);
      PIN(qn2);
    }
    STAMP(9);
    lds_wait(&ctrl[7], 1u);
    STAMP(10);
    {
      int it = 8; unsigned ka = ka0, va = va0, ta = ta0;
      asm volatile(
      "ds_read_b128 v[128:131], %[ka]\n"
      "ds_read_b128 v[140:143], %[ta]\n"
      "v_mov_b32 v120, 0\n"
      "v_mov_b32 v121, 0\n"
      "v_mov_b32 v122, 0\n"
      "v_mov_b32 v123, 0\n"
      "v_mov_b32 v124, 0\n"
      "v_mov_b32 v125, 0\n"
      "v_mov_b32 v126, 0\n"
      "v_mov_b32 v127, 0\n"
      "v_mov_b32 v136, 0\n"
      "v_mov_b32 v137, 0\n"
      "v_mov_b32 v138, 0\n"
      "v_mov_b32 v139, 0\n"
      "s_waitcnt lgkmcnt(1)\n"
      "v_mfma_f32_32x32x16_bf16 v[64:79], v[128:131], %[q0], 0\n"
      "v_mfma_f32_32x32x16_bf16 v[80:95], v[128:131], %[q1], 0\n"
      "ds_read_b128 v[128:131], %[ka] offset:2048\n"
      "ds_read_b128 v[132:135], %[va]\n"
      "s_nop 7\n"
      "s_waitcnt lgkmcnt(2)\n"
      "v_mfma_f32_32x32x4_2b_bf16 v[64:95], v[140:141], %[qa], v[64:95]\n"
      "s_nop 15\n"
      "s_nop 1\n"
      "1:\n"
      "s_setprio 1\n"
      "s_waitcnt lgkmcnt(1)\n"
      "v_mfma_f32_32x32x16_bf16 v[96:111], v[128:131], %[q0], 0\n"
      "v_exp_f32 v64, v64\n"
      "v_exp_f32 v65, v65\n"
      "v_exp_f32 v66, v66\n"
      "v_smfmac_f32_32x32x32_bf16 %[o1], v[136:139], v[120:127], %[ix]\n"
      "ds_read_b128 v[136:139], %[va] offset:640\n"
      "v_exp_f32 v67, v67\n"
      "v_exp_f32 v68, v68\n"
      "v_exp_f32 v69, v69\n"
      "v_mfma_f32_32x32x16_bf16 v[112:127], v[128:131], %[q1], 0\n"
      "ds_read_b128 v[128:131], %[ka] offset:4096\n"
      "v_exp_f32 v70, v70\n"
      "v_exp_f32 v71, v71\n"
      "v_exp_f32 v72, v72\n"
      "v_exp_f32 v73, v73\n"
      "s_waitcnt lgkmcnt(2)\n"
      "v_smfmac_f32_32x32x32_bf16 %[o0], v[132:135], v[64:71], %[ix]\n"
      "v_exp_f32 v74, v74\n"
      "v_exp_f32 v75, v75\n"
      "v_exp_f32 v76, v76\n"
      "v_exp_f32 v77, v77\n"
      "v_exp_f32 v78, v78\n"
      "v_mfma_f32_32x32x4_2b_bf16 v[96:127], v[140:141], %[qc], v[96:127]\n"
      "v_exp_f32 v79, v79\n"
      "v_exp_f32 v80, v80\n"
      "v_exp_f32 v81, v81\n"
      "v_exp_f32 v82, v82\n"
      "v_exp_f32 v83, v83\n"
      "v_exp_f32 v84, v84\n"
      "v_exp_f32 v85, v85\n"
      "v_exp_f32 v86, v86\n"
      "s_waitcnt lgkmcnt(1)\n"
      "v_smfmac_f32_32x32x32_bf16 %[o0], v[136:139], v[72:79], %[ix]\n"
      "v_exp_f32 v87, v87\n"
      "v_exp_f32 v88, v88\n"
      "v_exp_f32 v89, v89\n"
      "v_exp_f32 v90, v90\n"
      "v_smfmac_f32_32x32x32_bf16 %[o1], v[132:135], v[80:87], %[ix]\n"
      "ds_read_b128 v[132:135], %[va] offset:2560\n"
      "v_exp_f32 v91, v91\n"
      "v_exp_f32 v92, v92\n"
      "v_exp_f32 v93, v93\n"
      "v_exp_f32 v94, v94\n"
      "v_exp_f32 v95, v95\n"
      "s_setprio 0\n"
      "s_waitcnt lgkmcnt(1)\n"
      "v_mfma_f32_32x32x16_bf16 v[64:79], v[128:131], %[q0], 0\n"
      "v_exp_f32 v96, v96\n"
      "v_exp_f32 v97, v97\n"
      "v_exp_f32 v98, v98\n"
      "v_smfmac_f32_32x32x32_bf16 %[o1], v[136:139], v[88:95], %[ix]\n"
      "ds_read_b128 v[136:139], %[va] offset:3200\n"
      "v_exp_f32 v99, v99\n"
      "v_exp_f32 v100, v100\n"
      "v_exp_f32 v101, v101\n"
      "v_mfma_f32_32x32x16_bf16 v[80:95], v[128:131], %[q1], 0\n"
      "ds_read_b128 v[128:131], %[ka] offset:6144\n"
      "v_exp_f32 v102, v102\n"
      "v_exp_f32 v103, v103\n"
      "v_exp_f32 v104, v104\n"
      "v_exp_f32 v105, v105\n"
      "s_waitcnt lgkmcnt(2)\n"
      "v_smfmac_f32_32x32x32_bf16 %[o0], v[132:135], v[96:103], %[ix]\n"
      "v_exp_f32 v106, v106\n"
      "v_exp_f32 v107, v107\n"
      "v_exp_f32 v108, v108\n"
      "v_exp_f32 v109, v109\n"
      "v_exp_f32 v110, v110\n"
      "v_mfma_f32_32x32x4_2b_bf16 v[64:95], v[142:143], %[qa], v[64:95]\n"
      "v_exp_f32 v111, v111\n"
      "v_exp_f32 v112, v112\n"
      "v_exp_f32 v113, v113\n"
      "v_exp_f32 v114, v114\n"
      "v_exp_f32 v115, v115\n"
      "v_exp_f32 v116, v116\n"
      "v_exp_f32 v117, v117\n"
      "v_exp_f32 v118, v118\n"
      "s_waitcnt lgkmcnt(1)\n"
      "v_smfmac_f32_32x32x32_bf16 %[o0], v[136:139], v[104:111], %[ix]\n"
      "v_exp_f32 v119, v119\n"
      "v_exp_f32 v120, v120\n"
      "v_exp_f32 v121, v121\n"
      "v_exp_f32 v122, v122\n"
      "v_smfmac_f32_32x32x32_bf16 %[o1], v[132:135], v[112:119], %[ix]\n"
      "ds_read_b128 v[132:135], %[va] offset:5120\n"
      "v_exp_f32 v123, v123\n"
      "v_exp_f32 v124, v124\n"
      "v_exp_f32 v125, v125\n"
      "v_exp_f32 v126, v126\n"
      "v_exp_f32 v127, v127\n"
      "s_setprio 3\n"
      "s_waitcnt lgkmcnt(1)\n"
      "v_mfma_f32_32x32x16_bf16 v[96:111], v[128:131], %[q0], 0\n"
      "v_exp_f32 v64, v64\n"
      "v_exp_f32 v65, v65\n"
      "v_exp_f32 v66, v66\n"
      "v_smfmac_f32_32x32x32_bf16 %[o1], v[136:139], v[120:127], %[ix]\n"
      "ds_read_b128 v[136:139], %[va] offset:5760\n"
      "v_exp_f32 v67, v67\n"
      "v_exp_f32 v68, v68\n"
      "v_exp_f32 v69, v69\n"
      "v_mfma_f32_32x32x16_bf16 v[112:127], v[128:131], %[q1], 0\n"
      "s_setprio 2\n"
      "s_waitcnt lgkmcnt(1)\n"
      "v_mfma_f32_32x32x16_bf16 v[64:79], v[128:131], %[q0], 0\n"
      "v_exp_f32 v96, v96\n"
      "v_exp_f32 v97, v97\n"
      "v_exp_f32 v98, v98\n"
      "v_smfmac_f32_32x32x32_bf16 %[o1], v[136:139], v[88:95], %[ix]\n"
      "ds_read_b128 v[136:139], %[va] offset:8320\n"
      "v_exp_f32 v99, v99\n"
      "v_exp_f32 v100, v100\n"
      "v_exp_f32 v101, v101\n"
      "v_mfma_f32_32x32x16_bf16 v[80:95], v[128:131], %[q1], 0\n"
      "ds_read_b128 v[128:131], %[ka] offset:21504\n"
      "v_exp_f32 v102, v102\n"
      "v_exp_f32 v103, v103\n"
      "v_exp_f32 v104, v104\n"
      "v_exp_f32 v105, v105\n"
      "s_waitcnt lgkmcnt(2)\n"
      "v_smfmac_f32_32x32x32_bf16 %[o0], v[132:135], v[96:103], %[ix]\n"
      "v_exp_f32 v106, v106\n"
      "v_exp_f32 v107, v107\n"
      "v_exp_f32 v108, v108\n"
      "v_exp_f32 v109, v109\n"
      "v_exp_f32 v110, v110\n"
      "v_mfma_f32_32x32x4_2b_bf16 v[64:95], v[140:141], %[qa], v[64:95]\n"
      "v_exp_f32 v111, v111\n"
      "v_exp_f32 v112, v112\n"
      "v_exp_f32 v113, v113\n"
      "v_exp_f32 v114, v114\n"
      "v_exp_f32 v115, v115\n"
      "v_exp_f32 v116, v116\n"
      "v_exp_f32 v117, v117\n"
      "v_exp_f32 v118, v118\n"
      "s_waitcnt lgkmcnt(1)\n"
      "v_smfmac_f32_32x32x32_bf16 %[o0], v[136:139], v[104:111], %[ix]\n"
      "v_exp_f32 v119, v119\n"
      "v_exp_f32 v120, v120\n"
      "v_exp_f32 v121, v121\n"
      "v_exp_f32 v122, v122\n"
      "v_smfmac_f32_32x32x32_bf16 %[o1], v[132:135], v[112:119], %[ix]\n"
      "ds_read_b128 v[132:135], %[va] offset:19456\n"
      "v_exp_f32 v123, v123\n"
      "v_exp_f32 v124, v124\n"
      "v_exp_f32 v125, v125\n"
      "v_exp_f32 v126, v126\n"
      "v_exp_f32 v127, v127\n"
      "v_add_u32 %[ka], 19456, %[ka]\n"
      "v_add_u32 %[va], 19456, %[va]\n"
      "v_add_u32 %[ta], 19456, %[ta]\n"
      "s_sub_u32 %[it], %[it], 1\n"
      "s_cmp_lg_u32 %[it], 0\n"
      "s_cbranch_scc1 1b\n"
      "v_smfmac_f32_32x32x32_bf16 %[o1], v[136:139], v[120:127], %[ix]\n"
      "s_setprio 0\n"
      "s_waitcnt lgkmcnt(0)\n"
      "s_nop 15\n"
          : [o0] "+v"(o0), [o1] "+v"(o1), [ka] "+v"(ka), [va] "+v"(va), [ta] "+v"(ta), [it] "+s"(it)
          : [q0] "v"(q0), [q1] "v"(q1), [qa] "v"(qa), [qc] "v"(qc), [ix] "v"(0xdddddddd)
          : "v64", "v65", "v66", "v67", "v68", "v69", "v70", "v71", "v72", "v73", "v74", "v75", "v76", "v77", "v78", "v79", "v80", "v81", "v82", "v83", "v84", "v85", "v86", "v87", "v88", "v89", "v90", "v91", "v92", "v93", "v94", "v95", "v96", "v97", "v98", "v99", "v100", "v101", "v102", "v103", "v104", "v105", "v106", "v107", "v108", "v109", "v110", "v111", "v112", "v113", "v114", "v115", "v116", "v117", "v118", "v119", "v120", "v121", "v122", "v123", "v124", "v125", "v126", "v127", "v128", "v129", "v130", "v131", "v132", "v133", "v134", "v135", "v136", "v137", "v138", "v139", "v140", "v141", "v142", "v143", "scc", "memory");
    }
    STAMP(13);
    const lptr xo = lds + C_OX + lane * 16;
    auto put_half = [&]() {
#pragma unroll
      for (int i = 0; i < 3; ++i) {
        const f32x4 a = {o0[4 * i], o0[4 * i + 1], o0[4 * i + 2], o0[4 * i + 3]}, c = {o1[4 * i], o1[4 * i + 1], o1[4 * i + 2], o1[4 * i + 3]};
        *(LAS f32x4*)(xo + (wid * 3 + i) * 1024) = kp ? a : c;
      }
    };
    put_half();
    {
      const bool bad = __any(!(qn2 * __uint_as_float(bctl[8]) <= 8000.f));
      if (lane == 0) bctl[wid] = bad ? 1u : 0u;
    }
    BAR();
    if (any_bad()) {
      BAR(); BAR();
      const bf16x8 qf0 = __builtin_bit_cast(bf16x8, q0), qf1 = __builtin_bit_cast(bf16x8, q1);
      float m0 = -INFINITY, m1 = -INFINITY;
#pragma unroll 1
      for (int i = 0; i < 32; ++i) {
        const int off = (i >> 2) * CH_BYTES;
        const f32x32 s = qk_tile((lptr)(uintptr_t)(ka0 + off + (i & 3) * 2048), qf0, qf1, *(LAS const unsigned*)(uintptr_t)(ta0 + off + (i & 3) * 4), qa);
#pragma unroll
        for (int j = 0; j < 16; ++j) { m0 = fmaxf(m0, s[j]); m1 = fmaxf(m1, s[16 + j]); }
      }
      m0 = fmaxf(m0, __shfl_xor(m0, 32)); m1 = fmaxf(m1, __shfl_xor(m1, 32));
      *(LAS f32x2*)(lds + SPARE + tid * 8) = (f32x2){m0, m1};
      BAR();
      const f32x2 mo = *(LAS const f32x2*)(lds + SPARE + (tid ^ 256) * 8);
      m0 = fmaxf(m0, mo[0]); m1 = fmaxf(m1, mo[1]);
#pragma unroll
      for (int j = 0; j < 16; ++j) { o0[j] = 0.f; o1[j] = 0.f; }
#pragma unroll 1
      for (int i = 0; i < 32; ++i) {
        const int off = (i >> 2) * CH_BYTES;
        f32x32 s = qk_tile((lptr)(uintptr_t)(ka0 + off + (i & 3) * 2048), qf0, qf1, *(LAS const unsigned*)(uintptr_t)(ta0 + off + (i & 3) * 4), qa);
#pragma unroll
        for (int j = 0; j < 16; ++j) { s[j] = __builtin_amdgcn_exp2f(s[j] - m0); s[16 + j] = __builtin_amdgcn_exp2f(s[16 + j] - m1); }
        const bf16x8 v0 = *(LAS const bf16x8*)(uintptr_t)(va0 + off + (i & 3) * 8 * V_OCT), v1 = *(LAS const bf16x8*)(uintptr_t)(va0 + off + ((i & 3) * 8 + 2) * V_OCT);
        o0 = SMF(v0, sub8(s, 0), o0); o0 = SMF(v1, sub8(s, 8), o0); o1 = SMF(v0, sub8(s, 16), o1); o1 = SMF(v1, sub8(s, 24), o1);
      }
      BAR();
      put_half();
      BAR();
    }
    float f[12];
#pragma unroll
    for (int i = 0; i < 3; ++i) {
      const f32x4 p = *(LAS const f32x4*)(xo + ((wid ^ 4) * 3 + i) * 1024);
#pragma unroll
      for (int j = 0; j < 4; ++j) f[4 * i + j] = (kp ? o1[4 * i + j] : o0[4 * i + j]) + p[j];
    }
    const auto sw = __builtin_amdgcn_permlane32_swap(__float_as_uint(f[10]), __float_as_uint(f[10]), false, false);
    const float inv = __builtin_amdgcn_rcpf(__uint_as_float(sw[0]));
    const char* ob = ws + WS_O + (qb * 4 + kp * 2) * 18432 + hg * 2304;
    const u32x4 ov = {nosent(pk2(f[0] * inv, f[1] * inv)), nosent(pk2(f[2] * inv, f[3] * inv)), nosent(pk2(f[4] * inv, f[5] * inv)), nosent(pk2(f[6] * inv, f[7] * inv))};
    asm volatile("global_store_dwordx4 %0, %1, %2 sc1\n\ts_nop 1" ::"v"(oo), "v"(ov), "s"(ob + head * 512) : "memory");
    const unsigned ot = nosent(pk2(f[8] * inv, f[9] * inv));
    if (h == 0) asm volatile("global_store_dword %0, %1, %2 offset:2048 sc1" ::"v"(oo), "v"(ot), "s"(ob + head * 4) : "memory");
    STAMP(11);
  } else {
    const int sv = wid - 8, chl = sv >> 1, half = sv & 1, rot = b & 7;
    const unsigned l16 = lane * 16;
    VMWAIT(0);
    if (sv == 3) touchs(1024 + (lane & 3) * (2u << 20), ws, lds0 + A_W - 256);
    const unsigned tag = __builtin_amdgcn_readfirstlane(TAG());
    const float dsk = bf2f(((LAS const u16*)(lds + msc + 16))[kch & 7]);
#define KVC(s0) do { kv_group(ws, lds0, (s0) + chl, rot, 2 * half, l16); kv_group(ws, lds0, (s0) + chl, rot, 2 * half + 1, l16);            \
                     if (half) kv_group(ws, lds0, (s0) + chl, rot, 4, l16); } while (0)
    const char* fp0 = ws + WS_F + (((rot + 2 * sv + 2 + h) & 7) * 32 + r32) * 128;
    u32x4 fl0;
    if (sv < 3) {
      __builtin_amdgcn_s_setprio(1);
      kg_in in; kg_read(in);
      STAMP(1);
      f32x4 d[2][2];
      const lptr tb = lds + A_SCR + 2 * sv * KG_TAB;
      kgen_half(in, tb, lane, d);
      VMWAIT(0);
      STAMP(2);
      lds_wait(&ctrl[16 + sv], 1u);
#pragma unroll
      for (int i = 0; i < 4; ++i) d[i >> 1][i & 1] += *(LAS const f32x4*)(tb + KG_TAB + i * 1024 + lane * 16);
      if (lane == 0) d[0][0][0] += dsk;
      kgen_finish(d, lds + CVB + sv * R_BYTES, &ctrl[5], tb, lane);
      asm volatile("global_load_dwordx4 %0, %1, off sc1\n\ts_waitcnt vmcnt(0)" : "=&v"(fl0) : "v"(fp0) : "memory");
      flag_wait<0>(fp0, tag, fl0);
      if (lane < 56) *(LAS u32x4*)(lds + CVB + CV_U + sv * CV_UCH + (lane < 28 ? lane * 16 : CV_U1 + (lane - 28) * 16)) = (u32x4){0u, 0u, 0u, 0u};
      STAMP(4);
      __builtin_amdgcn_s_setprio(3);
    } else {
      unsigned vo[9];
      slice_offsets(vo, lane);
      const char* wsrc = (const char*)Wq + hg * 144 - 4096;
      lds_wait(&ctrl[3], XLEAD);
      STAMP(1);
      __builtin_amdgcn_s_setprio(3);
#pragma unroll 1
      for (int a = 0; a < 9; ++a) {
        if (a >= 3 && (a & 1)) lds_wait(&ctrl[12 + (a >> 1)], 2u);
        glds9(vo, wsrc + a * 64 * ROWB, lds0 + A_W + a * 9216);
        if (a >= 2) { VMWAIT(18); LSET(4, (unsigned)(a - 1)); }
      }
      STAMP(3);
      VMWAIT(9); LSET(4, 8u);
      VMWAIT(0); LSET(4, 9u);
      STAMP(4);
      const unsigned tg = tag;
      u32x4 fu;
      const char* fup = ws + WS_F + (r32 * 8 + (h ? 7 : b / 36)) * 128;
      asm volatile("global_load_dwordx4 %0, %1, off sc1\n\ts_waitcnt vmcnt(0)" : "=&v"(fl0) : "v"(fp0) : "memory");
#pragma unroll 1
      for (int spin = 0; spin < SPIN_MAX; ++spin) {
        if (__all(h == 1 || fl0[0] == tg)) break;
        __builtin_amdgcn_s_sleep(POLL0);
        if (h == 0 && fl0[0] != tg) asm volatile("global_load_dwordx4 %0, %1, off sc1\n\ts_waitcnt vmcnt(0)" : "+v"(fl0) : "v"(fp0) : "memory");
      }
      STAMP(5);
      lds_wait(&ctrl[12], 2u);
#pragma unroll 1
      for (int i = 0; i < 5; ++i) kv_group(ws, lds0, 0, rot, i, l16);
      LD16(fu, fup, " sc1");
      VMWAIT(1);
      LSET(7, 1u);
      STAMP(7);
      VMWAIT(0); PIN(fu);
      flag_wait<0>(fp0, tg, fl0);
      STAMP(9);
#pragma unroll 1
      for (int i = 0; i < 4; ++i) kv_group(ws, lds0, 1, rot, i, l16);
      flag_wait<2>(fup, tg, fu);
      lds_wait(&ctrl[5], 8u);
#pragma unroll 1
      for (int i = 0; i < 6; ++i)
        glds4c(l16, ws + WS_UT + (size_t)(i < 4 ? 2 * b + (i >> 1) : ce) * UT_ROW + 128 - ((i & 1) ? 0 : 2), lds0 + CVB + CV_U + (i >> 1) * CV_UCH + (i & 1) * CV_U1 + U_PADB);
      lds_wait(&ctrl[6], 8u);
      kv_group(ws, lds0, 1, rot, 4, l16);
      STAMP(10);
    }
    {
      unsigned m = fl0[1];
      m = umax(m, dppu<0x128>(m));
      { const auto t = __builtin_amdgcn_permlane16_swap(m, m, false, false); m = umax(t[0], t[1]); }
      { const auto t = __builtin_amdgcn_permlane32_swap(m, m, false, false); m = umax(t[0], t[1]); }
      if (lane < 8) bctl[-32 + sv * 8 + lane] = m;
    }
    const int rr = lane >> 3;
    const char* wo = (const char*)Wo + hg * 128;
    const unsigned wsw = ((lane & 7) ^ (2 * ((rr >> 1) & 1))) * 16;
    auto wo_group = [&](int gq) {
      const unsigned va = (rr * ROWB + wsw) ^ ((gq & 1) * 64), vb = (va ^ 64) + 8 * ROWB - 1024, vt = ((18 * (rr >> 1) + 16 + (rr & 1)) * ROWB + wsw) ^ ((gq & 1) * 64);
      const char* sb = wo + gq * 72 * ROWB;
      unsigned sv_;
      asm volatile("s_mov_b32 %0, m0\n\ts_add_u32 m0, %8, 0x2000\n\ts_nop 0\n\tglobal_load_lds_dwordx4 %3, %4\n\t"
                   "s_mov_b32 m0, %8\n\ts_nop 0\n\tglobal_load_lds_dwordx4 %1, %4\n\tglobal_load_lds_dwordx4 %2, %4 offset:1024\n\t"
                   "s_add_u32 m0, m0, 0x800\n\ts_nop 0\n\tglobal_load_lds_dwordx4 %1, %5\n\tglobal_load_lds_dwordx4 %2, %5 offset:1024\n\t"
                   "s_add_u32 m0, m0, 0x800\n\ts_nop 0\n\tglobal_load_lds_dwordx4 %1, %6\n\tglobal_load_lds_dwordx4 %2, %6 offset:1024\n\t"
                   "s_add_u32 m0, m0, 0x800\n\ts_nop 0\n\tglobal_load_lds_dwordx4 %1, %7\n\tglobal_load_lds_dwordx4 %2, %7 offset:1024\n\ts_mov_b32 m0, %0"
                   : "=&s"(sv_) : "v"(va), "v"(vb), "v"(vt), "s"(sb), "s"(sb + 18 * ROWB), "s"(sb + 36 * ROWB), "s"(sb + 54 * ROWB), "s"(lds0 + gq * 9216)
                   : "memory", "scc");
    };
    auto wo_rem = [&]() {
#pragma unroll 1
      for (int a = sv; a < 9; a += 4) {
        const int O = 8 * a + rr, gq = O / 9, o = O - gq * 9, e8 = lane & 7;
        gldss((gq * 72 + (o < 8 ? 18 * (o >> 1) + 8 * (o & 1) + e8 : 18 * (e8 >> 1) + 16 + (e8 & 1))) * ROWB, (const char*)Wo + 1024 + hg * 16, lds0 + C_REM + a * 1024);
      }
    };
    const int rofs = CVB + (1023 - l15 + 4 * g) * 4, uofs = CVB + CV_U + (l15 & 1) * CV_U1 + U_PADB + (32 * (l15 >> 1) + 8 * g) * 2;
    f32x4 acc[8], ea = {0.f, 0.f, 0.f, 0.f}, eb = ea;
#pragma unroll
    for (int c = 0; c < 8; ++c) acc[c] = ea;
#pragma unroll 1
    for (int iv = 0; iv < 4; ++iv) {
      STAMP(16);
      BARVM(0);
      STAMP(24);
      if (iv < 2) { KVC(2 + 2 * iv); }
      if (iv == 0) touch((const char*)Wo + ((b >> 3) * 162 + min(sv * 41 + lane, 161)) * 128, lds0 + B_CTRL - 384);
      if (iv == 1 && sv == 3) {
        const int l7 = lane & 7;
        const unsigned m = umax(umax(bctl[-32 + l7], bctl[-24 + l7]), umax(bctl[-16 + l7], bctl[-8 + l7]));
        const float k2 = __uint_as_float(__builtin_amdgcn_readlane(m, 0)) + __uint_as_float(__builtin_amdgcn_readlane(m, 1)) +
                         __uint_as_float(__builtin_amdgcn_readlane(m, 2)) + __uint_as_float(__builtin_amdgcn_readlane(m, 3));
        const float vf = __uint_as_float(umax(umax(__builtin_amdgcn_readlane(m, 4), __builtin_amdgcn_readlane(m, 5)), umax(__builtin_amdgcn_readlane(m, 6), __builtin_amdgcn_readlane(m, 7))));
        if (lane == 0) bctl[8] = (vf <= 1048576.f && vf >= 9.5367431640625e-7f) ? __float_as_uint(k2) : 0x7fc00000u;
      }
      if (iv == 3 && sv < 3) wo_group(3 + sv);
      if (iv == 2) {
        char* p = ws + WS_O + (qb * 4 + sv) * 18432 + 9 * hg * 256 + l16;
        st16c(p, (u32x4){SENT, SENT, SENT, SENT}); st16c(p + 1024, (u32x4){SENT, SENT, SENT, SENT});
        if (lane < 16) st16c(p + 2048, (u32x4){SENT, SENT, SENT, SENT});
      }
      {
        const int m0 = 4 * half + iv;
        const lptr rb = lds + chl * R_BYTES + rofs - 3584 - m0 * 64, ub = lds + chl * CV_UCH + uofs - m0 * 64;
        bf16x8 uf[8], kf[8];
#pragma unroll
        for (int z = 0; z < 8; ++z) {
          LAS const unsigned* rp = (LAS const unsigned*)(rb + 512 * (7 - z));
          kf[z] = __builtin_bit_cast(bf16x8, (u32x4){rp[0], rp[1], rp[2], rp[3]});
          uf[z] = *(LAS const bf16x8*)(ub + z * 512);
        }
        CFENCE();
#pragma unroll
        for (int c = 0; c < 8; ++c)
#pragma unroll
          for (int m1 = 0; m1 <= c; ++m1) acc[c] = MFMA16(uf[c - m1], kf[m1], acc[c]);
      }
      {
        const int m0 = 2 * sv + (iv >> 1), cB = 7 - cq;
        const lptr rb = lds + 2 * R_BYTES + rofs - m0 * 64, ub = lds + 2 * CV_UCH + uofs - m0 * 64;
        if ((iv & 1) == 0) {
#pragma unroll
          for (int i = 0; i < 5; ++i) {
            LAS const unsigned* rp = (LAS const unsigned*)(rb - 512 * i);
            eb = MFMA16(*(LAS const bf16x8*)(ub + 512 * (cB - i)), __builtin_bit_cast(bf16x8, (u32x4){rp[0], rp[1], rp[2], rp[3]}), eb);
          }
        } else {
          bf16x8 uf[4], kf[4];
#pragma unroll
          for (int i = 5; i < 9; ++i) {
            const bool isB = i <= cB;
            const int m1 = isB ? i : i - cB - 1, z = (isB ? cB : cq) - m1;
            LAS const unsigned* rp = (LAS const unsigned*)(rb - 512 * m1);
            kf[i - 5] = __builtin_bit_cast(bf16x8, (u32x4){rp[0], rp[1], rp[2], rp[3]});
            uf[i - 5] = *(LAS const bf16x8*)(ub + 512 * z);
          }
#pragma unroll
          for (int i = 5; i < 9; ++i) {
            if (i < 8 && i <= cB) eb = MFMA16(uf[i - 5], kf[i - 5], eb);
            else ea = MFMA16(uf[i - 5], kf[i - 5], ea);
          }
        }
      }
    }
    STAMP(6);
    const int ec = chl ? 7 - cq : cq;
    const lptr sc = lds + CV_SCR + l16;
    if (half) {
#pragma unroll
      for (int i = 0; i < 4; ++i) { *(LAS f32x4*)(sc + (sv * 4 + i) * 1024) = acc[i]; acc[i] = acc[4 + i]; }
    } else {
#pragma unroll
      for (int i = 0; i < 4; ++i) *(LAS f32x4*)(sc + (sv * 4 + i) * 1024) = acc[4 + i];
    }
    if (sv != 0) *(LAS f32x4*)(sc + 16384 + (sv - 1) * 1024) = ea;
    if (sv != 2) *(LAS f32x4*)(sc + 19456 + (sv > 2 ? 2 : sv) * 1024) = eb;
    STAMP(20);
    BAR();
    STAMP(27);
    KVC(6);
    if (half) wo_group(6 + chl);
    {
      const unsigned yo = g * 128 + l15 * 4;
      const char* yb = ws + WS_YT + (2 * b + chl) * 4096 + half * 2048;
#define YST(base, off, v) asm volatile("global_store_dword %0, %1, %2 offset:" #off " sc1" ::"v"(yo), "v"(v), "s"(base) : "memory")
#define YTILE(i, o0, o1) do { const f32x4 f = acc[i] + *(LAS const f32x4*)(sc + ((sv ^ 1) * 4 + (i)) * 1024); YST(yb, o0, pk2(f[0], f[1])); YST(yb, o1, pk2(f[2], f[3])); } while (0)
      YTILE(0, 0, 64); YTILE(1, 512, 576); YTILE(2, 1024, 1088); YTILE(3, 1536, 1600);
      if (half == 0) {
        f32x4 e = chl ? eb : ea;
#pragma unroll
        for (int s = 0; s < 3; ++s) e += *(LAS const f32x4*)(sc + 16384 + chl * 3072 + s * 1024);
        const char* ye = ws + WS_YT + ce * 4096 + ec * 512;
        YST(ye, 0, pk2(e[0], e[1])); YST(ye, 64, pk2(e[2], e[3]));
      }
    }
    STAMP(21);
    BARVM(0);
    STAMP(28);
    if (wid == 8 && lane == 0) st4c(myflag + 12, tag);
    if (sv) wo_group(sv - 1);
    wo_rem();
    const int tq = (qb >> 2) < 4 ? (qb >> 2) : 7 - (qb >> 2);
    const int pb0 = 32 * hg + (lane < 32 ? lane : 4 * (lane & 7) + tq);
    const char* f2a = ws + WS_F + (lane >= 48 ? qb * 8 + (lane & 7) : ((pb0 & 7) * 4 + (pb0 >> 6)) * 8 + ((pb0 >> 3) & 7)) * 128;
    const char* f2b = ws + WS_F + (sv * 64 + lane) * 128;
    u32x4 fa, fb = {tag, tag, tag, tag};
    LD16(fa, f2a, " sc1");
    if (b == 0) asm volatile("global_load_dwordx4 %0, %1, off sc1" : "+v"(fb) : "v"(f2b) : "memory");
    STAMP(22);
    BAR();
    STAMP(29);
    VMWAIT(0);
    PIN(fa); PIN(fb);
    flag_wait<3>(f2a, tag, fa); flag_wait<3>(f2b, tag, fb);
    {
      const char* yp = ws + WS_YT + hg * 64 * 4096 + qb * 128 + sv * 32;
      const unsigned yv = (lane >> 1) * 4096 + (lane & 1) * 16;
      gldssc(yv, yp, lds0 + C_YT + sv * 2304); gldssc(yv, yp + 32 * 4096, lds0 + C_YT + sv * 2304 + 1024);
      if (lane < 16) gldssc(yv, yp + (512 - hg * 56) * 4096, lds0 + C_YT + sv * 2304 + 2048);
    }
    VMWAIT(0);
    {
      const lptr rp = lds + C_YT + sv * 2304 + (((4 * g + p4) & 7) * 8 + q4) * 32 + (g >> 1) * 8, re = lds + C_YT + sv * 2304 + 2048 + q4 * 32 + p4 * 8;
      u16* orow = out + (size_t)(qb * 64 + sv * 16 + (lane & 3)) * OUTLD;
      u16* om = orow + h * 4 * OUTLD + hg * 64 + ((lane >> 2) & 7) * 8;
      *(u32x4*)om = __builtin_bit_cast(u32x4, trd2(rp, rp + 128));
      *(u32x4*)(om + 8 * OUTLD) = __builtin_bit_cast(u32x4, trd2(rp + 16, rp + 144));
      const u32x4 ve = __builtin_bit_cast(u32x4, trd2(re, re + 128));
      if (lane < 16) *(u32x4*)(orow + q4 * 4 * OUTLD + 512 + hg * 8) = ve;
    }
    STAMP(23);
    BAR();
    STAMP(12);
    if (b == 0 && wid == 8 && lane == 0) st4c(ws + WS_E, tag);
    __builtin_amdgcn_s_setprio(0);
    VMWAIT(0);
    STAMP(13);
    BAR();
    if (any_bad()) { BAR(); _Pragma("unroll 1") for (int i = 0; i < 8; i += 2) KVC(i); BARVM(0); BAR(); BAR(); wo_group(sv); wo_group(sv + 4); wo_rem(); BARVM(0); }
    __builtin_amdgcn_s_sleep(OSLEEP);
  }
  {
    FRESH_LANE();
    const int kt = wid >> 2;
    bf16x8 wf[6][4];
    {
      const int sw = 2 * (((q4 >> 1) & 1) | ((g & 1) << 1));
      const lptr wb = lds + kt * 24576 + (g * 8 + q4) * 128 + (p4 & 1) * 8;
#pragma unroll
      for (int nt = 0; nt < 4; ++nt) {
        const lptr p = wb + (((2 * nt + (p4 >> 1)) ^ sw) * 16);
#pragma unroll
        for (int kk = 0; kk < 6; ++kk) wf[kk][nt] = trd2(p + kk * 4096, p + kk * 4096 + 512);
      }
    }
    VMWAIT(0);
    if (CSLEEP) __builtin_amdgcn_s_sleep(CSLEEP);
    const lptr wr = lds + C_REM + kt * 3072 + (g * 8 + q4) * 16 + (p4 & 1) * 8;
    STAMP(14);
    const char* op = ws + WS_O + (size_t)((qb * 4 + mt) * 18 + kt * 6) * 1024 + lane * 16;
    u32x4 of0, of1, of2, of3, of4, of5;
    const char* op2 = op + 3072;
    asm volatile("global_load_dwordx4 %0, %6, off sc1\n\tglobal_load_dwordx4 %1, %6, off offset:1024 sc1\n\t"
                 "global_load_dwordx4 %2, %6, off offset:2048 sc1\n\tglobal_load_dwordx4 %3, %7, off sc1\n\t"
                 "global_load_dwordx4 %4, %7, off offset:1024 sc1\n\tglobal_load_dwordx4 %5, %7, off offset:2048 sc1\n\ts_waitcnt vmcnt(0)"
                 : "=&v"(of0), "=&v"(of1), "=&v"(of2), "=&v"(of3), "=&v"(of4), "=&v"(of5) : "v"(op), "v"(op2) : "memory");
#define MX4(x) umax(umax(x[0], x[1]), umax(x[2], x[3]))
#pragma unroll 1
    for (int spin = 0; spin < SPIN_MAX; ++spin) {
      const bool m0 = umax(umax(MX4(of0), MX4(of1)), MX4(of2)) == SENT, m1 = umax(umax(MX4(of3), MX4(of4)), MX4(of5)) == SENT;
      if (!__any(m0 || m1)) break;
      __builtin_amdgcn_s_sleep(ORETRY);
      if (m0) asm volatile("global_load_dwordx4 %0, %3, off sc1\n\tglobal_load_dwordx4 %1, %3, off offset:1024 sc1\n\t"
                           "global_load_dwordx4 %2, %3, off offset:2048 sc1\n\ts_waitcnt vmcnt(0)" : "+v"(of0), "+v"(of1), "+v"(of2) : "v"(op) : "memory");
      if (m1) asm volatile("global_load_dwordx4 %0, %3, off sc1\n\tglobal_load_dwordx4 %1, %3, off offset:1024 sc1\n\t"
                           "global_load_dwordx4 %2, %3, off offset:2048 sc1\n\ts_waitcnt vmcnt(0)" : "+v"(of3), "+v"(of4), "+v"(of5) : "v"(op2) : "memory");
    }
    const u32x4 of[6] = {of0, of1, of2, of3, of4, of5};
    STAMP(15);
    f32x4 acc[5];
#pragma unroll
    for (int nt = 0; nt < 5; ++nt) acc[nt] = (f32x4){0.f, 0.f, 0.f, 0.f};
#pragma unroll
    for (int kk = 0; kk < 6; ++kk) {
#pragma unroll
      for (int nt = 0; nt < 4; ++nt) acc[nt] = MFMA16(wf[kk][nt], __builtin_bit_cast(bf16x8, of[kk]), acc[nt]);
      acc[4] = MFMA16(trd2(wr + kk * 512, wr + kk * 512 + 64), __builtin_bit_cast(bf16x8, of[kk]), acc[4]);
    }
    const lptr rd = lds + C_RED2 + mt * 5120 + lane * 16, rd2 = lds + C_RED + mt * 5120 + lane * 16;
#pragma unroll
    for (int nt = 0; nt < 5; ++nt) {
      const int own = nt >> 1;
      if (kt != own) *(LAS f32x4*)(((kt + 1) % 3 == own ? rd : rd2) + nt * 1024) = acc[nt];
    }
    BAR();

    u16* cp = out + (size_t)(qb * 64 + mt * 16 + l15) * OUTLD + DM + g * 4 + (kt < 2 ? hg * 64 + kt * 32 : 512 + hg * 8);
    const f32x4 a0 = kt == 0 ? acc[0] : (kt == 1 ? acc[2] : acc[4]), a1 = kt == 0 ? acc[1] : acc[3];
    const f32x4 v = a0 + *(LAS const f32x4*)(rd + kt * 2048) + *(LAS const f32x4*)(rd2 + kt * 2048);
    if (kt < 2 || g < 2) *(u32x2*)cp = (u32x2){pk2(v[0], v[1]), pk2(v[2], v[3])};
    if (kt < 2) {
      const f32x4 w = a1 + *(LAS const f32x4*)(rd + kt * 2048 + 1024) + *(LAS const f32x4*)(rd2 + kt * 2048 + 1024);
      *(u32x2*)(cp + 16) = (u32x2){pk2(w[0], w[1]), pk2(w[2], w[3])};
    }
  }
}

extern "C" void kernel_launch(void* const* d_in, const int* in_sizes, int n_in, void* d_out, int out_size, void* d_ws, size_t ws_size,
                              hipStream_t stream) {
  fused_kernel<<<256, 768, 0, stream>>>((const u16*)d_in[0], (const u16*)d_in[1], (const u16*)d_in[2], (const u16*)d_in[3], (const u16*)d_in[4],
                                        (const u16*)d_in[5], (const u16*)d_in[6], (const u16*)d_in[7], (const u16*)d_in[8], (const u16*)d_in[9],
                                        (const u16*)d_in[10], (const u16*)d_in[11], (const u16*)d_in[12], (const u16*)d_in[13], (u16*)d_out,
                                        (char*)d_ws);
}
```

```cpp
#include <hip/hip_runtime.h>
#include <hip/hip_bf16.h>
#include <stdint.h>

typedef __bf16 bf16x8 __attribute__((ext_vector_type(8)));
typedef __bf16 bf16x16 __attribute__((ext_vector_type(16)));
typedef __bf16 bf16x2 __attribute__((ext_vector_type(2)));
typedef short s16x4 __attribute__((ext_vector_type(4)));
typedef short s16x8 __attribute__((ext_vector_type(8)));
typedef float f32x2 __attribute__((ext_vector_type(2)));
typedef float f32x4 __attribute__((ext_vector_type(4)));
typedef float f32x8 __attribute__((ext_vector_type(8)));
typedef float f32x16 __attribute__((ext_vector_type(16)));
typedef float f32x32 __attribute__((ext_vector_type(32)));
typedef unsigned u32x2 __attribute__((ext_vector_type(2)));
typedef unsigned u32x4 __attribute__((ext_vector_type(4)));
typedef unsigned short u16;
#define LAS __attribute__((address_space(3)))
#define DI __device__ __forceinline__
typedef LAS char* lptr;

constexpr int SEQ = 2048, DM = 576, NS = 128, OUTLD = 2 * DM, ROWB = DM * 2;
constexpr int CH_BYTES = 19456, KV_BYTES = 8 * CH_BYTES, CH_TAIL = 8192, CH_V = 9216, V_OCT = 320;
constexpr int R_BYTES = 4160, CV_U = 13312, CV_UCH = 4640 + 4544, CV_U1 = 4640, U_PADB = 448, UT_ROW = 4224;
constexpr size_t WS_E = 0, WS_F = 4096, WS_KV = WS_F + 256 * 128;
constexpr size_t WS_UT = WS_KV + KV_BYTES;
constexpr size_t WS_YT = WS_UT + (size_t)DM * UT_ROW;
constexpr size_t WS_O = WS_YT + (size_t)DM * SEQ * 2;
constexpr size_t WS_DBG = 64u << 20;
constexpr int LDS_BYTES = 163840, KV_LDS = 0, SPARE = KV_BYTES, CVB = 6 * CH_BYTES;
constexpr int A_UT = 0, A_WKV = 9216, A_X0 = 18432, A_Q = 36864, Q_ROW = 144, A_CTRL = A_Q + 64 * Q_ROW, A_MISC = A_CTRL + 128, A_SCR = A_MISC + 144, A_W = 80896, B_CTRL = LDS_BYTES - 64;
constexpr int CV_SCR = 0, C_REM = 73728, C_YT = 82944, C_RED = C_YT + 9216, C_OX = C_RED + 20480, C_RED2 = C_OX + 24576;
static_assert(A_SCR + 6 * 5712 <= A_W - 256 && A_SCR % 16 == 0 && A_W + 82944 == LDS_BYTES && CVB + CV_U + 3 * CV_UCH <= B_CTRL - 384 && C_OX + 24576 <= 7 * CH_BYTES + 1024 &&
              C_RED2 + 20480 <= B_CTRL - 384, "LDS map");
constexpr float QSCALE = 0.23570226039551584f * 1.4426950408889634f;
constexpr unsigned SENT = 0xffffffffu;
constexpr int SPIN_MAX = 1 << 16;
#define POLL1 12
#define POLL0 8
#define XLEAD 0u
#define CSLEEP 8
#define OSLEEP 30
#define ORETRY 8

DI float bf2f(u16 x) { return __uint_as_float((unsigned)x << 16); }
DI float bflo(unsigned x) { return __uint_as_float(x << 16); }
DI float bfhi(unsigned x) { return __uint_as_float(x & 0xffff0000u); }
DI unsigned pk2(float lo, float hi) { f32x2 v = {lo, hi}; return __builtin_bit_cast(unsigned, __builtin_convertvector(v, bf16x2)); }
DI float rbf(float x) { return bfhi(pk2(x, x)); }
DI unsigned nosent(unsigned x) { return x < 0xfffffffeu ? x : 0xfffffffeu; }
#define GLDS(SC) "s_mov_b32 %0, m0\n\ts_mov_b32 m0, %2\n\ts_nop 0\n\tglobal_load_lds_dwordx4 %1, off" SC "\n\ts_mov_b32 m0, %0"
DI void glds16(const void* g, unsigned l) { unsigned sv; asm volatile(GLDS("") : "=&s"(sv) : "v"(g), "s"(l) : "memory"); }
DI void glds16c(const void* g, unsigned l) { unsigned sv; asm volatile(GLDS(" sc1") : "=&s"(sv) : "v"(g), "s"(l) : "memory"); }
#define GLDSS(SC) "s_mov_b32 %0, m0\n\ts_mov_b32 m0, %3\n\ts_nop 0\n\tglobal_load_lds_dwordx4 %1, %2" SC "\n\ts_mov_b32 m0, %0"
DI void gldss(unsigned voff, const void* sbase, unsigned l) { unsigned sv; asm volatile(GLDSS("") : "=&s"(sv) : "v"(voff), "s"(sbase), "s"(l) : "memory"); }
DI void gldssc(unsigned voff, const void* sbase, unsigned l) { unsigned sv; asm volatile(GLDSS(" sc1") : "=&s"(sv) : "v"(voff), "s"(sbase), "s"(l) : "memory"); }
DI void glds9(const unsigned (&vo)[9], const void* sbase, unsigned l) {
  unsigned sv;
  asm volatile("s_mov_b32 %0, m0\n\ts_mov_b32 m0, %11\n\ts_nop 0\n\t"
               "global_load_lds_dwordx4 %1, %10\n\tglobal_load_lds_dwordx4 %2, %10 offset:1024\n\t"
               "global_load_lds_dwordx4 %3, %10 offset:2048\n\tglobal_load_lds_dwordx4 %4, %10 offset:3072\n\t"
               "s_add_u32 m0, m0, 0x1000\n\ts_nop 0\n\t"
               "global_load_lds_dwordx4 %5, %10\n\tglobal_load_lds_dwordx4 %6, %10 offset:1024\n\t"
               "global_load_lds_dwordx4 %7, %10 offset:2048\n\tglobal_load_lds_dwordx4 %8, %10 offset:3072\n\t"
               "s_add_u32 m0, m0, 0x1000\n\ts_nop 0\n\tglobal_load_lds_dwordx4 %9, %10\n\ts_mov_b32 m0, %0"
               : "=&s"(sv) : "v"(vo[0]), "v"(vo[1]), "v"(vo[2]), "v"(vo[3]), "v"(vo[4]), "v"(vo[5]), "v"(vo[6]), "v"(vo[7]), "v"(vo[8]),
                 "s"(sbase), "s"(l) : "memory", "scc");
}
DI void glds4c(unsigned voff, const void* sbase, unsigned l) {
  unsigned sv;
  asm volatile("s_mov_b32 %0, m0\n\ts_mov_b32 m0, %3\n\ts_nop 0\n\t"
               "global_load_lds_dwordx4 %1, %2 sc1\n\tglobal_load_lds_dwordx4 %1, %2 offset:1024 sc1\n\t"
               "global_load_lds_dwordx4 %1, %2 offset:2048 sc1\n\tglobal_load_lds_dwordx4 %1, %2 offset:3072 sc1\n\ts_mov_b32 m0, %0"
               : "=&s"(sv) : "v"(voff), "s"(sbase), "s"(l) : "memory");
}
DI void touch(const void* g, unsigned junk) {
  unsigned sv;
  asm volatile("s_mov_b32 %0, m0\n\ts_mov_b32 m0, %2\n\ts_nop 0\n\tglobal_load_lds_dword %1, off sc1\n\ts_mov_b32 m0, %0" : "=&s"(sv) : "v"(g), "s"(junk) : "memory");
}
template <int W> DI void flag_wait(const char* p, unsigned tag, u32x4& v) {
  if (__builtin_expect(!__all(v[W] == tag), 0)) {
#pragma unroll 1
    for (int spin = 0; spin < SPIN_MAX; ++spin) {
      __builtin_amdgcn_s_sleep(POLL1);
      if (v[W] != tag) asm volatile("global_load_dwordx4 %0, %1, off sc1\n\ts_waitcnt vmcnt(0)" : "+v"(v) : "v"(p) : "memory");
      if (__all(v[W] == tag)) break;
    }
  }
}
DI void touchs(unsigned voff, const void* sbase, unsigned junk) {
  unsigned sv;
  asm volatile("s_mov_b32 %0, m0\n\ts_mov_b32 m0, %3\n\ts_nop 0\n\tglobal_load_lds_dword %1, %2 sc1\n\ts_mov_b32 m0, %0" : "=&s"(sv) : "v"(voff), "s"(sbase), "s"(junk) : "memory");
}
DI void slice_offsets(unsigned (&vo)[9], int lane) {
#pragma unroll
  for (int r = 0; r < 9; ++r) {
    const int c = r * 64 + lane, row = c / 9, src = (row & ~7) + 4 * (row & 1) + ((row >> 1) & 3);
    vo[r] = src * ROWB + (c - row * 9) * 16 + 4096 - (r & 3) * 1024;
  }
}
DI void kv_group(const char* ws, unsigned lds0, int slot, int rot, int grp, unsigned l16) {
  const int off = grp < 4 ? grp * 4096 : 15360;
  glds4c(l16, ws + WS_KV + ((slot + rot) & 7) * CH_BYTES + off, lds0 + KV_LDS + slot * CH_BYTES + off);
}
DI void lds_wait(LAS unsigned* p, unsigned n) {
  if (__builtin_expect(__builtin_amdgcn_readfirstlane(__hip_atomic_load(p, __ATOMIC_RELAXED, __HIP_MEMORY_SCOPE_WORKGROUP)) < n, 0)) {
    do __builtin_amdgcn_s_sleep(1); while (__builtin_amdgcn_readfirstlane(__hip_atomic_load(p, __ATOMIC_RELAXED, __HIP_MEMORY_SCOPE_WORKGROUP)) < n);
  }
  asm volatile("" ::: "memory");
}
DI void st16c(void* p, u32x4 v) { asm volatile("global_store_dwordx4 %0, %1, off sc1\n\ts_nop 1" ::"v"(p), "v"(v) : "memory"); }
#define OST ""
DI void ost8(void* p, u32x2 v) { asm volatile("global_store_dwordx2 %0, %1, off" OST ::"v"(p), "v"(v) : "memory"); }
DI void ost16(void* p, u32x4 v) { asm volatile("global_store_dwordx4 %0, %1, off" OST "\n\ts_nop 1" ::"v"(p), "v"(v) : "memory"); }
DI void st8c(void* p, u32x2 v) { asm volatile("global_store_dwordx2 %0, %1, off sc1" ::"v"(p), "v"(v) : "memory"); }
DI void st4c(void* p, unsigned v) { asm volatile("global_store_dword %0, %1, off sc1" ::"v"(p), "v"(v) : "memory"); }
#define LD4(d, p, SC) asm volatile("global_load_dword %0, %1, off" SC : "=v"(d) : "v"(p) : "memory")
#define LD8(d, p, SC) asm volatile("global_load_dwordx2 %0, %1, off" SC : "=v"(d) : "v"(p) : "memory")
#define LD16(d, p, SC) asm volatile("global_load_dwordx4 %0, %1, off" SC : "=v"(d) : "v"(p) : "memory")
#define PIN(x) asm volatile("" : "+v"(x))
#define VMWAIT(n) do { asm volatile("s_waitcnt vmcnt(" #n ")" ::: "memory"); __builtin_amdgcn_sched_barrier(0); } while (0)
#define BAR() asm volatile("s_waitcnt lgkmcnt(0)\n\ts_barrier" ::: "memory")
#define BARVM(n) asm volatile("s_waitcnt vmcnt(" #n ") lgkmcnt(0)\n\ts_barrier" ::: "memory")
#define CFENCE() asm volatile("" ::: "memory")
DI s16x4 trd(lptr p) { return __builtin_amdgcn_ds_read_tr16_b64_v4i16((LAS s16x4*)p); }
DI bf16x8 trd2(lptr a, lptr b) { return __builtin_bit_cast(bf16x8, __builtin_shufflevector(trd(a), trd(b), 0, 1, 2, 3, 4, 5, 6, 7)); }
#define MFMA16(a, b, c) __builtin_amdgcn_mfma_f32_16x16x32_bf16((a), (b), (c), 0, 0, 0)
#define MFMA32(a, b, c) __builtin_amdgcn_mfma_f32_32x32x16_bf16((a), (b), (c), 0, 0, 0)
#define SMF(a, b, c) __builtin_amdgcn_smfmac_f32_32x32x32_bf16((a), (b), (c), (int)0xdddddddd, 0, 0)
template <int CTRL> DI unsigned dppu(unsigned x) { return (unsigned)__builtin_amdgcn_update_dpp((int)x, (int)x, CTRL, 0xf, 0xf, false); }
DI unsigned umax(unsigned a, unsigned b) { return a > b ? a : b; }
DI unsigned max8(unsigned x) { x = umax(x, dppu<0xB1>(x)); x = umax(x, dppu<0x4E>(x)); return umax(x, dppu<0x141>(x)); }
DI unsigned max16(unsigned x) { x = max8(x); return umax(x, dppu<0x140>(x)); }

#define STAMP(i)

DI f32x2 cmul(f32x2 a, f32x2 b) {
  const f32x2 t = (f32x2){a[0], a[0]} * b;
  return __builtin_elementwise_fma((f32x2){a[1], a[1]}, (f32x2){-b[1], b[0]}, t);
}
DI f32x2 cpow(float zr, float zi, float c) {
  const float e = __builtin_amdgcn_exp2f(zr * c), th = __builtin_amdgcn_fractf(zi * c);
  return e * (f32x2){__builtin_amdgcn_cosf(th), __builtin_amdgcn_sinf(th)};
}
constexpr int KG_ROW = 272, KG_TAB = 21 * KG_ROW;
struct kg_in { unsigned are, aim, bre, bim, cre, cim, ldt; };
DI void kgen_half(const kg_in& in, lptr tb, int lane, f32x4 (&d)[2][2]) {
  const float dt = rbf(expf(bflo(in.ldt)));
  const float zr = (dt * bflo(in.are)) * 1.4426950408889634f, zi = (dt * bflo(in.aim)) * 0.15915494309189535f;
  const f32x2 dB = {dt * bflo(in.bre), dt * bflo(in.bim)}, Cc = {bflo(in.cre), bflo(in.cim)};
  lptr tw = tb + lane * 4;
#define PUT(row, v) do { const f32x2 v_ = (v); *(LAS unsigned*)(tw + (row) * KG_ROW) = pk2(v_[0], v_[1]); } while (0)
  const f32x2 w1 = cpow(zr, -zi, 1.f), w8 = cpow(zr, zi, 8.f), w256 = cpow(zr, -zi, 256.f);
  const f32x2 w2 = cmul(w1, w1), w3 = cmul(w2, w1), w4 = cmul(w2, w2), w6 = cmul(w2, w4);
  *(LAS unsigned*)tw = 0x3f80u;
  PUT(1, w1); PUT(2, w2); PUT(3, w3); PUT(4, w4); PUT(5, cmul(w1, w4)); PUT(6, w6); PUT(7, cmul(w3, w4));
  PUT(8, w256); PUT(9, cmul(w2, w256)); PUT(10, cmul(w4, w256)); PUT(11, cmul(w6, w256));
  const f32x2 w512 = cmul(w256, w256), w768 = cmul(w512, w256), w1024 = cmul(w768, w256);
  PUT(12, w512); PUT(13, cmul(w512, w4)); PUT(14, w768); PUT(15, cmul(w768, w4));
  PUT(16, w1024); PUT(17, cmul(w256, w1024)); PUT(18, cmul(w512, w1024)); PUT(19, cmul(w768, w1024)); PUT(20, cmul(w1024, w1024));
  CFENCE();
  const int l15 = lane & 15, g = lane >> 4;
  lptr fb = tb + l15 * KG_ROW + g * 16, fb1 = tb + (16 + min(l15, 4)) * KG_ROW + g * 16;
  bf16x8 a0[4], a1[4];
#pragma unroll
  for (int kk = 0; kk < 4; ++kk) { a0[kk] = *(LAS const bf16x8*)(fb + kk * 64); a1[kk] = *(LAS const bf16x8*)(fb1 + kk * 64); }
  CFENCE();
  const f32x2 w16 = cmul(w8, w8), w32 = cmul(w16, w16), w64 = cmul(w32, w32);
  f32x2 bb[8];
  bb[0] = cmul(Cc, dB); bb[1] = cmul(bb[0], w8); bb[2] = cmul(bb[0], w16); bb[3] = cmul(bb[1], w16);
#pragma unroll
  for (int i = 0; i < 4; ++i) bb[4 + i] = cmul(bb[i], w32);
#pragma unroll
  for (int ct = 0; ct < 2; ++ct) {
#pragma unroll
    for (int i = 0; i < 2; ++i) {
#pragma unroll
      for (int j = 0; j < 8; ++j) PUT(8 * i + j, bb[j]);
      if (ct == 0 || i == 0) {
#pragma unroll
        for (int j = 0; j < 8; ++j) bb[j] = cmul(bb[j], w64);
      }
    }
    CFENCE();
    bf16x8 bf[4];
#pragma unroll
    for (int kk = 0; kk < 4; ++kk) bf[kk] = *(LAS const bf16x8*)(fb + kk * 64);
    CFENCE();
    d[ct][0] = (f32x4){0.f, 0.f, 0.f, 0.f}; d[ct][1] = d[ct][0];
#pragma unroll
    for (int kk = 0; kk < 4; ++kk) { d[ct][0] = MFMA16(a0[kk], bf[kk], d[ct][0]); d[ct][1] = MFMA16(a1[kk], bf[kk], d[ct][1]); }
  }
}
DI void kgen_finish(const f32x4 (&d)[2][2], lptr img, LAS unsigned* gate, lptr tb, int lane) {
  const int l15 = lane & 15, g = lane >> 4;
  {
    const int e0 = g < 2 ? 8 * l15 + 4 * g : (g == 2 ? 256 + 4 * l15 : 384 + 2 * l15), es = g < 2 ? 128 : (g == 2 ? 64 : 32), e1 = g == 3 ? 64 : 2;
    const int f0 = g == 0 ? 512 + l15 : (lane == 16 ? 640 : 700);
#pragma unroll
    for (int ct = 0; ct < 2; ++ct) {
      const lptr p = tb + (e0 + ct * es) * 2;
      *(LAS unsigned*)p = pk2(d[ct][0][0], d[ct][0][1]); *(LAS unsigned*)(p + e1 * 2) = pk2(d[ct][0][2], d[ct][0][3]);
      LAS u16* q = (LAS u16*)tb + f0 + ct * 16;
      const unsigned x0 = pk2(d[ct][1][0], d[ct][1][1]), x1 = pk2(d[ct][1][2], d[ct][1][3]);
      q[0] = (u16)x0; q[32] = (u16)(x0 >> 16); q[64] = (u16)x1; q[96] = (u16)(x1 >> 16);
    }
  }
  lds_wait(gate, 8u);
  LAS const u16* C = (LAS const u16*)tb;
  const int rl = 63 - lane;
#pragma unroll
  for (int r = 0; r < 2; ++r) {
    const unsigned a = C[512 + 64 * (1 - r) + rl], bb = C[513 + 64 * (1 - r) + rl];
    const unsigned aa = a | (a << 16), b2x = bb | (bb << 16);
    *(LAS u32x4*)(img + r * 1024 + lane * 16) = (u32x4){b2x, (rl & 1) ? b2x : (bb | (a << 16)), aa, aa};
  }
  {
    const unsigned ab = *(LAS const unsigned*)(C + 384 + 2 * rl), c = C[386 + 2 * rl];
    *(LAS u32x4*)(img + 2048 + lane * 16) = (u32x4){c | (c << 16), __builtin_amdgcn_perm(ab, ab, 0x03020302u), __builtin_amdgcn_perm(ab, ab, 0x01000302u),
                                                __builtin_amdgcn_perm(ab, ab, 0x01000100u)};
  }
  {
    const bool hi = lane < 32;
    const int e = hi ? 128 + 4 * rl : 8 * rl;
    const u32x2 w01 = *(LAS const u32x2*)(C + e), w23 = *(LAS const u32x2*)(C + e + 4);
    const unsigned o0 = hi ? __builtin_amdgcn_perm(w01[1], w23[0], 0x07060100u) : __builtin_amdgcn_perm(w23[1], w23[1], 0x01000302u);
    const unsigned o1 = __builtin_amdgcn_perm(w23[0], w01[1], hi ? 0x01000100u : 0x05040706u);
    const unsigned o2 = __builtin_amdgcn_perm(w01[1], w01[0], hi ? 0x03020504u : 0x05040706u);
    const unsigned o3 = __builtin_amdgcn_perm(w01[0], w01[0], hi ? 0x01000100u : 0x01000302u);
    *(LAS u32x4*)(img + 3072 + lane * 16) = (u32x4){o0, o1, o2, o3};
  }
  if (lane < 4) *(LAS u32x4*)(img + 4096 + lane * 16) = (u32x4){0u, 0u, 0u, 0u};
}

template <int NT> DI void qgemm(lptr lds, LAS unsigned* ctrl, const bf16x8 (&xa)[18], int mt, int lane) {
  const int l15 = lane & 15, g = lane >> 4;
  constexpr int nt0 = NT == 2 ? 3 : 0;
  f32x4 acc[NT];
#pragma unroll
  for (int i = 0; i < NT; ++i) acc[i] = (f32x4){0.f, 0.f, 0.f, 0.f};
  const lptr wq = lds + A_W + (g * 8 + (l15 >> 2) * 2) * 144 + (l15 & 3) * 8 + nt0 * 32;
  constexpr int PD = NT == 3 ? 4 : 6;
  bf16x8 wf[PD + 1][NT];
  unsigned stage = __builtin_amdgcn_readfirstlane(__hip_atomic_load(&ctrl[4], __ATOMIC_RELAXED, __HIP_MEMORY_SCOPE_WORKGROUP));
#define WNEED(n) do { if (__builtin_expect(stage < (n), 0)) {                                                                             \
      do stage = __builtin_amdgcn_readfirstlane(__hip_atomic_load(&ctrl[4], __ATOMIC_RELAXED, __HIP_MEMORY_SCOPE_WORKGROUP)); while (stage < (n)); } \
    CFENCE(); } while (0)
#pragma unroll
  for (int kk = 0; kk < 18 + PD; ++kk) {
    if (kk >= PD) {
#pragma unroll
      for (int i = 0; i < NT; ++i) acc[i] = MFMA16(wf[(kk - PD) % (PD + 1)][i], xa[kk - PD], acc[i]);
    }
    __builtin_amdgcn_sched_barrier(0);
    if (kk < 18) {
      if ((kk & 1) == 0) WNEED((unsigned)(kk / 2 + 1));
#pragma unroll
      for (int i = 0; i < NT; ++i) wf[kk % (PD + 1)][i] = trd2(wq + kk * 4608 + i * 32, wq + kk * 4608 + 144 + i * 32);
    }
    __builtin_amdgcn_sched_barrier(0);
  }
  LAS unsigned* qw = (LAS unsigned*)(lds + A_Q + (mt * 16 + l15) * Q_ROW + nt0 * 32 + g * 8);
#pragma unroll
  for (int i = 0; i < NT; ++i)
    if (nt0 + i < 4 || g < 2) *(LAS u32x2*)(qw + 8 * i) = (u32x2){pk2(acc[i][0] * QSCALE, acc[i][1] * QSCALE), pk2(acc[i][2] * QSCALE, acc[i][3] * QSCALE)};
  CFENCE();
  if (lane == 0) __hip_atomic_fetch_add(&ctrl[5], 1u, __ATOMIC_RELAXED, __HIP_MEMORY_SCOPE_WORKGROUP);
}

DI f32x32 qk_tile(lptr kp, const bf16x8& q0, const bf16x8& q1, unsigned kt, u32x2 qa) {
  const bf16x8 k = *(LAS const bf16x8*)kp;
  f32x16 z;
#pragma unroll
  for (int j = 0; j < 16; ++j) z[j] = 0.f;
  const f32x16 a = MFMA32(k, q0, z), b = MFMA32(k, q1, z);
  f32x32 s = __builtin_shufflevector(a, b, 0, 1, 2, 3, 4, 5, 6, 7, 8, 9, 10, 11, 12, 13, 14, 15, 16, 17, 18, 19, 20, 21, 22, 23, 24, 25, 26, 27, 28, 29, 30, 31);
  return __builtin_amdgcn_mfma_f32_32x32x4bf16_1k(__builtin_bit_cast(s16x4, (u32x2){kt, 0u}), __builtin_bit_cast(s16x4, qa), s, 0, 0, 0);
}
DI bf16x16 sub8(const f32x32& s, int o) {
  f32x8 t = {s[o], s[o + 1], s[o + 2], s[o + 3], s[o + 4], s[o + 5], s[o + 6], s[o + 7]};
  return __builtin_bit_cast(bf16x16, t);
}

__global__ __launch_bounds__(768, 3) void fused_kernel(const u16* __restrict__ U, const u16* __restrict__ Are, const u16* __restrict__ Aim,
                                                       const u16* __restrict__ Bre, const u16* __restrict__ Bim, const u16* __restrict__ Cre,
                                                       const u16* __restrict__ Cim, const u16* __restrict__ logdt, const u16* __restrict__ Dskip,
                                                       const u16* __restrict__ X, const u16* __restrict__ Wq, const u16* __restrict__ Wk,
                                                       const u16* __restrict__ Wv, const u16* __restrict__ Wo, u16* __restrict__ out,
                                                       char* __restrict__ ws) {
  __shared__ __attribute__((aligned(16))) char lds_raw[LDS_BYTES];
  const lptr lds = (lptr)lds_raw;
  const unsigned lds0 = (unsigned)(uintptr_t)lds_raw;
  STAMP(0);
  const int tid = threadIdx.x, lane = tid & 63, wid = __builtin_amdgcn_readfirstlane(tid >> 6);
  const int l15 = lane & 15, g = lane >> 4, r32 = lane & 31, h = lane >> 5, q4 = l15 >> 2, p4 = l15 & 3;
  const int b = blockIdx.x, qb = (b & 7) * 4 + (b >> 6), hg = (b >> 3) & 7;
  const int ce = 512 + (b >> 2), cq = b & 3, mt = wid & 3;
  char* const myflag = ws + WS_F + (qb * 8 + hg) * 128;
  LAS unsigned* ctrl = (LAS unsigned*)(lds + A_CTRL);
  LAS unsigned* bctl = (LAS unsigned*)(lds + B_CTRL);
#define LADD(i) do { if (lane == 0) __hip_atomic_fetch_add(&ctrl[i], 1u, __ATOMIC_RELAXED, __HIP_MEMORY_SCOPE_WORKGROUP); } while (0)
#define LSET(i, v) do { if (lane == 0) __hip_atomic_store(&ctrl[i], (v), __ATOMIC_RELAXED, __HIP_MEMORY_SCOPE_WORKGROUP); } while (0)
  {
    const int arg = wid >= 6 ? wid - 5 : (mt == 0 ? 9 : (wid < 4 ? 10 : 11 + (hg >> 2)));
    const char* base = ((const char* const __attribute__((address_space(4)))*)__builtin_amdgcn_kernarg_segment_ptr())[arg];
    const int o0 = wid >= 6 ? b * 512 : (mt == 0 ? qb * 73728 + hg * 9216 + wid * 1152 : (wid < 4 ? ((b >> 3) * 3 + wid - 1) * 6912 : (b >> 6) * 5184));
    const int st = wid >= 6 ? 16 : (mt == 0 ? 72 : (wid < 4 ? 108 : 80));
    unsigned vo = lane * st + o0;
    if (wid >= 6 && lane >= 32) vo = ce * 256 + r32 * 8;
    touchs(vo, base, lds0 + A_W - 256);
    const char* base2 = ((const char* const __attribute__((address_space(4)))*)__builtin_amdgcn_kernarg_segment_ptr())[(0x087F >> (4 * mt)) & 15];
    touchs(mt == 3 ? qb * 73728 + (wid >> 2) * 36000 : 0, base2, lds0 + A_W - 256);
  }
  if (tid < 32) ctrl[tid] = 0u;
  BAR();
  asm volatile("" :: "s"(U), "s"(Are), "s"(Aim), "s"(Bre), "s"(Bim), "s"(Cre), "s"(Cim), "s"(logdt), "s"(Dskip), "s"(X), "s"(Wq), "s"(Wk), "s"(Wv), "s"(Wo),
               "s"(out), "s"(ws));
  const int xoff = mt == 0 ? A_X0 : A_W + 9216 + mt * 18432;
  const unsigned xv0 = (lane >> 3) * ROWB + (((lane & 7) ^ (lane >> 4)) * 16), xv1 = (8 + (lane >> 3)) * ROWB + (((lane & 7) ^ (4 + (lane >> 4))) * 16);
  const char* xs = (const char*)X + (size_t)(qb * 64 + mt * 16) * ROWB;
  const bool kgw = mt != (wid < 4 ? 0 : 3) && (wid < 4 || wid >= 8);
  const int kc = wid < 4 ? wid - 1 : wid - 8, kch = kc < 2 ? 2 * b + kc : ce;
  int msc = kgw ? A_SCR + (2 * kc + (wid < 4 ? 1 : 0)) * KG_TAB + 49 * 16 : A_MISC + (wid == 11 ? 80 : (wid == 0 ? 64 : mt * 16));
  asm volatile("" : "+v"(msc)); msc = __builtin_amdgcn_readfirstlane(msc);
  if (wid >= 4 && wid < 8) {
    __builtin_amdgcn_s_setprio(3);
#pragma unroll
    for (int kb = 0; kb < 2; ++kb) { gldss(xv0, xs + kb * 128, lds0 + xoff + kb * 2048); gldss(xv1, xs + kb * 128, lds0 + xoff + kb * 2048 + 1024); }
  } else if (wid == 0) {
    __builtin_amdgcn_s_setprio(3);
    const char* wx = (const char*)(hg < 4 ? Wk : Wv) + 8 * (hg & 3);
#pragma unroll
    for (int i = 0; i < 8; ++i) gldss(lane * 36, wx + i * 64 * 36, lds0 + A_WKV + i * 1024);
    if (lane < 63) gldss(lane * 36, wx + 512 * 36, lds0 + A_WKV + 8192);
    if (lane == 0) {
      unsigned sv_;
      asm volatile("s_mov_b32 %0, m0\n\ts_mov_b32 m0, %3\n\ts_nop 0\n\tglobal_load_lds_dwordx3 %1, %2\n\ts_mov_b32 m0, %0"
                   : "=&s"(sv_) : "v"(575 * 36), "s"(wx), "s"(lds0 + A_WKV + 575 * 16) : "memory");
    }
  }
  {
    const int a6 = lane >> 3;
    const u16* pa = a6 < 2 ? (a6 == 0 ? Are : Aim) : (a6 < 4 ? (a6 == 2 ? Bre : Bim) : (a6 == 4 ? Cre : Cim));
    pa += kch * NS + (wid < 4 ? 64 : 0) + (lane & 7) * 8;
    if (lane == 48) pa = logdt + (kch & ~7);
    if (lane == 49) pa = (const u16*)(ws + WS_E);
    if (lane >= 50) pa = Dskip + (kch & ~7);
    if (lane >= (kgw ? 0 : 49) && lane < (kgw ? 51 : 50)) glds16c(pa, lds0 + msc - 49 * 16);
  }
  STAMP(17);
#define TAG() (*(LAS const unsigned*)(lds + msc) * 2654435761u + 0x9e3779b9u)
  auto kg_read = [&](kg_in& in) {
    LAS const u16* p = (LAS const u16*)(lds + msc - 49 * 16) + lane;
    in.are = p[0]; in.aim = p[64]; in.bre = p[128]; in.bim = p[192]; in.cre = p[256]; in.cim = p[320];
    in.ldt = ((LAS const u16*)(lds + msc - 16))[kch & 7];
    CFENCE();
  };
#define FRESH_LANE() int tid_ = threadIdx.x; asm volatile("" : "+v"(tid_));                                                          \
  const int lane = tid_ & 63, l15 = lane & 15, g = lane >> 4, r32 = lane & 31, h = lane >> 5, q4 = l15 >> 2, p4 = l15 & 3;             \
  (void)r32; (void)h; (void)q4; (void)p4; (void)g
  auto any_bad = [&]() {
    const u32x4 a = *(LAS const u32x4*)bctl, c = *(LAS const u32x4*)(bctl + 4);
    return __builtin_amdgcn_readfirstlane((int)(a[0] | a[1] | a[2] | a[3] | c[0] | c[1] | c[2] | c[3]));
  };

  if (wid < 8) {
    {
      const lptr xb0 = lds + xoff + l15 * 128 + ((g ^ (l15 >> 1)) * 16), xb1 = lds + xoff + l15 * 128 + (((4 + g) ^ (l15 >> 1)) * 16);
      bf16x8 xa[18];
      if (wid >= 4) {
#pragma unroll
        for (int kb = 2; kb < 9; ++kb) {
          gldss(xv0, xs + kb * 128, lds0 + xoff + kb * 2048); gldss(xv1, xs + kb * 128, lds0 + xoff + kb * 2048 + 1024);
        }
        STAMP(1);
        char* cbp = ws + WS_KV + (qb >> 2) * CH_BYTES;
        const int key0 = (qb & 3) * 64 + mt * 16;
        char* sp;
        if (hg < 4) {
          const int key = key0 + l15;
          sp = g == 0 ? cbp + (key >> 5) * 1024 + (hg >> 1) * 512 + (key & 31) * 16 + (hg & 1) * 8 : cbp + CH_TAIL + (key & 63) * 16 + (key >> 6) * 4;
        } else {
          const int key = key0 + 4 * g, dim = 4 * (hg - 4) + l15;
          const int vrow = l15 < 4 ? ((dim & 3) | ((dim & 4) << 1) | ((dim & 8) >> 1)) : 12 + l15;
          sp = cbp + CH_V + (key >> 3) * V_OCT + (key & 4) * 2 + vrow * 16;
        }
        PIN(sp);
        lds_wait(&ctrl[0], 1u);
        const lptr wb = lds + A_WKV + (g * 8 + q4) * 16 + (l15 & 1) * 8;
        bf16x8 wk[18];
#pragma unroll
        for (int i = 0; i < 18; ++i) wk[i] = trd2(wb + i * 512, wb + i * 512 + 64);
        STAMP(2);
        f32x4 acc = {0.f, 0.f, 0.f, 0.f}, acc2 = acc;
#define XGRP(kb0, n, KTYPE)                                                                                                    \
        { VMWAIT(n);                                                                                                           \
          if ((n) == 0) LSET(8 + mt, 1u);                                                                                      \
          _Pragma("unroll") for (int i = 0; i < 6; ++i) xa[2 * (kb0) + i] = *(LAS const bf16x8*)(((i & 1) ? xb1 : xb0) + ((kb0) + (i >> 1)) * 2048); \
          CFENCE();                                                                                                            \
          if ((n) == 0) LADD(12 + mt);                                                                                         \
          _Pragma("unroll") for (int i = 2 * (kb0); i < 2 * (kb0) + 6; i += 2) {                                                \
            if (KTYPE) { acc = MFMA16(wk[i], xa[i], acc); acc2 = MFMA16(wk[i + 1], xa[i + 1], acc2); }                          \
            else { acc = MFMA16(xa[i], wk[i], acc); acc2 = MFMA16(xa[i + 1], wk[i + 1], acc2); } } }
        if (hg < 4) { XGRP(0, 12, 1) XGRP(3, 6, 1) XGRP(6, 0, 1) } else { XGRP(0, 12, 0) XGRP(3, 6, 0) XGRP(6, 0, 0) }
        STAMP(3);
        acc += acc2;
        u32x2 v = {pk2(acc[0], acc[1]), pk2(acc[2], acc[3])};
        unsigned mv;
        if (hg < 4) {
          if (g == 0) st8c(sp, v);
          if (g == 1 && hg == 3) st4c(sp, v[0]);
          float n2 = bflo(v[0]) * bflo(v[0]) + bfhi(v[0]) * bfhi(v[0]);
          if (g == 0) n2 += bflo(v[1]) * bflo(v[1]) + bfhi(v[1]) * bfhi(v[1]);
          const unsigned m16 = max16(__float_as_uint(n2) & 0x7fffffffu);
          const float mq = __uint_as_float(__builtin_amdgcn_readlane(m16, 0)), mtl = __uint_as_float(__builtin_amdgcn_readlane(m16, 16));
          mv = __float_as_uint(hg == 3 ? mq + mtl : mq) & 0x7fffffffu;
        } else {
          unsigned am = umax(umax(v[0] << 16 & 0x7fffffffu, v[0] & 0x7fff0000u), umax(v[1] << 16 & 0x7fffffffu, v[1] & 0x7fff0000u));
          if (l15 >= 6) am = 0u;
          if (l15 == 6) v = (u32x2){0x3f803f80u, 0x3f803f80u};
          if (l15 == 7) v = (u32x2){0u, 0u};
          if (l15 < 4 || (hg == 7 && l15 < 8)) st8c(sp, v);
          am = max16(am);
          mv = umax(umax(__builtin_amdgcn_readlane(am, 0), __builtin_amdgcn_readlane(am, 16)), umax(__builtin_amdgcn_readlane(am, 32), __builtin_amdgcn_readlane(am, 48)));
        }
        if (lane == 0) __hip_atomic_fetch_max(&ctrl[2], mv, __ATOMIC_RELAXED, __HIP_MEMORY_SCOPE_WORKGROUP);
        STAMP(4);
      } else {
        if (wid == 0) {
          __builtin_amdgcn_s_setprio(2);
          unsigned vo[9];
          slice_offsets(vo, lane);
          VMWAIT(1);
          LSET(0, 1u);
          STAMP(1);
          glds9(vo, (const char*)U + (size_t)qb * 64 * ROWB + hg * 144 - 4096, lds0 + A_UT);
          const lptr rp = lds + A_UT + (((4 * g + p4) & 7) * 8 + 2 * q4) * 144 + (g >> 1) * 8;
          char* up = ws + WS_UT + (size_t)(hg * 72 + (lane >> 5) * 4 + (lane & 3)) * UT_ROW + 128 + qb * 128 + ((lane >> 2) & 7) * 16;
          VMWAIT(0);
          bf16x8 ut[9];
#pragma unroll
          for (int i = 0; i < 9; ++i) ut[i] = trd2(rp + i * 16, rp + i * 16 + 144);
          CFENCE();
#pragma unroll
          for (int i = 0; i < 9; ++i) st16c(up + i * 8 * UT_ROW, __builtin_bit_cast(u32x4, ut[i]));
          if (__builtin_expect(qb == 0, 0)) {
            st16c(ws + WS_UT + (size_t)(hg * 72 + lane) * UT_ROW + 112, (u32x4){0u, 0u, 0u, 0u});
            st16c(ws + WS_UT + (size_t)(hg * 72 + 64 + (lane & 7)) * UT_ROW + 112, (u32x4){0u, 0u, 0u, 0u});
          }
          STAMP(2);
        } else {
          __builtin_amdgcn_s_setprio(2);
          VMWAIT(0);
          kg_in in; kg_read(in);
          STAMP(1);
          f32x4 d[2][2];
          const lptr tb = lds + A_SCR + (2 * wid - 1) * KG_TAB;
          kgen_half(in, tb, lane, d);
#pragma unroll
          for (int i = 0; i < 4; ++i) *(LAS f32x4*)(tb + i * 1024 + lane * 16) = d[i >> 1][i & 1];
          CFENCE();
          LSET(15 + wid, 1u);
          VMWAIT(0);
          STAMP(2);
        }
        lds_wait(&ctrl[8 + mt], 1u);
#pragma unroll
        for (int kk = 0; kk < 18; ++kk) xa[kk] = *(LAS const bf16x8*)(((kk & 1) ? xb1 : xb0) + (kk >> 1) * 2048);
        CFENCE();
        LADD(12 + mt);
        STAMP(3);
        STAMP(4);
      }
      if (wid >= 4) {
        VMWAIT(0);
        if (lane == 0 && __hip_atomic_fetch_add(&ctrl[1], 1u, __ATOMIC_RELAXED, __HIP_MEMORY_SCOPE_WORKGROUP) == 3u)
          st8c(myflag, (u32x2){TAG(), __hip_atomic_load(&ctrl[2], __ATOMIC_RELAXED, __HIP_MEMORY_SCOPE_WORKGROUP)});
        __builtin_amdgcn_s_setprio(2);
        STAMP(5);
        qgemm<2>(lds, ctrl, xa, mt, lane);
      } else {
        VMWAIT(0);
        if (wid == 0 && lane == 0) st4c(myflag + 8, TAG());
        STAMP(5);
        qgemm<3>(lds, ctrl, xa, mt, lane);
      }
    }
    STAMP(8);
    FRESH_LANE();
    const int kp = wid >> 2, head = wid & 3;
    f32x16 o0, o1;
#pragma unroll
    for (int j = 0; j < 16; ++j) { o0[j] = 0.f; o1[j] = 0.f; }
    const unsigned ka0 = lds0 + KV_LDS + kp * 1024 + lane * 16, ta0 = lds0 + KV_LDS + CH_TAIL + kp * 512 + r32 * 16,
                   va0 = lds0 + KV_LDS + CH_V + kp * 4 * V_OCT + h * V_OCT + (r32 < 19 ? r32 : 19) * 16;
    unsigned oo = (r32 >> 4) * 18432 + l15 * 16 + h * 256;
    PIN(oo);
    u32x4 q0, q1; unsigned qtl;
    lds_wait(&ctrl[5], 8u);
    {
      LAS const unsigned* qp = (LAS const unsigned*)(lds + A_Q + r32 * Q_ROW + head * 36 + h * 16);
#pragma unroll
      for (int j = 0; j < 4; ++j) { q0[j] = qp[j]; q1[j] = qp[32 * Q_ROW / 4 + j]; }
      qtl = *(LAS const unsigned*)(lds + A_Q + lane * Q_ROW + head * 36 + 32);
    }
    CFENCE();
    LADD(6);
    const u32x2 qa = {qtl, 0u}, qc = {0u, qtl};
    float qn2;
    {
#define DOT2(x, c) __builtin_amdgcn_fdot2_f32_bf16(__builtin_bit_cast(bf16x2, x), __builtin_bit_cast(bf16x2, x), (c), false)
      const float nt = DOT2(qtl, 0.f);
      float n0 = h ? 0.f : nt, n1 = h ? nt : 0.f;
#pragma unroll
      for (int j = 0; j < 4; ++j) { n0 = DOT2(q0[j], n0); n1 = DOT2(q1[j], n1); }
      const auto sw = __builtin_amdgcn_permlane32_swap(__float_as_uint(n0), __float_as_uint(n1), false, false);
      qn2 = __uint_as_float(sw[0]) + __uint_as_float(sw[1]);
      PIN(qn2);
    }
    STAMP(9);
    lds_wait(&ctrl[7], 1u);
    STAMP(10);
    {
      int it = 8; unsigned ka = ka0, va = va0, ta = ta0;
      asm volatile(
      "ds_read_b128 v[128:131], %[ka]\n"
      "ds_read_b128 v[140:143], %[ta]\n"
      "v_mov_b32 v120, 0\n"
      "v_mov_b32 v121, 0\n"
      "v_mov_b32 v122, 0\n"
      "v_mov_b32 v123, 0\n"
      "v_mov_b32 v124, 0\n"
      "v_mov_b32 v125, 0\n"
      "v_mov_b32 v126, 0\n"
      "v_mov_b32 v127, 0\n"
      "v_mov_b32 v136, 0\n"
      "v_mov_b32 v137, 0\n"
      "v_mov_b32 v138, 0\n"
      "v_mov_b32 v139, 0\n"
      "s_waitcnt lgkmcnt(1)\n"
      "v_mfma_f32_32x32x16_bf16 v[64:79], v[128:131], %[q0], 0\n"
      "v_mfma_f32_32x32x16_bf16 v[80:95], v[128:131], %[q1], 0\n"
      "ds_read_b128 v[128:131], %[ka] offset:2048\n"
      "ds_read_b128 v[132:135], %[va]\n"
      "s_nop 7\n"
      "s_waitcnt lgkmcnt(2)\n"
      "v_mfma_f32_32x32x4_2b_bf16 v[64:95], v[140:141], %[qa], v[64:95]\n"
      "s_nop 15\n"
      "s_nop 1\n"
      "1:\n"
      "s_setprio 1\n"
      "s_waitcnt lgkmcnt(1)\n"
      "v_mfma_f32_32x32x16_bf16 v[96:111], v[128:131], %[q0], 0\n"
      "v_exp_f32 v64, v64\n"
      "v_exp_f32 v65, v65\n"
      "v_exp_f32 v66, v66\n"
      "v_smfmac_f32_32x32x32_bf16 %[o1], v[136:139], v[120:127], %[ix]\n"
      "ds_read_b128 v[136:139], %[va] offset:640\n"
      "v_exp_f32 v67, v67\n"
      "v_exp_f32 v68, v68\n"
      "v_exp_f32 v69, v69\n"
      "v_mfma_f32_32x32x16_bf16 v[112:127], v[128:131], %[q1], 0\n"
      "ds_read_b128 v[128:131], %[ka] offset:4096\n"
      "v_exp_f32 v70, v70\n"
      "v_exp_f32 v71, v71\n"
      "v_exp_f32 v72, v72\n"
      "v_exp_f32 v73, v73\n"
      "s_waitcnt lgkmcnt(2)\n"
      "v_smfmac_f32_32x32x32_bf16 %[o0], v[132:135], v[64:71], %[ix]\n"
      "v_exp_f32 v74, v74\n"
      "v_exp_f32 v75, v75\n"
      "v_exp_f32 v76, v76\n"
      "v_exp_f32 v77, v77\n"
      "v_exp_f32 v78, v78\n"
      "v_mfma_f32_32x32x4_2b_bf16 v[96:127], v[140:141], %[qc], v[96:127]\n"
      "v_exp_f32 v79, v79\n"
      "v_exp_f32 v80, v80\n"
      "v_exp_f32 v81, v81\n"
      "v_exp_f32 v82, v82\n"
      "v_exp_f32 v83, v83\n"
      "v_exp_f32 v84, v84\n"
      "v_exp_f32 v85, v85\n"
      "v_exp_f32 v86, v86\n"
      "s_waitcnt lgkmcnt(1)\n"
      "v_smfmac_f32_32x32x32_bf16 %[o0], v[136:139], v[72:79], %[ix]\n"
      "v_exp_f32 v87, v87\n"
      "v_exp_f32 v88, v88\n"
      "v_exp_f32 v89, v89\n"
      "v_exp_f32 v90, v90\n"
      "v_smfmac_f32_32x32x32_bf16 %[o1], v[132:135], v[80:87], %[ix]\n"
      "ds_read_b128 v[132:135], %[va] offset:2560\n"
      "v_exp_f32 v91, v91\n"
      "v_exp_f32 v92, v92\n"
      "v_exp_f32 v93, v93\n"
      "v_exp_f32 v94, v94\n"
      "v_exp_f32 v95, v95\n"
      "s_setprio 0\n"
      "s_waitcnt lgkmcnt(1)\n"
      "v_mfma_f32_32x32x16_bf16 v[64:79], v[128:131], %[q0], 0\n"
      "v_exp_f32 v96, v96\n"
      "v_exp_f32 v97, v97\n"
      "v_exp_f32 v98, v98\n"
      "v_smfmac_f32_32x32x32_bf16 %[o1], v[136:139], v[88:95], %[ix]\n"
      "ds_read_b128 v[136:139], %[va] offset:3200\n"
      "v_exp_f32 v99, v99\n"
      "v_exp_f32 v100, v100\n"
      "v_exp_f32 v101, v101\n"
      "v_mfma_f32_32x32x16_bf16 v[80:95], v[128:131], %[q1], 0\n"
      "ds_read_b128 v[128:131], %[ka] offset:6144\n"
      "v_exp_f32 v102, v102\n"
      "v_exp_f32 v103, v103\n"
      "v_exp_f32 v104, v104\n"
      "v_exp_f32 v105, v105\n"
      "s_waitcnt lgkmcnt(2)\n"
      "v_smfmac_f32_32x32x32_bf16 %[o0], v[132:135], v[96:103], %[ix]\n"
      "v_exp_f32 v106, v106\n"
      "v_exp_f32 v107, v107\n"
      "v_exp_f32 v108, v108\n"
      "v_exp_f32 v109, v109\n"
      "v_exp_f32 v110, v110\n"
      "v_mfma_f32_32x32x4_2b_bf16 v[64:95], v[142:143], %[qa], v[64:95]\n"
      "v_exp_f32 v111, v111\n"
      "v_exp_f32 v112, v112\n"
      "v_exp_f32 v113, v113\n"
      "v_exp_f32 v114, v114\n"
      "v_exp_f32 v115, v115\n"
      "v_exp_f32 v116, v116\n"
      "v_exp_f32 v117, v117\n"
      "v_exp_f32 v118, v118\n"
      "s_waitcnt lgkmcnt(1)\n"
      "v_smfmac_f32_32x32x32_bf16 %[o0], v[136:139], v[104:111], %[ix]\n"
      "v_exp_f32 v119, v119\n"
      "v_exp_f32 v120, v120\n"
      "v_exp_f32 v121, v121\n"
      "v_exp_f32 v122, v122\n"
      "v_smfmac_f32_32x32x32_bf16 %[o1], v[132:135], v[112:119], %[ix]\n"
      "ds_read_b128 v[132:135], %[va] offset:5120\n"
      "v_exp_f32 v123, v123\n"
      "v_exp_f32 v124, v124\n"
      "v_exp_f32 v125, v125\n"
      "v_exp_f32 v126, v126\n"
      "v_exp_f32 v127, v127\n"
      "s_setprio 3\n"
      "s_waitcnt lgkmcnt(1)\n"
      "v_mfma_f32_32x32x16_bf16 v[96:111], v[128:131], %[q0], 0\n"
      "v_exp_f32 v64, v64\n"
      "v_exp_f32 v65, v65\n"
      "v_exp_f32 v66, v66\n"
      "v_smfmac_f32_32x32x32_bf16 %[o1], v[136:139], v[120:127], %[ix]\n"
      "ds_read_b128 v[136:139], %[va] offset:5760\n"
      "v_exp_f32 v67, v67\n"
      "v_exp_f32 v68, v68\n"
      "v_exp_f32 v69, v69\n"
      "v_mfma_f32_32x32x16_bf16 v[112:127], v[128:131], %[q1], 0\n"
      "s_setprio 2\n"
      "s_waitcnt lgkmcnt(1)\n"
      "v_mfma_f32_32x32x16_bf16 v[64:79], v[128:131], %[q0], 0\n"
      "v_exp_f32 v96, v96\n"
      "v_exp_f32 v97, v97\n"
      "v_exp_f32 v98, v98\n"
      "v_smfmac_f32_32x32x32_bf16 %[o1], v[136:139], v[88:95], %[ix]\n"
      "ds_read_b128 v[136:139], %[va] offset:8320\n"
      "v_exp_f32 v99, v99\n"
      "v_exp_f32 v100, v100\n"
      "v_exp_f32 v101, v101\n"
      "v_mfma_f32_32x32x16_bf16 v[80:95], v[128:131], %[q1], 0\n"
      "ds_read_b128 v[128:131], %[ka] offset:21504\n"
      "v_exp_f32 v102, v102\n"
      "v_exp_f32 v103, v103\n"
      "v_exp_f32 v104, v104\n"
      "v_exp_f32 v105, v105\n"
      "s_waitcnt lgkmcnt(2)\n"
      "v_smfmac_f32_32x32x32_bf16 %[o0], v[132:135], v[96:103], %[ix]\n"
      "v_exp_f32 v106, v106\n"
      "v_exp_f32 v107, v107\n"
      "v_exp_f32 v108, v108\n"
      "v_exp_f32 v109, v109\n"
      "v_exp_f32 v110, v110\n"
      "v_mfma_f32_32x32x4_2b_bf16 v[64:95], v[140:141], %[qa], v[64:95]\n"
      "v_exp_f32 v111, v111\n"
      "v_exp_f32 v112, v112\n"
      "v_exp_f32 v113, v113\n"
      "v_exp_f32 v114, v114\n"
      "v_exp_f32 v115, v115\n"
      "v_exp_f32 v116, v116\n"
      "v_exp_f32 v117, v117\n"
      "v_exp_f32 v118, v118\n"
      "s_waitcnt lgkmcnt(1)\n"
      "v_smfmac_f32_32x32x32_bf16 %[o0], v[136:139], v[104:111], %[ix]\n"
      "v_exp_f32 v119, v119\n"
      "v_exp_f32 v120, v120\n"
      "v_exp_f32 v121, v121\n"
      "v_exp_f32 v122, v122\n"
      "v_smfmac_f32_32x32x32_bf16 %[o1], v[132:135], v[112:119], %[ix]\n"
      "ds_read_b128 v[132:135], %[va] offset:19456\n"
      "v_exp_f32 v123, v123\n"
      "v_exp_f32 v124, v124\n"
      "v_exp_f32 v125, v125\n"
      "v_exp_f32 v126, v126\n"
      "v_exp_f32 v127, v127\n"
      "v_add_u32 %[ka], 19456, %[ka]\n"
      "v_add_u32 %[va], 19456, %[va]\n"
      "v_add_u32 %[ta], 19456, %[ta]\n"
      "s_sub_u32 %[it], %[it], 1\n"
      "s_cmp_lg_u32 %[it], 0\n"
      "s_cbranch_scc1 1b\n"
      "v_smfmac_f32_32x32x32_bf16 %[o1], v[136:139], v[120:127], %[ix]\n"
      "s_setprio 0\n"
      "s_waitcnt lgkmcnt(0)\n"
      "s_nop 15\n"
          : [o0] "+v"(o0), [o1] "+v"(o1), [ka] "+v"(ka), [va] "+v"(va), [ta] "+v"(ta), [it] "+s"(it)
          : [q0] "v"(q0), [q1] "v"(q1), [qa] "v"(qa), [qc] "v"(qc), [ix] "v"(0xdddddddd)
          : "v64", "v65", "v66", "v67", "v68", "v69", "v70", "v71", "v72", "v73", "v74", "v75", "v76", "v77", "v78", "v79", "v80", "v81", "v82", "v83", "v84", "v85", "v86", "v87", "v88", "v89", "v90", "v91", "v92", "v93", "v94", "v95", "v96", "v97", "v98", "v99", "v100", "v101", "v102", "v103", "v104", "v105", "v106", "v107", "v108", "v109", "v110", "v111", "v112", "v113", "v114", "v115", "v116", "v117", "v118", "v119", "v120", "v121", "v122", "v123", "v124", "v125", "v126", "v127", "v128", "v129", "v130", "v131", "v132", "v133", "v134", "v135", "v136", "v137", "v138", "v139", "v140", "v141", "v142", "v143", "scc", "memory");
    }
    STAMP(13);
    const lptr xo = lds + C_OX + lane * 16;
    auto put_half = [&]() {
#pragma unroll
      for (int i = 0; i < 3; ++i) {
        const f32x4 a = {o0[4 * i], o0[4 * i + 1], o0[4 * i + 2], o0[4 * i + 3]}, c = {o1[4 * i], o1[4 * i + 1], o1[4 * i + 2], o1[4 * i + 3]};
        *(LAS f32x4*)(xo + (wid * 3 + i) * 1024) = kp ? a : c;
      }
    };
    put_half();
    {
      const bool bad = __any(!(qn2 * __uint_as_float(bctl[8]) <= 8000.f));
      if (lane == 0) bctl[wid] = bad ? 1u : 0u;
    }
    BAR();
    if (__builtin_expect(any_bad(), 0)) {
      BAR(); BAR();
      const bf16x8 qf0 = __builtin_bit_cast(bf16x8, q0), qf1 = __builtin_bit_cast(bf16x8, q1);
      float m0 = -INFINITY, m1 = -INFINITY;
#pragma unroll 1
      for (int i = 0; i < 32; ++i) {
        const int off = (i >> 2) * CH_BYTES;
        const f32x32 s = qk_tile((lptr)(uintptr_t)(ka0 + off + (i & 3) * 2048), qf0, qf1, *(LAS const unsigned*)(uintptr_t)(ta0 + off + (i & 3) * 4), qa);
#pragma unroll
        for (int j = 0; j < 16; ++j) { m0 = fmaxf(m0, s[j]); m1 = fmaxf(m1, s[16 + j]); }
      }
      m0 = fmaxf(m0, __shfl_xor(m0, 32)); m1 = fmaxf(m1, __shfl_xor(m1, 32));
      *(LAS f32x2*)(lds + SPARE + tid * 8) = (f32x2){m0, m1};
      BAR();
      const f32x2 mo = *(LAS const f32x2*)(lds + SPARE + (tid ^ 256) * 8);
      m0 = fmaxf(m0, mo[0]); m1 = fmaxf(m1, mo[1]);
#pragma unroll
      for (int j = 0; j < 16; ++j) { o0[j] = 0.f; o1[j] = 0.f; }
#pragma unroll 1
      for (int i = 0; i < 32; ++i) {
        const int off = (i >> 2) * CH_BYTES;
        f32x32 s = qk_tile((lptr)(uintptr_t)(ka0 + off + (i & 3) * 2048), qf0, qf1, *(LAS const unsigned*)(uintptr_t)(ta0 + off + (i & 3) * 4), qa);
#pragma unroll
        for (int j = 0; j < 16; ++j) { s[j] = __builtin_amdgcn_exp2f(s[j] - m0); s[16 + j] = __builtin_amdgcn_exp2f(s[16 + j] - m1); }
        const bf16x8 v0 = *(LAS const bf16x8*)(uintptr_t)(va0 + off + (i & 3) * 8 * V_OCT), v1 = *(LAS const bf16x8*)(uintptr_t)(va0 + off + ((i & 3) * 8 + 2) * V_OCT);
        o0 = SMF(v0, sub8(s, 0), o0); o0 = SMF(v1, sub8(s, 8), o0); o1 = SMF(v0, sub8(s, 16), o1); o1 = SMF(v1, sub8(s, 24), o1);
      }
      BAR();
      put_half();
      BAR();
    }
    float f[12];
#pragma unroll
    for (int i = 0; i < 3; ++i) {
      const f32x4 p = *(LAS const f32x4*)(xo + ((wid ^ 4) * 3 + i) * 1024);
#pragma unroll
      for (int j = 0; j < 4; ++j) f[4 * i + j] = (kp ? o1[4 * i + j] : o0[4 * i + j]) + p[j];
    }
    const auto sw = __builtin_amdgcn_permlane32_swap(__float_as_uint(f[10]), __float_as_uint(f[10]), false, false);
    const float inv = __builtin_amdgcn_rcpf(__uint_as_float(sw[0]));
    const char* ob = ws + WS_O + (qb * 4 + kp * 2) * 18432 + hg * 2304;
    const u32x4 ov = {nosent(pk2(f[0] * inv, f[1] * inv)), nosent(pk2(f[2] * inv, f[3] * inv)), nosent(pk2(f[4] * inv, f[5] * inv)), nosent(pk2(f[6] * inv, f[7] * inv))};
    asm volatile("global_store_dwordx4 %0, %1, %2 sc1\n\ts_nop 1" ::"v"(oo), "v"(ov), "s"(ob + head * 512) : "memory");
    const unsigned ot = nosent(pk2(f[8] * inv, f[9] * inv));
    if (h == 0) asm volatile("global_store_dword %0, %1, %2 offset:2048 sc1" ::"v"(oo), "v"(ot), "s"(ob + head * 4) : "memory");
    STAMP(11);
  } else {
    const int sv = wid - 8, chl = sv >> 1, half = sv & 1, rot = b & 7;
    const unsigned l16 = lane * 16;
    unsigned vo[9];
    const char* wsrc = (const char*)Wq + hg * 144 - 4096;
    if (sv == 3) {
      slice_offsets(vo, lane);
#pragma unroll
      for (int a = 0; a < 3; ++a) glds9(vo, wsrc + a * 64 * ROWB, lds0 + A_W + a * 9216);
      touchs(1024 + (lane & 3) * (2u << 20), ws, lds0 + A_W - 256);
      VMWAIT(1); LSET(4, 3u);
    } else VMWAIT(0);
    const unsigned tag = __builtin_amdgcn_readfirstlane(TAG());
    const float dsk = bf2f(((LAS const u16*)(lds + msc + 16))[kch & 7]);
#define KVC(s0) do { kv_group(ws, lds0, (s0) + chl, rot, 2 * half, l16); kv_group(ws, lds0, (s0) + chl, rot, 2 * half + 1, l16);            \
                     if (half) kv_group(ws, lds0, (s0) + chl, rot, 4, l16); } while (0)
    const char* fp0 = ws + WS_F + (((rot + 2 * sv + 2 + h) & 7) * 32 + r32) * 128;
    u32x4 fl0;
    if (sv < 3) {
      __builtin_amdgcn_s_setprio(1);
      kg_in in; kg_read(in);
      STAMP(1);
      f32x4 d[2][2];
      const lptr tb = lds + A_SCR + 2 * sv * KG_TAB;
      kgen_half(in, tb, lane, d);
      VMWAIT(0);
      STAMP(2);
      lds_wait(&ctrl[16 + sv], 1u);
#pragma unroll
      for (int i = 0; i < 4; ++i) d[i >> 1][i & 1] += *(LAS const f32x4*)(tb + KG_TAB + i * 1024 + lane * 16);
      if (lane == 0) d[0][0][0] += dsk;
      kgen_finish(d, lds + CVB + sv * R_BYTES, &ctrl[5], tb, lane);
      asm volatile("global_load_dwordx4 %0, %1, off sc1\n\ts_waitcnt vmcnt(0)" : "=&v"(fl0) : "v"(fp0) : "memory");
      flag_wait<0>(fp0, tag, fl0);
      if (lane < 56) *(LAS u32x4*)(lds + CVB + CV_U + sv * CV_UCH + (lane < 28 ? lane * 16 : CV_U1 + (lane - 28) * 16)) = (u32x4){0u, 0u, 0u, 0u};
      STAMP(4);
      __builtin_amdgcn_s_setprio(3);
    } else {
      STAMP(1);
      __builtin_amdgcn_s_setprio(3);
#pragma unroll
      for (int a = 3; a < 9; ++a) {
        if (a & 1) lds_wait(&ctrl[12 + (a >> 1)], 2u);
        glds9(vo, wsrc + a * 64 * ROWB, lds0 + A_W + a * 9216);
      }
      STAMP(3);
      VMWAIT(36); LSET(4, 5u);
      VMWAIT(18); LSET(4, 7u);
      VMWAIT(9); LSET(4, 8u);
      VMWAIT(0); LSET(4, 9u);
      STAMP(4);
      const unsigned tg = tag;
      u32x4 fu;
      const char* fup = ws + WS_F + (r32 * 8 + (h ? 7 : b / 36)) * 128;
      asm volatile("global_load_dwordx4 %0, %1, off sc1\n\ts_waitcnt vmcnt(0)" : "=&v"(fl0) : "v"(fp0) : "memory");
#pragma unroll 1
      for (int spin = 0; spin < SPIN_MAX; ++spin) {
        if (__all(h == 1 || fl0[0] == tg)) break;
        __builtin_amdgcn_s_sleep(POLL0);
        if (h == 0 && fl0[0] != tg) asm volatile("global_load_dwordx4 %0, %1, off sc1\n\ts_waitcnt vmcnt(0)" : "+v"(fl0) : "v"(fp0) : "memory");
      }
      STAMP(5);
      lds_wait(&ctrl[12], 2u);
#pragma unroll
      for (int i = 0; i < 5; ++i) kv_group(ws, lds0, 0, rot, i, l16);
      LD16(fu, fup, " sc1");
      VMWAIT(1);
      LSET(7, 1u);
      STAMP(7);
      VMWAIT(0); PIN(fu);
      flag_wait<0>(fp0, tg, fl0);
      STAMP(9);
#pragma unroll
      for (int i = 0; i < 4; ++i) kv_group(ws, lds0, 1, rot, i, l16);
      flag_wait<2>(fup, tg, fu);
      lds_wait(&ctrl[5], 8u);
#pragma unroll
      for (int i = 0; i < 6; ++i)
        glds4c(l16, ws + WS_UT + (size_t)(i < 4 ? 2 * b + (i >> 1) : ce) * UT_ROW + 128 - ((i & 1) ? 0 : 2), lds0 + CVB + CV_U + (i >> 1) * CV_UCH + (i & 1) * CV_U1 + U_PADB);
      lds_wait(&ctrl[6], 8u);
      kv_group(ws, lds0, 1, rot, 4, l16);
      STAMP(10);
    }
    {
      unsigned m = fl0[1];
      m = umax(m, dppu<0x128>(m));
      { const auto t = __builtin_amdgcn_permlane16_swap(m, m, false, false); m = umax(t[0], t[1]); }
      { const auto t = __builtin_amdgcn_permlane32_swap(m, m, false, false); m = umax(t[0], t[1]); }
      if (lane < 8) bctl[-32 + sv * 8 + lane] = m;
    }
    const int rr = lane >> 3;
    const char* wo = (const char*)Wo + hg * 128;
    const unsigned wsw = ((lane & 7) ^ (2 * ((rr >> 1) & 1))) * 16;
    auto wo_group = [&](int gq) {
      const unsigned va = (rr * ROWB + wsw) ^ ((gq & 1) * 64), vb = (va ^ 64) + 8 * ROWB - 1024, vt = ((18 * (rr >> 1) + 16 + (rr & 1)) * ROWB + wsw) ^ ((gq & 1) * 64);
      const char* sb = wo + gq * 72 * ROWB;
      unsigned sv_;
      asm volatile("s_mov_b32 %0, m0\n\ts_add_u32 m0, %8, 0x2000\n\ts_nop 0\n\tglobal_load_lds_dwordx4 %3, %4\n\t"
                   "s_mov_b32 m0, %8\n\ts_nop 0\n\tglobal_load_lds_dwordx4 %1, %4\n\tglobal_load_lds_dwordx4 %2, %4 offset:1024\n\t"
                   "s_add_u32 m0, m0, 0x800\n\ts_nop 0\n\tglobal_load_lds_dwordx4 %1, %5\n\tglobal_load_lds_dwordx4 %2, %5 offset:1024\n\t"
                   "s_add_u32 m0, m0, 0x800\n\ts_nop 0\n\tglobal_load_lds_dwordx4 %1, %6\n\tglobal_load_lds_dwordx4 %2, %6 offset:1024\n\t"
                   "s_add_u32 m0, m0, 0x800\n\ts_nop 0\n\tglobal_load_lds_dwordx4 %1, %7\n\tglobal_load_lds_dwordx4 %2, %7 offset:1024\n\ts_mov_b32 m0, %0"
                   : "=&s"(sv_) : "v"(va), "v"(vb), "v"(vt), "s"(sb), "s"(sb + 18 * ROWB), "s"(sb + 36 * ROWB), "s"(sb + 54 * ROWB), "s"(lds0 + gq * 9216)
                   : "memory", "scc");
    };
    auto wo_rem = [&]() {
#pragma unroll 1
      for (int a = sv; a < 9; a += 4) {
        const int O = 8 * a + rr, gq = O / 9, o = O - gq * 9, e8 = lane & 7;
        gldss((gq * 72 + (o < 8 ? 18 * (o >> 1) + 8 * (o & 1) + e8 : 18 * (e8 >> 1) + 16 + (e8 & 1))) * ROWB, (const char*)Wo + 1024 + hg * 16, lds0 + C_REM + a * 1024);
      }
    };
    const int rofs = CVB + (1023 - l15 + 4 * g) * 4, uofs = CVB + CV_U + (l15 & 1) * CV_U1 + U_PADB + (32 * (l15 >> 1) + 8 * g) * 2;
    f32x4 acc[8], ea = {0.f, 0.f, 0.f, 0.f}, eb = ea;
#pragma unroll
    for (int c = 0; c < 8; ++c) acc[c] = ea;
#pragma unroll 1
    for (int iv = 0; iv < 4; ++iv) {
      STAMP(16);
      BARVM(0);
      STAMP(24);
      if (iv < 2) { KVC(2 + 2 * iv); }
      if (iv == 0) touch((const char*)Wo + ((b >> 3) * 162 + min(sv * 41 + lane, 161)) * 128, lds0 + B_CTRL - 384);
      if (iv == 1 && sv == 3) {
        const int l7 = lane & 7;
        const unsigned m = umax(umax(bctl[-32 + l7], bctl[-24 + l7]), umax(bctl[-16 + l7], bctl[-8 + l7]));
        const float k2 = __uint_as_float(__builtin_amdgcn_readlane(m, 0)) + __uint_as_float(__builtin_amdgcn_readlane(m, 1)) +
                         __uint_as_float(__builtin_amdgcn_readlane(m, 2)) + __uint_as_float(__builtin_amdgcn_readlane(m, 3));
        const float vf = __uint_as_float(umax(umax(__builtin_amdgcn_readlane(m, 4), __builtin_amdgcn_readlane(m, 5)), umax(__builtin_amdgcn_readlane(m, 6), __builtin_amdgcn_readlane(m, 7))));
        if (lane == 0) bctl[8] = (vf <= 1048576.f && vf >= 9.5367431640625e-7f) ? __float_as_uint(k2) : 0x7fc00000u;
      }
      if (iv == 3 && sv < 3) wo_group(3 + sv);
      if (iv == 2) {
        char* p = ws + WS_O + (qb * 4 + sv) * 18432 + 9 * hg * 256 + l16;
        st16c(p, (u32x4){SENT, SENT, SENT, SENT}); st16c(p + 1024, (u32x4){SENT, SENT, SENT, SENT});
        if (lane < 16) st16c(p + 2048, (u32x4){SENT, SENT, SENT, SENT});
      }
      {
        const int m0 = 4 * half + iv;
        const lptr rb = lds + chl * R_BYTES + rofs - 3584 - m0 * 64, ub = lds + chl * CV_UCH + uofs - m0 * 64;
        bf16x8 uf[8], kf[8];
#pragma unroll
        for (int z = 0; z < 8; ++z) {
          LAS const unsigned* rp = (LAS const unsigned*)(rb + 512 * (7 - z));
          kf[z] = __builtin_bit_cast(bf16x8, (u32x4){rp[0], rp[1], rp[2], rp[3]});
          uf[z] = *(LAS const bf16x8*)(ub + z * 512);
        }
        CFENCE();
#pragma unroll
        for (int c = 0; c < 8; ++c)
#pragma unroll
          for (int m1 = 0; m1 <= c; ++m1) acc[c] = MFMA16(uf[c - m1], kf[m1], acc[c]);
      }
      {
        const int m0 = 2 * sv + (iv >> 1), cB = 7 - cq;
        const lptr rb = lds + 2 * R_BYTES + rofs - m0 * 64, ub = lds + 2 * CV_UCH + uofs - m0 * 64;
        if ((iv & 1) == 0) {
#pragma unroll
          for (int i = 0; i < 5; ++i) {
            LAS const unsigned* rp = (LAS const unsigned*)(rb - 512 * i);
            eb = MFMA16(*(LAS const bf16x8*)(ub + 512 * (cB - i)), __builtin_bit_cast(bf16x8, (u32x4){rp[0], rp[1], rp[2], rp[3]}), eb);
          }
        } else {
          bf16x8 uf[4], kf[4];
#pragma unroll
          for (int i = 5; i < 9; ++i) {
            const bool isB = i <= cB;
            const int m1 = isB ? i : i - cB - 1, z = (isB ? cB : cq) - m1;
            LAS const unsigned* rp = (LAS const unsigned*)(rb - 512 * m1);
            kf[i - 5] = __builtin_bit_cast(bf16x8, (u32x4){rp[0], rp[1], rp[2], rp[3]});
            uf[i - 5] = *(LAS const bf16x8*)(ub + 512 * z);
          }
#pragma unroll
          for (int i = 5; i < 9; ++i) {
            if (i < 8 && i <= cB) eb = MFMA16(uf[i - 5], kf[i - 5], eb);
            else ea = MFMA16(uf[i - 5], kf[i - 5], ea);
          }
        }
      }
    }
    STAMP(6);
    const int ec = chl ? 7 - cq : cq;
    const lptr sc = lds + CV_SCR + l16;
    if (half) {
#pragma unroll
      for (int i = 0; i < 4; ++i) { *(LAS f32x4*)(sc + (sv * 4 + i) * 1024) = acc[i]; acc[i] = acc[4 + i]; }
    } else {
#pragma unroll
      for (int i = 0; i < 4; ++i) *(LAS f32x4*)(sc + (sv * 4 + i) * 1024) = acc[4 + i];
    }
    if (sv != 0) *(LAS f32x4*)(sc + 16384 + (sv - 1) * 1024) = ea;
    if (sv != 2) *(LAS f32x4*)(sc + 19456 + (sv > 2 ? 2 : sv) * 1024) = eb;
    STAMP(20);
    BAR();
    STAMP(27);
    KVC(6);
    if (half) wo_group(6 + chl);
    {
      const unsigned yo = g * 128 + l15 * 4;
      const char* yb = ws + WS_YT + (2 * b + chl) * 4096 + half * 2048;
#define YST(base, off, v) asm volatile("global_store_dword %0, %1, %2 offset:" #off " sc1" ::"v"(yo), "v"(v), "s"(base) : "memory")
#define YTILE(i, o0, o1) do { const f32x4 f = acc[i] + *(LAS const f32x4*)(sc + ((sv ^ 1) * 4 + (i)) * 1024); YST(yb, o0, pk2(f[0], f[1])); YST(yb, o1, pk2(f[2], f[3])); } while (0)
      YTILE(0, 0, 64); YTILE(1, 512, 576); YTILE(2, 1024, 1088); YTILE(3, 1536, 1600);
      if (half == 0) {
        f32x4 e = chl ? eb : ea;
#pragma unroll
        for (int s = 0; s < 3; ++s) e += *(LAS const f32x4*)(sc + 16384 + chl * 3072 + s * 1024);
        const char* ye = ws + WS_YT + ce * 4096 + ec * 512;
        YST(ye, 0, pk2(e[0], e[1])); YST(ye, 64, pk2(e[2], e[3]));
      }
    }
    STAMP(21);
    BARVM(0);
    STAMP(28);
    if (wid == 8 && lane == 0) st4c(myflag + 12, tag);
    if (sv) wo_group(sv - 1);
    wo_rem();
    const int tq = (qb >> 2) < 4 ? (qb >> 2) : 7 - (qb >> 2);
    const int pb0 = 32 * hg + (lane < 32 ? lane : 4 * (lane & 7) + tq);
    const char* f2a = ws + WS_F + (lane >= 48 ? qb * 8 + (lane & 7) : ((pb0 & 7) * 4 + (pb0 >> 6)) * 8 + ((pb0 >> 3) & 7)) * 128;
    const char* f2b = ws + WS_F + (sv * 64 + lane) * 128;
    u32x4 fa, fb = {tag, tag, tag, tag};
    LD16(fa, f2a, " sc1");
    if (b == 0) asm volatile("global_load_dwordx4 %0, %1, off sc1" : "+v"(fb) : "v"(f2b) : "memory");
    STAMP(22);
    BAR();
    STAMP(29);
    VMWAIT(0);
    PIN(fa); PIN(fb);
    flag_wait<3>(f2a, tag, fa); flag_wait<3>(f2b, tag, fb);
    {
      const char* yp = ws + WS_YT + hg * 64 * 4096 + qb * 128 + sv * 32;
      const unsigned yv = (lane >> 1) * 4096 + (lane & 1) * 16;
      gldssc(yv, yp, lds0 + C_YT + sv * 2304); gldssc(yv, yp + 32 * 4096, lds0 + C_YT + sv * 2304 + 1024);
      if (lane < 16) gldssc(yv, yp + (512 - hg * 56) * 4096, lds0 + C_YT + sv * 2304 + 2048);
    }
    VMWAIT(0);
    {
      const lptr rp = lds + C_YT + sv * 2304 + (((4 * g + p4) & 7) * 8 + q4) * 32 + (g >> 1) * 8, re = lds + C_YT + sv * 2304 + 2048 + q4 * 32 + p4 * 8;
      u16* orow = out + (size_t)(qb * 64 + sv * 16 + (lane & 3)) * OUTLD;
      u16* om = orow + h * 4 * OUTLD + hg * 64 + ((lane >> 2) & 7) * 8;
      const u32x4 v0 = __builtin_bit_cast(u32x4, trd2(rp, rp + 128)), v1 = __builtin_bit_cast(u32x4, trd2(rp + 16, rp + 144));
      const u32x4 ve = __builtin_bit_cast(u32x4, trd2(re, re + 128));
      ost16(om, v0); ost16(om + 8 * OUTLD, v1);
      if (lane < 16) ost16(orow + q4 * 4 * OUTLD + 512 + hg * 8, ve);
    }
    STAMP(23);
    BAR();
    STAMP(12);
    if (b == 0 && wid == 8 && lane == 0) st4c(ws + WS_E, tag);
    __builtin_amdgcn_s_setprio(0);
    VMWAIT(0);
    STAMP(13);
    BAR();
    if (__builtin_expect(any_bad(), 0)) { BAR(); _Pragma("unroll 1") for (int i = 0; i < 8; i += 2) KVC(i); BARVM(0); BAR(); BAR(); wo_group(sv); wo_group(sv + 4); wo_rem(); BARVM(0); }
    __builtin_amdgcn_s_sleep(OSLEEP);
  }
  {
    FRESH_LANE();
    const int kt = wid >> 2;
    bf16x8 wf[6][4];
    {
      const int sw = 2 * (((q4 >> 1) & 1) | ((g & 1) << 1));
      const lptr wb = lds + kt * 24576 + (g * 8 + q4) * 128 + (p4 & 1) * 8;
#pragma unroll
      for (int nt = 0; nt < 4; ++nt) {
        const lptr p = wb + (((2 * nt + (p4 >> 1)) ^ sw) * 16);
#pragma unroll
        for (int kk = 0; kk < 6; ++kk) wf[kk][nt] = trd2(p + kk * 4096, p + kk * 4096 + 512);
      }
    }
    VMWAIT(0);
    if (CSLEEP) __builtin_amdgcn_s_sleep(CSLEEP);
    const lptr wr = lds + C_REM + kt * 3072 + (g * 8 + q4) * 16 + (p4 & 1) * 8;
    STAMP(14);
    const char* op = ws + WS_O + (size_t)((qb * 4 + mt) * 18 + kt * 6) * 1024 + lane * 16;
    u32x4 of0, of1, of2, of3, of4, of5;
    const char* op2 = op + 3072;
    asm volatile("global_load_dwordx4 %0, %6, off sc1\n\tglobal_load_dwordx4 %1, %6, off offset:1024 sc1\n\t"
                 "global_load_dwordx4 %2, %6, off offset:2048 sc1\n\tglobal_load_dwordx4 %3, %7, off sc1\n\t"
                 "global_load_dwordx4 %4, %7, off offset:1024 sc1\n\tglobal_load_dwordx4 %5, %7, off offset:2048 sc1\n\ts_waitcnt vmcnt(0)"
                 : "=&v"(of0), "=&v"(of1), "=&v"(of2), "=&v"(of3), "=&v"(of4), "=&v"(of5) : "v"(op), "v"(op2) : "memory");
#define MX4(x) umax(umax(x[0], x[1]), umax(x[2], x[3]))
    if (__builtin_expect(__any(umax(umax(umax(MX4(of0), MX4(of1)), umax(MX4(of2), MX4(of3))), umax(MX4(of4), MX4(of5))) == SENT), 0))
#pragma unroll 1
    for (int spin = 0; spin < SPIN_MAX; ++spin) {
      const bool m0 = umax(umax(MX4(of0), MX4(of1)), MX4(of2)) == SENT, m1 = umax(umax(MX4(of3), MX4(of4)), MX4(of5)) == SENT;
      if (!__any(m0 || m1)) break;
      __builtin_amdgcn_s_sleep(ORETRY);
      if (m0) asm volatile("global_load_dwordx4 %0, %3, off sc1\n\tglobal_load_dwordx4 %1, %3, off offset:1024 sc1\n\t"
                           "global_load_dwordx4 %2, %3, off offset:2048 sc1\n\ts_waitcnt vmcnt(0)" : "+v"(of0), "+v"(of1), "+v"(of2) : "v"(op) : "memory");
      if (m1) asm volatile("global_load_dwordx4 %0, %3, off sc1\n\tglobal_load_dwordx4 %1, %3, off offset:1024 sc1\n\t"
                           "global_load_dwordx4 %2, %3, off offset:2048 sc1\n\ts_waitcnt vmcnt(0)" : "+v"(of3), "+v"(of4), "+v"(of5) : "v"(op2) : "memory");
    }
    const u32x4 of[6] = {of0, of1, of2, of3, of4, of5};
    STAMP(15);
    f32x4 acc[5];
#pragma unroll
    for (int nt = 0; nt < 5; ++nt) acc[nt] = (f32x4){0.f, 0.f, 0.f, 0.f};
#pragma unroll
    for (int kk = 0; kk < 6; ++kk) {
#pragma unroll
      for (int nt = 0; nt < 4; ++nt) acc[nt] = MFMA16(wf[kk][nt], __builtin_bit_cast(bf16x8, of[kk]), acc[nt]);
      acc[4] = MFMA16(trd2(wr + kk * 512, wr + kk * 512 + 64), __builtin_bit_cast(bf16x8, of[kk]), acc[4]);
    }
    const lptr rd = lds + C_RED2 + mt * 5120 + lane * 16, rd2 = lds + C_RED + mt * 5120 + lane * 16;
#pragma unroll
    for (int nt = 0; nt < 5; ++nt) {
      const int own = nt >> 1;
      if (kt != own) *(LAS f32x4*)(((kt + 1) % 3 == own ? rd : rd2) + nt * 1024) = acc[nt];
    }
    BAR();

    u16* cp = out + (size_t)(qb * 64 + mt * 16 + l15) * OUTLD + DM + g * 4 + (kt < 2 ? hg * 64 + kt * 32 : 512 + hg * 8);
    const f32x4 a0 = kt == 0 ? acc[0] : (kt == 1 ? acc[2] : acc[4]), a1 = kt == 0 ? acc[1] : acc[3];
    const f32x4 v = a0 + *(LAS const f32x4*)(rd + kt * 2048) + *(LAS const f32x4*)(rd2 + kt * 2048);
    if (kt < 2 || g < 2) ost8(cp, (u32x2){pk2(v[0], v[1]), pk2(v[2], v[3])});
    if (kt < 2) {
      const f32x4 w = a1 + *(LAS const f32x4*)(rd + kt * 2048 + 1024) + *(LAS const f32x4*)(rd2 + kt * 2048 + 1024);
      ost8(cp + 16, (u32x2){pk2(w[0], w[1]), pk2(w[2], w[3])});
    }
  }
}

extern "C" void kernel_launch(void* const* d_in, const int* in_sizes, int n_in, void* d_out, int out_size, void* d_ws, size_t ws_size,
                              hipStream_t stream) {
  fused_kernel<<<256, 768, 0, stream>>>((const u16*)d_in[0], (const u16*)d_in[1], (const u16*)d_in[2], (const u16*)d_in[3], (const u16*)d_in[4],
                                        (const u16*)d_in[5], (const u16*)d_in[6], (const u16*)d_in[7], (const u16*)d_in[8], (const u16*)d_in[9],
                                        (const u16*)d_in[10], (const u16*)d_in[11], (const u16*)d_in[12], (const u16*)d_in[13], (u16*)d_out,
                                        (char*)d_ws);
}
```

```cpp
#include <hip/hip_runtime.h>
#include <hip/hip_bf16.h>
#include <stdint.h>

typedef __bf16 bf16x8 __attribute__((ext_vector_type(8)));
typedef __bf16 bf16x16 __attribute__((ext_vector_type(16)));
typedef __bf16 bf16x2 __attribute__((ext_vector_type(2)));
typedef short s16x4 __attribute__((ext_vector_type(4)));
typedef short s16x8 __attribute__((ext_vector_type(8)));
typedef float f32x2 __attribute__((ext_vector_type(2)));
typedef float f32x4 __attribute__((ext_vector_type(4)));
typedef float f32x8 __attribute__((ext_vector_type(8)));
typedef float f32x16 __attribute__((ext_vector_type(16)));
typedef float f32x32 __attribute__((ext_vector_type(32)));
typedef unsigned u32x2 __attribute__((ext_vector_type(2)));
typedef unsigned u32x4 __attribute__((ext_vector_type(4)));
typedef unsigned short u16;
#define LAS __attribute__((address_space(3)))
#define DI __device__ __forceinline__
typedef LAS char* lptr;

constexpr int SEQ = 2048, DM = 576, NS = 128, OUTLD = 2 * DM, ROWB = DM * 2;
constexpr int CH_BYTES = 19456, KV_BYTES = 8 * CH_BYTES, CH_TAIL = 8192, CH_V = 9216, V_OCT = 320;
constexpr int R_BYTES = 4160, CV_U = 13312, CV_UCH = 4640 + 4544, CV_U1 = 4640, U_PADB = 448, UT_ROW = 4224;
constexpr size_t WS_E = 0, WS_F = 4096, WS_KV = WS_F + 256 * 128;
constexpr size_t WS_UT = WS_KV + KV_BYTES;
constexpr size_t WS_YT = WS_UT + (size_t)DM * UT_ROW;
constexpr size_t WS_O = WS_YT + (size_t)DM * SEQ * 2;
constexpr size_t WS_DBG = 64u << 20;
constexpr int LDS_BYTES = 163840, KV_LDS = 0, SPARE = KV_BYTES, CVB = 6 * CH_BYTES;
constexpr int A_UT = 0, A_WKV = 9216, A_X0 = 18432, A_Q = 36864, Q_ROW = 144, A_CTRL = A_Q + 64 * Q_ROW, A_MISC = A_CTRL + 128, A_SCR = A_MISC + 144, A_W = 80896, B_CTRL = LDS_BYTES - 64;
constexpr int CV_SCR = 0, C_REM = 73728, C_YT = 82944, C_RED = C_YT + 9216, C_OX = C_RED + 20480, C_RED2 = C_OX + 24576;
static_assert(A_SCR + 6 * 5712 <= A_W - 256 && A_SCR % 16 == 0 && A_W + 82944 == LDS_BYTES && CVB + CV_U + 3 * CV_UCH <= B_CTRL - 384 && C_OX + 24576 <= 7 * CH_BYTES + 1024 &&
              C_RED2 + 20480 <= B_CTRL - 384, "LDS map");
constexpr float QSCALE = 0.23570226039551584f * 1.4426950408889634f;
constexpr unsigned SENT = 0xffffffffu;
constexpr int SPIN_MAX = 1 << 16;
#define POLL1 12
#define POLL0 8
#define XLEAD 0u
#define CSLEEP 10
#define OSLEEP 12
#define ORETRY 8

DI float bf2f(u16 x) { return __uint_as_float((unsigned)x << 16); }
DI float bflo(unsigned x) { return __uint_as_float(x << 16); }
DI float bfhi(unsigned x) { return __uint_as_float(x & 0xffff0000u); }
DI unsigned pk2(float lo, float hi) { f32x2 v = {lo, hi}; return __builtin_bit_cast(unsigned, __builtin_convertvector(v, bf16x2)); }
DI float rbf(float x) { return bfhi(pk2(x, x)); }
DI unsigned nosent(unsigned x) { return x < 0xfffffffeu ? x : 0xfffffffeu; }
#define GLDS(SC) "s_mov_b32 %0, m0\n\ts_mov_b32 m0, %2\n\ts_nop 0\n\tglobal_load_lds_dwordx4 %1, off" SC "\n\ts_mov_b32 m0, %0"
DI void glds16(const void* g, unsigned l) { unsigned sv; asm volatile(GLDS("") : "=&s"(sv) : "v"(g), "s"(l) : "memory"); }
DI void glds16c(const void* g, unsigned l) { unsigned sv; asm volatile(GLDS(" sc1") : "=&s"(sv) : "v"(g), "s"(l) : "memory"); }
#define GLDSS(SC) "s_mov_b32 %0, m0\n\ts_mov_b32 m0, %3\n\ts_nop 0\n\tglobal_load_lds_dwordx4 %1, %2" SC "\n\ts_mov_b32 m0, %0"
DI void gldss(unsigned voff, const void* sbase, unsigned l) { unsigned sv; asm volatile(GLDSS("") : "=&s"(sv) : "v"(voff), "s"(sbase), "s"(l) : "memory"); }
DI void gldssc(unsigned voff, const void* sbase, unsigned l) { unsigned sv; asm volatile(GLDSS(" sc1") : "=&s"(sv) : "v"(voff), "s"(sbase), "s"(l) : "memory"); }
DI void glds9(const unsigned (&vo)[9], const void* sbase, unsigned l) {
  unsigned sv;
  asm volatile("s_mov_b32 %0, m0\n\ts_mov_b32 m0, %11\n\ts_nop 0\n\t"
               "global_load_lds_dwordx4 %1, %10\n\tglobal_load_lds_dwordx4 %2, %10 offset:1024\n\t"
               "global_load_lds_dwordx4 %3, %10 offset:2048\n\tglobal_load_lds_dwordx4 %4, %10 offset:3072\n\t"
               "s_add_u32 m0, m0, 0x1000\n\ts_nop 0\n\t"
               "global_load_lds_dwordx4 %5, %10\n\tglobal_load_lds_dwordx4 %6, %10 offset:1024\n\t"
               "global_load_lds_dwordx4 %7, %10 offset:2048\n\tglobal_load_lds_dwordx4 %8, %10 offset:3072\n\t"
               "s_add_u32 m0, m0, 0x1000\n\ts_nop 0\n\tglobal_load_lds_dwordx4 %9, %10\n\ts_mov_b32 m0, %0"
               : "=&s"(sv) : "v"(vo[0]), "v"(vo[1]), "v"(vo[2]), "v"(vo[3]), "v"(vo[4]), "v"(vo[5]), "v"(vo[6]), "v"(vo[7]), "v"(vo[8]),
                 "s"(sbase), "s"(l) : "memory", "scc");
}
DI void glds4c(unsigned voff, const void* sbase, unsigned l) {
  unsigned sv;
  asm volatile("s_mov_b32 %0, m0\n\ts_mov_b32 m0, %3\n\ts_nop 0\n\t"
               "global_load_lds_dwordx4 %1, %2 sc1\n\tglobal_load_lds_dwordx4 %1, %2 offset:1024 sc1\n\t"
               "global_load_lds_dwordx4 %1, %2 offset:2048 sc1\n\tglobal_load_lds_dwordx4 %1, %2 offset:3072 sc1\n\ts_mov_b32 m0, %0"
               : "=&s"(sv) : "v"(voff), "s"(sbase), "s"(l) : "memory");
}
DI void touch(const void* g, unsigned junk) {
  unsigned sv;
  asm volatile("s_mov_b32 %0, m0\n\ts_mov_b32 m0, %2\n\ts_nop 0\n\tglobal_load_lds_dword %1, off sc1\n\ts_mov_b32 m0, %0" : "=&s"(sv) : "v"(g), "s"(junk) : "memory");
}
template <int W> DI void flag_wait(const char* p, unsigned tag, u32x4& v) {
  if (__builtin_expect(!__all(v[W] == tag), 0)) {
#pragma unroll 1
    for (int spin = 0; spin < SPIN_MAX; ++spin) {
      __builtin_amdgcn_s_sleep(POLL1);
      if (v[W] != tag) asm volatile("global_load_dwordx4 %0, %1, off sc1\n\ts_waitcnt vmcnt(0)" : "+v"(v) : "v"(p) : "memory");
      if (__all(v[W] == tag)) break;
    }
  }
}
DI void touchs(unsigned voff, const void* sbase, unsigned junk) {
  unsigned sv;
  asm volatile("s_mov_b32 %0, m0\n\ts_mov_b32 m0, %3\n\ts_nop 0\n\tglobal_load_lds_dword %1, %2 sc1\n\ts_mov_b32 m0, %0" : "=&s"(sv) : "v"(voff), "s"(sbase), "s"(junk) : "memory");
}
DI void slice_offsets(unsigned (&vo)[9], int lane) {
#pragma unroll
  for (int r = 0; r < 9; ++r) {
    const int c = r * 64 + lane, row = c / 9, src = (row & ~7) + 4 * (row & 1) + ((row >> 1) & 3);
    vo[r] = src * ROWB + (c - row * 9) * 16 + 4096 - (r & 3) * 1024;
  }
}
DI void kv_group(const char* ws, unsigned lds0, int slot, int rot, int grp, unsigned l16) {
  const int off = grp < 4 ? grp * 4096 : 15360;
  glds4c(l16, ws + WS_KV + ((slot + rot) & 7) * CH_BYTES + off, lds0 + KV_LDS + slot * CH_BYTES + off);
}
DI void lds_wait(LAS unsigned* p, unsigned n) {
  if (__builtin_expect(__builtin_amdgcn_readfirstlane(__hip_atomic_load(p, __ATOMIC_RELAXED, __HIP_MEMORY_SCOPE_WORKGROUP)) < n, 0)) {
    do __builtin_amdgcn_s_sleep(1); while (__builtin_amdgcn_readfirstlane(__hip_atomic_load(p, __ATOMIC_RELAXED, __HIP_MEMORY_SCOPE_WORKGROUP)) < n);
  }
  asm volatile("" ::: "memory");
}
DI void st16c(void* p, u32x4 v) { asm volatile("global_store_dwordx4 %0, %1, off sc1\n\ts_nop 1" ::"v"(p), "v"(v) : "memory"); }
#define OST ""
DI void ost8(void* p, u32x2 v) { asm volatile("global_store_dwordx2 %0, %1, off" OST ::"v"(p), "v"(v) : "memory"); }
DI void ost16(void* p, u32x4 v) { asm volatile("global_store_dwordx4 %0, %1, off" OST "\n\ts_nop 1" ::"v"(p), "v"(v) : "memory"); }
DI void st8c(void* p, u32x2 v) { asm volatile("global_store_dwordx2 %0, %1, off sc1" ::"v"(p), "v"(v) : "memory"); }
DI void st4c(void* p, unsigned v) { asm volatile("global_store_dword %0, %1, off sc1" ::"v"(p), "v"(v) : "memory"); }
#define LD4(d, p, SC) asm volatile("global_load_dword %0, %1, off" SC : "=v"(d) : "v"(p) : "memory")
#define LD8(d, p, SC) asm volatile("global_load_dwordx2 %0, %1, off" SC : "=v"(d) : "v"(p) : "memory")
#define LD16(d, p, SC) asm volatile("global_load_dwordx4 %0, %1, off" SC : "=v"(d) : "v"(p) : "memory")
#define PIN(x) asm volatile("" : "+v"(x))
#define VMWAIT(n) do { asm volatile("s_waitcnt vmcnt(" #n ")" ::: "memory"); __builtin_amdgcn_sched_barrier(0); } while (0)
#define BAR() asm volatile("s_waitcnt lgkmcnt(0)\n\ts_barrier" ::: "memory")
#define BARVM(n) asm volatile("s_waitcnt vmcnt(" #n ") lgkmcnt(0)\n\ts_barrier" ::: "memory")
#define CFENCE() asm volatile("" ::: "memory")
DI s16x4 trd(lptr p) { return __builtin_amdgcn_ds_read_tr16_b64_v4i16((LAS s16x4*)p); }
DI bf16x8 trd2(lptr a, lptr b) { return __builtin_bit_cast(bf16x8, __builtin_shufflevector(trd(a), trd(b), 0, 1, 2, 3, 4, 5, 6, 7)); }
#define MFMA16(a, b, c) __builtin_amdgcn_mfma_f32_16x16x32_bf16((a), (b), (c), 0, 0, 0)
#define MFMA32(a, b, c) __builtin_amdgcn_mfma_f32_32x32x16_bf16((a), (b), (c), 0, 0, 0)
#define SMF(a, b, c) __builtin_amdgcn_smfmac_f32_32x32x32_bf16((a), (b), (c), (int)0xdddddddd, 0, 0)
template <int CTRL> DI unsigned dppu(unsigned x) { return (unsigned)__builtin_amdgcn_update_dpp((int)x, (int)x, CTRL, 0xf, 0xf, false); }
DI unsigned umax(unsigned a, unsigned b) { return a > b ? a : b; }
DI unsigned max8(unsigned x) { x = umax(x, dppu<0xB1>(x)); x = umax(x, dppu<0x4E>(x)); return umax(x, dppu<0x141>(x)); }
DI unsigned max16(unsigned x) { x = max8(x); return umax(x, dppu<0x140>(x)); }

#define STAMP(i)

DI f32x2 cmul(f32x2 a, f32x2 b) {
  const f32x2 t = (f32x2){a[0], a[0]} * b;
  return __builtin_elementwise_fma((f32x2){a[1], a[1]}, (f32x2){-b[1], b[0]}, t);
}
DI f32x2 cpow(float zr, float zi, float c) {
  const float e = __builtin_amdgcn_exp2f(zr * c), th = __builtin_amdgcn_fractf(zi * c);
  return e * (f32x2){__builtin_amdgcn_cosf(th), __builtin_amdgcn_sinf(th)};
}
constexpr int KG_ROW = 272, KG_TAB = 21 * KG_ROW;
struct kg_in { unsigned are, aim, bre, bim, cre, cim, ldt; };
DI void kgen_half(const kg_in& in, lptr tb, int lane, f32x4 (&d)[2][2]) {
  const float dt = rbf(expf(bflo(in.ldt)));
  const float zr = (dt * bflo(in.are)) * 1.4426950408889634f, zi = (dt * bflo(in.aim)) * 0.15915494309189535f;
  const f32x2 dB = {dt * bflo(in.bre), dt * bflo(in.bim)}, Cc = {bflo(in.cre), bflo(in.cim)};
  lptr tw = tb + lane * 4;
#define PUT(row, v) do { const f32x2 v_ = (v); *(LAS unsigned*)(tw + (row) * KG_ROW) = pk2(v_[0], v_[1]); } while (0)
  const f32x2 w1 = cpow(zr, -zi, 1.f), w8 = cpow(zr, zi, 8.f), w256 = cpow(zr, -zi, 256.f);
  const f32x2 w2 = cmul(w1, w1), w3 = cmul(w2, w1), w4 = cmul(w2, w2), w6 = cmul(w2, w4);
  *(LAS unsigned*)tw = 0x3f80u;
  PUT(1, w1); PUT(2, w2); PUT(3, w3); PUT(4, w4); PUT(5, cmul(w1, w4)); PUT(6, w6); PUT(7, cmul(w3, w4));
  PUT(8, w256); PUT(9, cmul(w2, w256)); PUT(10, cmul(w4, w256)); PUT(11, cmul(w6, w256));
  const f32x2 w512 = cmul(w256, w256), w768 = cmul(w512, w256), w1024 = cmul(w768, w256);
  PUT(12, w512); PUT(13, cmul(w512, w4)); PUT(14, w768); PUT(15, cmul(w768, w4));
  PUT(16, w1024); PUT(17, cmul(w256, w1024)); PUT(18, cmul(w512, w1024)); PUT(19, cmul(w768, w1024)); PUT(20, cmul(w1024, w1024));
  CFENCE();
  const int l15 = lane & 15, g = lane >> 4;
  lptr fb = tb + l15 * KG_ROW + g * 16, fb1 = tb + (16 + min(l15, 4)) * KG_ROW + g * 16;
  bf16x8 a0[4], a1[4];
#pragma unroll
  for (int kk = 0; kk < 4; ++kk) { a0[kk] = *(LAS const bf16x8*)(fb + kk * 64); a1[kk] = *(LAS const bf16x8*)(fb1 + kk * 64); }
  CFENCE();
  const f32x2 w16 = cmul(w8, w8), w32 = cmul(w16, w16), w64 = cmul(w32, w32);
  f32x2 bb[8];
  bb[0] = cmul(Cc, dB); bb[1] = cmul(bb[0], w8); bb[2] = cmul(bb[0], w16); bb[3] = cmul(bb[1], w16);
#pragma unroll
  for (int i = 0; i < 4; ++i) bb[4 + i] = cmul(bb[i], w32);
#pragma unroll
  for (int ct = 0; ct < 2; ++ct) {
#pragma unroll
    for (int i = 0; i < 2; ++i) {
#pragma unroll
      for (int j = 0; j < 8; ++j) PUT(8 * i + j, bb[j]);
      if (ct == 0 || i == 0) {
#pragma unroll
        for (int j = 0; j < 8; ++j) bb[j] = cmul(bb[j], w64);
      }
    }
    CFENCE();
    bf16x8 bf[4];
#pragma unroll
    for (int kk = 0; kk < 4; ++kk) bf[kk] = *(LAS const bf16x8*)(fb + kk * 64);
    CFENCE();
    d[ct][0] = (f32x4){0.f, 0.f, 0.f, 0.f}; d[ct][1] = d[ct][0];
#pragma unroll
    for (int kk = 0; kk < 4; ++kk) { d[ct][0] = MFMA16(a0[kk], bf[kk], d[ct][0]); d[ct][1] = MFMA16(a1[kk], bf[kk], d[ct][1]); }
  }
}
DI void kgen_finish(const f32x4 (&d)[2][2], lptr img, LAS unsigned* gate, lptr tb, int lane) {
  const int l15 = lane & 15, g = lane >> 4;
  {
    const int e0 = g < 2 ? 8 * l15 + 4 * g : (g == 2 ? 256 + 4 * l15 : 384 + 2 * l15), es = g < 2 ? 128 : (g == 2 ? 64 : 32), e1 = g == 3 ? 64 : 2;
    const int f0 = g == 0 ? 512 + l15 : (lane == 16 ? 640 : 700);
#pragma unroll
    for (int ct = 0; ct < 2; ++ct) {
      const lptr p = tb + (e0 + ct * es) * 2;
      *(LAS unsigned*)p = pk2(d[ct][0][0], d[ct][0][1]); *(LAS unsigned*)(p + e1 * 2) = pk2(d[ct][0][2], d[ct][0][3]);
      LAS u16* q = (LAS u16*)tb + f0 + ct * 16;
      const unsigned x0 = pk2(d[ct][1][0], d[ct][1][1]), x1 = pk2(d[ct][1][2], d[ct][1][3]);
      q[0] = (u16)x0; q[32] = (u16)(x0 >> 16); q[64] = (u16)x1; q[96] = (u16)(x1 >> 16);
    }
  }
  lds_wait(gate, 8u);
  LAS const u16* C = (LAS const u16*)tb;
  const int rl = 63 - lane;
#pragma unroll
  for (int r = 0; r < 2; ++r) {
    const unsigned a = C[512 + 64 * (1 - r) + rl], bb = C[513 + 64 * (1 - r) + rl];
    const unsigned aa = a | (a << 16), b2x = bb | (bb << 16);
    *(LAS u32x4*)(img + r * 1024 + lane * 16) = (u32x4){b2x, (rl & 1) ? b2x : (bb | (a << 16)), aa, aa};
  }
  {
    const unsigned ab = *(LAS const unsigned*)(C + 384 + 2 * rl), c = C[386 + 2 * rl];
    *(LAS u32x4*)(img + 2048 + lane * 16) = (u32x4){c | (c << 16), __builtin_amdgcn_perm(ab, ab, 0x03020302u), __builtin_amdgcn_perm(ab, ab, 0x01000302u),
                                                __builtin_amdgcn_perm(ab, ab, 0x01000100u)};
  }
  {
    const bool hi = lane < 32;
    const int e = hi ? 128 + 4 * rl : 8 * rl;
    const u32x2 w01 = *(LAS const u32x2*)(C + e), w23 = *(LAS const u32x2*)(C + e + 4);
    const unsigned o0 = hi ? __builtin_amdgcn_perm(w01[1], w23[0], 0x07060100u) : __builtin_amdgcn_perm(w23[1], w23[1], 0x01000302u);
    const unsigned o1 = __builtin_amdgcn_perm(w23[0], w01[1], hi ? 0x01000100u : 0x05040706u);
    const unsigned o2 = __builtin_amdgcn_perm(w01[1], w01[0], hi ? 0x03020504u : 0x05040706u);
    const unsigned o3 = __builtin_amdgcn_perm(w01[0], w01[0], hi ? 0x01000100u : 0x01000302u);
    *(LAS u32x4*)(img + 3072 + lane * 16) = (u32x4){o0, o1, o2, o3};
  }
  if (lane < 4) *(LAS u32x4*)(img + 4096 + lane * 16) = (u32x4){0u, 0u, 0u, 0u};
}

template <int NT> DI void qgemm(lptr lds, LAS unsigned* ctrl, const bf16x8 (&xa)[18], int mt, int lane) {
  const int l15 = lane & 15, g = lane >> 4;
  constexpr int nt0 = NT == 2 ? 3 : 0;
  f32x4 acc[NT];
#pragma unroll
  for (int i = 0; i < NT; ++i) acc[i] = (f32x4){0.f, 0.f, 0.f, 0.f};
  const lptr wq = lds + A_W + (g * 8 + (l15 >> 2) * 2) * 144 + (l15 & 3) * 8 + nt0 * 32;
  constexpr int PD = NT == 3 ? 4 : 6;
  bf16x8 wf[PD + 1][NT];
  unsigned stage = __builtin_amdgcn_readfirstlane(__hip_atomic_load(&ctrl[4], __ATOMIC_RELAXED, __HIP_MEMORY_SCOPE_WORKGROUP));
#define WNEED(n) do { if (__builtin_expect(stage < (n), 0)) {                                                                             \
      do stage = __builtin_amdgcn_readfirstlane(__hip_atomic_load(&ctrl[4], __ATOMIC_RELAXED, __HIP_MEMORY_SCOPE_WORKGROUP)); while (stage < (n)); } \
    CFENCE(); } while (0)
#pragma unroll
  for (int kk = 0; kk < 18 + PD; ++kk) {
    if (kk >= PD) {
#pragma unroll
      for (int i = 0; i < NT; ++i) acc[i] = MFMA16(wf[(kk - PD) % (PD + 1)][i], xa[kk - PD], acc[i]);
    }
    __builtin_amdgcn_sched_barrier(0);
    if (kk < 18) {
      if ((kk & 1) == 0) WNEED((unsigned)(kk / 2 + 1));
#pragma unroll
      for (int i = 0; i < NT; ++i) wf[kk % (PD + 1)][i] = trd2(wq + kk * 4608 + i * 32, wq + kk * 4608 + 144 + i * 32);
    }
    __builtin_amdgcn_sched_barrier(0);
  }
  LAS unsigned* qw = (LAS unsigned*)(lds + A_Q + (mt * 16 + l15) * Q_ROW + nt0 * 32 + g * 8);
#pragma unroll
  for (int i = 0; i < NT; ++i)
    if (nt0 + i < 4 || g < 2) *(LAS u32x2*)(qw + 8 * i) = (u32x2){pk2(acc[i][0] * QSCALE, acc[i][1] * QSCALE), pk2(acc[i][2] * QSCALE, acc[i][3] * QSCALE)};
  CFENCE();
  if (lane == 0) __hip_atomic_fetch_add(&ctrl[5], 1u, __ATOMIC_RELAXED, __HIP_MEMORY_SCOPE_WORKGROUP);
}

DI f32x32 qk_tile(lptr kp, const bf16x8& q0, const bf16x8& q1, unsigned kt, u32x2 qa) {
  const bf16x8 k = *(LAS const bf16x8*)kp;
  f32x16 z;
#pragma unroll
  for (int j = 0; j < 16; ++j) z[j] = 0.f;
  const f32x16 a = MFMA32(k, q0, z), b = MFMA32(k, q1, z);
  f32x32 s = __builtin_shufflevector(a, b, 0, 1, 2, 3, 4, 5, 6, 7, 8, 9, 10, 11, 12, 13, 14, 15, 16, 17, 18, 19, 20, 21, 22, 23, 24, 25, 26, 27, 28, 29, 30, 31);
  return __builtin_amdgcn_mfma_f32_32x32x4bf16_1k(__builtin_bit_cast(s16x4, (u32x2){kt, 0u}), __builtin_bit_cast(s16x4, qa), s, 0, 0, 0);
}
DI bf16x16 sub8(const f32x32& s, int o) {
  f32x8 t = {s[o], s[o + 1], s[o + 2], s[o + 3], s[o + 4], s[o + 5], s[o + 6], s[o + 7]};
  return __builtin_bit_cast(bf16x16, t);
}

__global__ __launch_bounds__(768, 3) void fused_kernel(const u16* __restrict__ U, const u16* __restrict__ Are, const u16* __restrict__ Aim,
                                                       const u16* __restrict__ Bre, const u16* __restrict__ Bim, const u16* __restrict__ Cre,
                                                       const u16* __restrict__ Cim, const u16* __restrict__ logdt, const u16* __restrict__ Dskip,
                                                       const u16* __restrict__ X, const u16* __restrict__ Wq, const u16* __restrict__ Wk,
                                                       const u16* __restrict__ Wv, const u16* __restrict__ Wo, u16* __restrict__ out,
                                                       char* __restrict__ ws) {
  __shared__ __attribute__((aligned(16))) char lds_raw[LDS_BYTES];
  const lptr lds = (lptr)lds_raw;
  const unsigned lds0 = (unsigned)(uintptr_t)lds_raw;
  STAMP(0);
  const int tid = threadIdx.x, lane = tid & 63, wid = __builtin_amdgcn_readfirstlane(tid >> 6);
  const int l15 = lane & 15, g = lane >> 4, r32 = lane & 31, h = lane >> 5, q4 = l15 >> 2, p4 = l15 & 3;
  const int b = blockIdx.x, qb = (b & 7) * 4 + (b >> 6), hg = (b >> 3) & 7;
  const int ce = 512 + (b >> 2), cq = b & 3, mt = wid & 3;
  char* const myflag = ws + WS_F + (qb * 8 + hg) * 128;
  LAS unsigned* ctrl = (LAS unsigned*)(lds + A_CTRL);
  LAS unsigned* bctl = (LAS unsigned*)(lds + B_CTRL);
#define LADD(i) do { if (lane == 0) __hip_atomic_fetch_add(&ctrl[i], 1u, __ATOMIC_RELAXED, __HIP_MEMORY_SCOPE_WORKGROUP); } while (0)
#define LSET(i, v) do { if (lane == 0) __hip_atomic_store(&ctrl[i], (v), __ATOMIC_RELAXED, __HIP_MEMORY_SCOPE_WORKGROUP); } while (0)
  {
    const int arg = wid >= 6 ? wid - 5 : (mt == 0 ? 9 : (wid < 4 ? 10 : 11 + (hg >> 2)));
    const char* base = ((const char* const __attribute__((address_space(4)))*)__builtin_amdgcn_kernarg_segment_ptr())[arg];
    const int o0 = wid >= 6 ? b * 512 : (mt == 0 ? qb * 73728 + hg * 9216 + wid * 1152 : (wid < 4 ? ((b >> 3) * 3 + wid - 1) * 6912 : (b >> 6) * 5184));
    const int st = wid >= 6 ? 16 : (mt == 0 ? 72 : (wid < 4 ? 108 : 80));
    unsigned vo = lane * st + o0;
    if (wid >= 6 && lane >= 32) vo = ce * 256 + r32 * 8;
    touchs(vo, base, lds0 + A_W - 256);
    const char* base2 = ((const char* const __attribute__((address_space(4)))*)__builtin_amdgcn_kernarg_segment_ptr())[(0x087F >> (4 * mt)) & 15];
    touchs(mt == 3 ? qb * 73728 + (wid >> 2) * 36000 : 0, base2, lds0 + A_W - 256);
  }
  if (tid < 32) ctrl[tid] = 0u;
  BAR();
  asm volatile("" :: "s"(U), "s"(Are), "s"(Aim), "s"(Bre), "s"(Bim), "s"(Cre), "s"(Cim), "s"(logdt), "s"(Dskip), "s"(X), "s"(Wq), "s"(Wk), "s"(Wv), "s"(Wo),
               "s"(out), "s"(ws));
  const int xoff = mt == 0 ? A_X0 : A_W + 9216 + mt * 18432;
  const unsigned xv0 = (lane >> 3) * ROWB + (((lane & 7) ^ (lane >> 4)) * 16), xv1 = (8 + (lane >> 3)) * ROWB + (((lane & 7) ^ (4 + (lane >> 4))) * 16);
  const char* xs = (const char*)X + (size_t)(qb * 64 + mt * 16) * ROWB;
  const bool kgw = mt != (wid < 4 ? 0 : 3) && (wid < 4 || wid >= 8);
  const int kc = wid < 4 ? wid - 1 : wid - 8, kch = kc < 2 ? 2 * b + kc : ce;
  int msc = kgw ? A_SCR + (2 * kc + (wid < 4 ? 1 : 0)) * KG_TAB + 49 * 16 : A_MISC + (wid == 11 ? 80 : (wid == 0 ? 64 : mt * 16));
  asm volatile("" : "+v"(msc)); msc = __builtin_amdgcn_readfirstlane(msc);
  if (wid >= 4 && wid < 8) {
    __builtin_amdgcn_s_setprio(3);
#pragma unroll
    for (int kb = 0; kb < 2; ++kb) { gldss(xv0, xs + kb * 128, lds0 + xoff + kb * 2048); gldss(xv1, xs + kb * 128, lds0 + xoff + kb * 2048 + 1024); }
  } else if (wid == 0) {
    __builtin_amdgcn_s_setprio(3);
    const char* wx = (const char*)(hg < 4 ? Wk : Wv) + 8 * (hg & 3);
#pragma unroll
    for (int i = 0; i < 8; ++i) gldss(lane * 36, wx + i * 64 * 36, lds0 + A_WKV + i * 1024);
    if (lane < 63) gldss(lane * 36, wx + 512 * 36, lds0 + A_WKV + 8192);
    if (lane == 0) {
      unsigned sv_;
      asm volatile("s_mov_b32 %0, m0\n\ts_mov_b32 m0, %3\n\ts_nop 0\n\tglobal_load_lds_dwordx3 %1, %2\n\ts_mov_b32 m0, %0"
                   : "=&s"(sv_) : "v"(575 * 36), "s"(wx), "s"(lds0 + A_WKV + 575 * 16) : "memory");
    }
  }
  {
    const int a6 = lane >> 3;
    const u16* pa = a6 < 2 ? (a6 == 0 ? Are : Aim) : (a6 < 4 ? (a6 == 2 ? Bre : Bim) : (a6 == 4 ? Cre : Cim));
    pa += kch * NS + (wid < 4 ? 64 : 0) + (lane & 7) * 8;
    if (lane == 48) pa = logdt + (kch & ~7);
    if (lane == 49) pa = (const u16*)(ws + WS_E);
    if (lane >= 50) pa = Dskip + (kch & ~7);
    if (lane >= (kgw ? 0 : 49) && lane < (kgw ? 51 : 50)) glds16c(pa, lds0 + msc - 49 * 16);
  }
  STAMP(17);
#define TAG() (*(LAS const unsigned*)(lds + msc) * 2654435761u + 0x9e3779b9u)
  auto kg_read = [&](kg_in& in) {
    LAS const u16* p = (LAS const u16*)(lds + msc - 49 * 16) + lane;
    in.are = p[0]; in.aim = p[64]; in.bre = p[128]; in.bim = p[192]; in.cre = p[256]; in.cim = p[320];
    in.ldt = ((LAS const u16*)(lds + msc - 16))[kch & 7];
    CFENCE();
  };
#define FRESH_LANE() int tid_ = threadIdx.x; asm volatile("" : "+v"(tid_));                                                          \
  const int lane = tid_ & 63, l15 = lane & 15, g = lane >> 4, r32 = lane & 31, h = lane >> 5, q4 = l15 >> 2, p4 = l15 & 3;             \
  (void)r32; (void)h; (void)q4; (void)p4; (void)g
  auto any_bad = [&]() {
    const u32x4 a = *(LAS const u32x4*)bctl, c = *(LAS const u32x4*)(bctl + 4);
    return __builtin_amdgcn_readfirstlane((int)(a[0] | a[1] | a[2] | a[3] | c[0] | c[1] | c[2] | c[3]));
  };

  if (wid < 8) {
    {
      const lptr xb0 = lds + xoff + l15 * 128 + ((g ^ (l15 >> 1)) * 16), xb1 = lds + xoff + l15 * 128 + (((4 + g) ^ (l15 >> 1)) * 16);
      bf16x8 xa[18];
      if (wid >= 4) {
#pragma unroll
        for (int kb = 2; kb < 9; ++kb) {
          gldss(xv0, xs + kb * 128, lds0 + xoff + kb * 2048); gldss(xv1, xs + kb * 128, lds0 + xoff + kb * 2048 + 1024);
        }
        STAMP(1);
        char* cbp = ws + WS_KV + (qb >> 2) * CH_BYTES;
        const int key0 = (qb & 3) * 64 + mt * 16;
        char* sp;
        if (hg < 4) {
          const int key = key0 + l15;
          sp = g == 0 ? cbp + (key >> 5) * 1024 + (hg >> 1) * 512 + (key & 31) * 16 + (hg & 1) * 8 : cbp + CH_TAIL + (key & 63) * 16 + (key >> 6) * 4;
        } else {
          const int key = key0 + 4 * g, dim = 4 * (hg - 4) + l15;
          const int vrow = l15 < 4 ? ((dim & 3) | ((dim & 4) << 1) | ((dim & 8) >> 1)) : 12 + l15;
          sp = cbp + CH_V + (key >> 3) * V_OCT + (key & 4) * 2 + vrow * 16;
        }
        PIN(sp);
        lds_wait(&ctrl[0], 1u);
        const lptr wb = lds + A_WKV + (g * 8 + q4) * 16 + (l15 & 1) * 8;
        bf16x8 wk[18];
#pragma unroll
        for (int i = 0; i < 18; ++i) wk[i] = trd2(wb + i * 512, wb + i * 512 + 64);
        STAMP(2);
        f32x4 acc = {0.f, 0.f, 0.f, 0.f}, acc2 = acc;
#define XGRP(kb0, n, KTYPE)                                                                                                    \
        { VMWAIT(n);                                                                                                           \
          if ((n) == 0) LSET(8 + mt, 1u);                                                                                      \
          _Pragma("unroll") for (int i = 0; i < 6; ++i) xa[2 * (kb0) + i] = *(LAS const bf16x8*)(((i & 1) ? xb1 : xb0) + ((kb0) + (i >> 1)) * 2048); \
          CFENCE();                                                                                                            \
          if ((n) == 0) LADD(12 + mt);                                                                                         \
          _Pragma("unroll") for (int i = 2 * (kb0); i < 2 * (kb0) + 6; i += 2) {                                                \
            if (KTYPE) { acc = MFMA16(wk[i], xa[i], acc); acc2 = MFMA16(wk[i + 1], xa[i + 1], acc2); }                          \
            else { acc = MFMA16(xa[i], wk[i], acc); acc2 = MFMA16(xa[i + 1], wk[i + 1], acc2); } } }
        if (hg < 4) { XGRP(0, 12, 1) XGRP(3, 6, 1) XGRP(6, 0, 1) } else { XGRP(0, 12, 0) XGRP(3, 6, 0) XGRP(6, 0, 0) }
        STAMP(3);
        acc += acc2;
        u32x2 v = {pk2(acc[0], acc[1]), pk2(acc[2], acc[3])};
        unsigned mv;
        if (hg < 4) {
          if (g == 0) st8c(sp, v);
          if (g == 1 && hg == 3) st4c(sp, v[0]);
          float n2 = bflo(v[0]) * bflo(v[0]) + bfhi(v[0]) * bfhi(v[0]);
          if (g == 0) n2 += bflo(v[1]) * bflo(v[1]) + bfhi(v[1]) * bfhi(v[1]);
          const unsigned m16 = max16(__float_as_uint(n2) & 0x7fffffffu);
          const float mq = __uint_as_float(__builtin_amdgcn_readlane(m16, 0)), mtl = __uint_as_float(__builtin_amdgcn_readlane(m16, 16));
          mv = __float_as_uint(hg == 3 ? mq + mtl : mq) & 0x7fffffffu;
        } else {
          unsigned am = umax(umax(v[0] << 16 & 0x7fffffffu, v[0] & 0x7fff0000u), umax(v[1] << 16 & 0x7fffffffu, v[1] & 0x7fff0000u));
          if (l15 >= 6) am = 0u;
          if (l15 == 6) v = (u32x2){0x3f803f80u, 0x3f803f80u};
          if (l15 == 7) v = (u32x2){0u, 0u};
          if (l15 < 4 || (hg == 7 && l15 < 8)) st8c(sp, v);
          am = max16(am);
          mv = umax(umax(__builtin_amdgcn_readlane(am, 0), __builtin_amdgcn_readlane(am, 16)), umax(__builtin_amdgcn_readlane(am, 32), __builtin_amdgcn_readlane(am, 48)));
        }
        if (lane == 0) __hip_atomic_fetch_max(&ctrl[2], mv, __ATOMIC_RELAXED, __HIP_MEMORY_SCOPE_WORKGROUP);
        STAMP(4);
      } else {
        if (wid == 0) {
          __builtin_amdgcn_s_setprio(2);
          unsigned vo[9];
          slice_offsets(vo, lane);
          VMWAIT(1);
          LSET(0, 1u);
          STAMP(1);
          glds9(vo, (const char*)U + (size_t)qb * 64 * ROWB + hg * 144 - 4096, lds0 + A_UT);
          const lptr rp = lds + A_UT + (((4 * g + p4) & 7) * 8 + 2 * q4) * 144 + (g >> 1) * 8;
          char* up = ws + WS_UT + (size_t)(hg * 72 + (lane >> 5) * 4 + (lane & 3)) * UT_ROW + 128 + qb * 128 + ((lane >> 2) & 7) * 16;
          VMWAIT(0);
          bf16x8 ut[9];
#pragma unroll
          for (int i = 0; i < 9; ++i) ut[i] = trd2(rp + i * 16, rp + i * 16 + 144);
          CFENCE();
#pragma unroll
          for (int i = 0; i < 9; ++i) st16c(up + i * 8 * UT_ROW, __builtin_bit_cast(u32x4, ut[i]));
          if (__builtin_expect(qb == 0, 0)) {
            st16c(ws + WS_UT + (size_t)(hg * 72 + lane) * UT_ROW + 112, (u32x4){0u, 0u, 0u, 0u});
            st16c(ws + WS_UT + (size_t)(hg * 72 + 64 + (lane & 7)) * UT_ROW + 112, (u32x4){0u, 0u, 0u, 0u});
          }
          STAMP(2);
        } else {
          __builtin_amdgcn_s_setprio(2);
          VMWAIT(0);
          kg_in in; kg_read(in);
          STAMP(1);
          f32x4 d[2][2];
          const lptr tb = lds + A_SCR + (2 * wid - 1) * KG_TAB;
          kgen_half(in, tb, lane, d);
#pragma unroll
          for (int i = 0; i < 4; ++i) *(LAS f32x4*)(tb + i * 1024 + lane * 16) = d[i >> 1][i & 1];
          CFENCE();
          LSET(15 + wid, 1u);
          VMWAIT(0);
          STAMP(2);
        }
        lds_wait(&ctrl[8 + mt], 1u);
#pragma unroll
        for (int kk = 0; kk < 18; ++kk) xa[kk] = *(LAS const bf16x8*)(((kk & 1) ? xb1 : xb0) + (kk >> 1) * 2048);
        CFENCE();
        LADD(12 + mt);
        STAMP(3);
        STAMP(4);
      }
      if (wid >= 4) {
        VMWAIT(0);
        if (lane == 0 && __hip_atomic_fetch_add(&ctrl[1], 1u, __ATOMIC_RELAXED, __HIP_MEMORY_SCOPE_WORKGROUP) == 3u)
          st8c(myflag, (u32x2){TAG(), __hip_atomic_load(&ctrl[2], __ATOMIC_RELAXED, __HIP_MEMORY_SCOPE_WORKGROUP)});
        __builtin_amdgcn_s_setprio(2);
        STAMP(5);
        qgemm<2>(lds, ctrl, xa, mt, lane);
      } else {
        VMWAIT(0);
        if (wid == 0 && lane == 0) st4c(myflag + 8, TAG());
        STAMP(5);
        qgemm<3>(lds, ctrl, xa, mt, lane);
      }
    }
    STAMP(8);
    FRESH_LANE();
    const int kp = wid >> 2, head = wid & 3;
    f32x16 o0, o1;
#pragma unroll
    for (int j = 0; j < 16; ++j) { o0[j] = 0.f; o1[j] = 0.f; }
    const unsigned ka0 = lds0 + KV_LDS + kp * 1024 + lane * 16, ta0 = lds0 + KV_LDS + CH_TAIL + kp * 512 + r32 * 16,
                   va0 = lds0 + KV_LDS + CH_V + kp * 4 * V_OCT + h * V_OCT + (r32 < 19 ? r32 : 19) * 16;
    unsigned oo = (r32 >> 4) * 18432 + l15 * 16 + h * 256;
    PIN(oo);
    u32x4 q0, q1; unsigned qtl;
    lds_wait(&ctrl[5], 8u);
    {
      LAS const unsigned *qp0 = (LAS const unsigned*)(lds + A_Q + (kp * 32 + r32) * Q_ROW + head * 36 + h * 16), *qp1 = qp0 + (32 - 64 * kp) * (Q_ROW / 4);
#pragma unroll
      for (int j = 0; j < 4; ++j) { q0[j] = qp0[j]; q1[j] = qp1[j]; }
      qtl = *(LAS const unsigned*)(lds + A_Q + (lane ^ (kp * 32)) * Q_ROW + head * 36 + 32);
    }
    CFENCE();
    LADD(6);
    const u32x2 qa = {qtl, 0u}, qc = {0u, qtl};
    float qn2;
    {
#define DOT2(x, c) __builtin_amdgcn_fdot2_f32_bf16(__builtin_bit_cast(bf16x2, x), __builtin_bit_cast(bf16x2, x), (c), false)
      const float nt = DOT2(qtl, 0.f);
      float n0 = h ? 0.f : nt, n1 = h ? nt : 0.f;
#pragma unroll
      for (int j = 0; j < 4; ++j) { n0 = DOT2(q0[j], n0); n1 = DOT2(q1[j], n1); }
      const auto sw = __builtin_amdgcn_permlane32_swap(__float_as_uint(n0), __float_as_uint(n1), false, false);
      qn2 = __uint_as_float(sw[0]) + __uint_as_float(sw[1]);
      PIN(qn2);
    }
    STAMP(9);
    lds_wait(&ctrl[7], 1u);
    STAMP(10);
    {
      int it = 8; unsigned ka = ka0, va = va0, ta = ta0;
      asm volatile(
      "ds_read_b128 v[128:131], %[ka]\n"
      "ds_read_b128 v[140:143], %[ta]\n"
      "v_mov_b32 v120, 0\n"
      "v_mov_b32 v121, 0\n"
      "v_mov_b32 v122, 0\n"
      "v_mov_b32 v123, 0\n"
      "v_mov_b32 v124, 0\n"
      "v_mov_b32 v125, 0\n"
      "v_mov_b32 v126, 0\n"
      "v_mov_b32 v127, 0\n"
      "v_mov_b32 v136, 0\n"
      "v_mov_b32 v137, 0\n"
      "v_mov_b32 v138, 0\n"
      "v_mov_b32 v139, 0\n"
      "s_waitcnt lgkmcnt(1)\n"
      "v_mfma_f32_32x32x16_bf16 v[64:79], v[128:131], %[q0], 0\n"
      "v_mfma_f32_32x32x16_bf16 v[80:95], v[128:131], %[q1], 0\n"
      "ds_read_b128 v[128:131], %[ka] offset:2048\n"
      "ds_read_b128 v[132:135], %[va]\n"
      "s_nop 7\n"
      "s_waitcnt lgkmcnt(2)\n"
      "v_mfma_f32_32x32x4_2b_bf16 v[64:95], v[140:141], %[qa], v[64:95]\n"
      "s_nop 15\n"
      "s_nop 1\n"
      "1:\n"
      "s_setprio 1\n"
      "s_waitcnt lgkmcnt(1)\n"
      "v_mfma_f32_32x32x16_bf16 v[96:111], v[128:131], %[q0], 0\n"
      "v_exp_f32 v64, v64\n"
      "v_exp_f32 v65, v65\n"
      "v_exp_f32 v66, v66\n"
      "v_smfmac_f32_32x32x32_bf16 %[o1], v[136:139], v[120:127], %[ix]\n"
      "ds_read_b128 v[136:139], %[va] offset:640\n"
      "v_exp_f32 v67, v67\n"
      "v_exp_f32 v68, v68\n"
      "v_exp_f32 v69, v69\n"
      "v_mfma_f32_32x32x16_bf16 v[112:127], v[128:131], %[q1], 0\n"
      "ds_read_b128 v[128:131], %[ka] offset:4096\n"
      "v_exp_f32 v70, v70\n"
      "v_exp_f32 v71, v71\n"
      "v_exp_f32 v72, v72\n"
      "v_exp_f32 v73, v73\n"
      "s_waitcnt lgkmcnt(2)\n"
      "v_smfmac_f32_32x32x32_bf16 %[o0], v[132:135], v[64:71], %[ix]\n"
      "v_exp_f32 v74, v74\n"
      "v_exp_f32 v75, v75\n"
      "v_exp_f32 v76, v76\n"
      "v_exp_f32 v77, v77\n"
      "v_exp_f32 v78, v78\n"
      "v_mfma_f32_32x32x4_2b_bf16 v[96:127], v[140:141], %[qc], v[96:127]\n"
      "v_exp_f32 v79, v79\n"
      "v_exp_f32 v80, v80\n"
      "v_exp_f32 v81, v81\n"
      "v_exp_f32 v82, v82\n"
      "v_exp_f32 v83, v83\n"
      "v_exp_f32 v84, v84\n"
      "v_exp_f32 v85, v85\n"
      "v_exp_f32 v86, v86\n"
      "s_waitcnt lgkmcnt(1)\n"
      "v_smfmac_f32_32x32x32_bf16 %[o0], v[136:139], v[72:79], %[ix]\n"
      "v_exp_f32 v87, v87\n"
      "v_exp_f32 v88, v88\n"
      "v_exp_f32 v89, v89\n"
      "v_exp_f32 v90, v90\n"
      "v_smfmac_f32_32x32x32_bf16 %[o1], v[132:135], v[80:87], %[ix]\n"
      "ds_read_b128 v[132:135], %[va] offset:2560\n"
      "v_exp_f32 v91, v91\n"
      "v_exp_f32 v92, v92\n"
      "v_exp_f32 v93, v93\n"
      "v_exp_f32 v94, v94\n"
      "v_exp_f32 v95, v95\n"
      "s_setprio 0\n"
      "s_waitcnt lgkmcnt(1)\n"
      "v_mfma_f32_32x32x16_bf16 v[64:79], v[128:131], %[q0], 0\n"
      "v_exp_f32 v96, v96\n"
      "v_exp_f32 v97, v97\n"
      "v_exp_f32 v98, v98\n"
      "v_smfmac_f32_32x32x32_bf16 %[o1], v[136:139], v[88:95], %[ix]\n"
      "ds_read_b128 v[136:139], %[va] offset:3200\n"
      "v_exp_f32 v99, v99\n"
      "v_exp_f32 v100, v100\n"
      "v_exp_f32 v101, v101\n"
      "v_mfma_f32_32x32x16_bf16 v[80:95], v[128:131], %[q1], 0\n"
      "ds_read_b128 v[128:131], %[ka] offset:6144\n"
      "v_exp_f32 v102, v102\n"
      "v_exp_f32 v103, v103\n"
      "v_exp_f32 v104, v104\n"
      "v_exp_f32 v105, v105\n"
      "s_waitcnt lgkmcnt(2)\n"
      "v_smfmac_f32_32x32x32_bf16 %[o0], v[132:135], v[96:103], %[ix]\n"
      "v_exp_f32 v106, v106\n"
      "v_exp_f32 v107, v107\n"
      "v_exp_f32 v108, v108\n"
      "v_exp_f32 v109, v109\n"
      "v_exp_f32 v110, v110\n"
      "v_mfma_f32_32x32x4_2b_bf16 v[64:95], v[142:143], %[qa], v[64:95]\n"
      "v_exp_f32 v111, v111\n"
      "v_exp_f32 v112, v112\n"
      "v_exp_f32 v113, v113\n"
      "v_exp_f32 v114, v114\n"
      "v_exp_f32 v115, v115\n"
      "v_exp_f32 v116, v116\n"
      "v_exp_f32 v117, v117\n"
      "v_exp_f32 v118, v118\n"
      "s_waitcnt lgkmcnt(1)\n"
      "v_smfmac_f32_32x32x32_bf16 %[o0], v[136:139], v[104:111], %[ix]\n"
      "v_exp_f32 v119, v119\n"
      "v_exp_f32 v120, v120\n"
      "v_exp_f32 v121, v121\n"
      "v_exp_f32 v122, v122\n"
      "v_smfmac_f32_32x32x32_bf16 %[o1], v[132:135], v[112:119], %[ix]\n"
      "ds_read_b128 v[132:135], %[va] offset:5120\n"
      "v_exp_f32 v123, v123\n"
      "v_exp_f32 v124, v124\n"
      "v_exp_f32 v125, v125\n"
      "v_exp_f32 v126, v126\n"
      "v_exp_f32 v127, v127\n"
      "s_setprio 3\n"
      "s_waitcnt lgkmcnt(1)\n"
      "v_mfma_f32_32x32x16_bf16 v[96:111], v[128:131], %[q0], 0\n"
      "v_exp_f32 v64, v64\n"
      "v_exp_f32 v65, v65\n"
      "v_exp_f32 v66, v66\n"
      "v_smfmac_f32_32x32x32_bf16 %[o1], v[136:139], v[120:127], %[ix]\n"
      "ds_read_b128 v[136:139], %[va] offset:5760\n"
      "v_exp_f32 v67, v67\n"
      "v_exp_f32 v68, v68\n"
      "v_exp_f32 v69, v69\n"
      "v_mfma_f32_32x32x16_bf16 v[112:127], v[128:131], %[q1], 0\n"
      "s_setprio 2\n"
      "s_waitcnt lgkmcnt(1)\n"
      "v_mfma_f32_32x32x16_bf16 v[64:79], v[128:131], %[q0], 0\n"
      "v_exp_f32 v96, v96\n"
      "v_exp_f32 v97, v97\n"
      "v_exp_f32 v98, v98\n"
      "v_smfmac_f32_32x32x32_bf16 %[o1], v[136:139], v[88:95], %[ix]\n"
      "ds_read_b128 v[136:139], %[va] offset:8320\n"
      "v_exp_f32 v99, v99\n"
      "v_exp_f32 v100, v100\n"
      "v_exp_f32 v101, v101\n"
      "v_mfma_f32_32x32x16_bf16 v[80:95], v[128:131], %[q1], 0\n"
      "ds_read_b128 v[128:131], %[ka] offset:21504\n"
      "v_exp_f32 v102, v102\n"
      "v_exp_f32 v103, v103\n"
      "v_exp_f32 v104, v104\n"
      "v_exp_f32 v105, v105\n"
      "s_waitcnt lgkmcnt(2)\n"
      "v_smfmac_f32_32x32x32_bf16 %[o0], v[132:135], v[96:103], %[ix]\n"
      "v_exp_f32 v106, v106\n"
      "v_exp_f32 v107, v107\n"
      "v_exp_f32 v108, v108\n"
      "v_exp_f32 v109, v109\n"
      "v_exp_f32 v110, v110\n"
      "v_mfma_f32_32x32x4_2b_bf16 v[64:95], v[140:141], %[qa], v[64:95]\n"
      "v_exp_f32 v111, v111\n"
      "v_exp_f32 v112, v112\n"
      "v_exp_f32 v113, v113\n"
      "v_exp_f32 v114, v114\n"
      "v_exp_f32 v115, v115\n"
      "v_exp_f32 v116, v116\n"
      "v_exp_f32 v117, v117\n"
      "v_exp_f32 v118, v118\n"
      "s_waitcnt lgkmcnt(1)\n"
      "v_smfmac_f32_32x32x32_bf16 %[o0], v[136:139], v[104:111], %[ix]\n"
      "v_exp_f32 v119, v119\n"
      "v_exp_f32 v120, v120\n"
      "v_exp_f32 v121, v121\n"
      "v_exp_f32 v122, v122\n"
      "v_smfmac_f32_32x32x32_bf16 %[o1], v[132:135], v[112:119], %[ix]\n"
      "ds_read_b128 v[132:135], %[va] offset:19456\n"
      "v_exp_f32 v123, v123\n"
      "v_exp_f32 v124, v124\n"
      "v_exp_f32 v125, v125\n"
      "v_exp_f32 v126, v126\n"
      "v_exp_f32 v127, v127\n"
      "v_add_u32 %[ka], 19456, %[ka]\n"
      "v_add_u32 %[va], 19456, %[va]\n"
      "v_add_u32 %[ta], 19456, %[ta]\n"
      "s_sub_u32 %[it], %[it], 1\n"
      "s_cmp_lg_u32 %[it], 0\n"
      "s_cbranch_scc1 1b\n"
      "v_smfmac_f32_32x32x32_bf16 %[o1], v[136:139], v[120:127], %[ix]\n"
      "s_setprio 0\n"
      "s_waitcnt lgkmcnt(0)\n"
      "s_nop 15\n"
          : [o0] "+v"(o0), [o1] "+v"(o1), [ka] "+v"(ka), [va] "+v"(va), [ta] "+v"(ta), [it] "+s"(it)
          : [q0] "v"(q0), [q1] "v"(q1), [qa] "v"(qa), [qc] "v"(qc), [ix] "v"(0xdddddddd)
          : "v64", "v65", "v66", "v67", "v68", "v69", "v70", "v71", "v72", "v73", "v74", "v75", "v76", "v77", "v78", "v79", "v80", "v81", "v82", "v83", "v84", "v85", "v86", "v87", "v88", "v89", "v90", "v91", "v92", "v93", "v94", "v95", "v96", "v97", "v98", "v99", "v100", "v101", "v102", "v103", "v104", "v105", "v106", "v107", "v108", "v109", "v110", "v111", "v112", "v113", "v114", "v115", "v116", "v117", "v118", "v119", "v120", "v121", "v122", "v123", "v124", "v125", "v126", "v127", "v128", "v129", "v130", "v131", "v132", "v133", "v134", "v135", "v136", "v137", "v138", "v139", "v140", "v141", "v142", "v143", "scc", "memory");
    }
    STAMP(13);
    const lptr xo = lds + C_OX + lane * 16;
    auto put_half = [&]() {
#pragma unroll
      for (int i = 0; i < 3; ++i) {
        *(LAS f32x4*)(xo + (wid * 3 + i) * 1024) = (f32x4){o1[4 * i], o1[4 * i + 1], o1[4 * i + 2], o1[4 * i + 3]};
      }
    };
    put_half();
    {
      const bool bad = __any(!(qn2 * __uint_as_float(bctl[8]) <= 8000.f));
      if (lane == 0) bctl[wid] = bad ? 1u : 0u;
    }
    BAR();
    f32x4 pp[3];
#pragma unroll
    for (int i = 0; i < 3; ++i) pp[i] = *(LAS const f32x4*)(xo + ((wid ^ 4) * 3 + i) * 1024);
    if (__builtin_expect(any_bad(), 0)) {
      BAR(); BAR();
      const bf16x8 qf0 = __builtin_bit_cast(bf16x8, q0), qf1 = __builtin_bit_cast(bf16x8, q1);
      float m0 = -INFINITY, m1 = -INFINITY;
#pragma unroll 1
      for (int i = 0; i < 32; ++i) {
        const int off = (i >> 2) * CH_BYTES;
        const f32x32 s = qk_tile((lptr)(uintptr_t)(ka0 + off + (i & 3) * 2048), qf0, qf1, *(LAS const unsigned*)(uintptr_t)(ta0 + off + (i & 3) * 4), qa);
#pragma unroll
        for (int j = 0; j < 16; ++j) { m0 = fmaxf(m0, s[j]); m1 = fmaxf(m1, s[16 + j]); }
      }
      m0 = fmaxf(m0, __shfl_xor(m0, 32)); m1 = fmaxf(m1, __shfl_xor(m1, 32));
      *(LAS f32x2*)(lds + SPARE + tid * 8) = (f32x2){m0, m1};
      BAR();
      const f32x2 mo = *(LAS const f32x2*)(lds + SPARE + (tid ^ 256) * 8);
      m0 = fmaxf(m0, mo[1]); m1 = fmaxf(m1, mo[0]);
#pragma unroll
      for (int j = 0; j < 16; ++j) { o0[j] = 0.f; o1[j] = 0.f; }
#pragma unroll 1
      for (int i = 0; i < 32; ++i) {
        const int off = (i >> 2) * CH_BYTES;
        f32x32 s = qk_tile((lptr)(uintptr_t)(ka0 + off + (i & 3) * 2048), qf0, qf1, *(LAS const unsigned*)(uintptr_t)(ta0 + off + (i & 3) * 4), qa);
#pragma unroll
        for (int j = 0; j < 16; ++j) { s[j] = __builtin_amdgcn_exp2f(s[j] - m0); s[16 + j] = __builtin_amdgcn_exp2f(s[16 + j] - m1); }
        const bf16x8 v0 = *(LAS const bf16x8*)(uintptr_t)(va0 + off + (i & 3) * 8 * V_OCT), v1 = *(LAS const bf16x8*)(uintptr_t)(va0 + off + ((i & 3) * 8 + 2) * V_OCT);
        o0 = SMF(v0, sub8(s, 0), o0); o0 = SMF(v1, sub8(s, 8), o0); o1 = SMF(v0, sub8(s, 16), o1); o1 = SMF(v1, sub8(s, 24), o1);
      }
      BAR();
      put_half();
      BAR();
#pragma unroll
      for (int i = 0; i < 3; ++i) pp[i] = *(LAS const f32x4*)(xo + ((wid ^ 4) * 3 + i) * 1024);
    }
    float f[12];
#pragma unroll
    for (int i = 0; i < 12; ++i) f[i] = o0[i] + pp[i >> 2][i & 3];
    const auto sw = __builtin_amdgcn_permlane32_swap(__float_as_uint(f[10]), __float_as_uint(f[10]), false, false);
    const float inv = __builtin_amdgcn_rcpf(__uint_as_float(sw[0]));
    const char* ob = ws + WS_O + (qb * 4 + kp * 2) * 18432 + hg * 2304;
    const u32x4 ov = {nosent(pk2(f[0] * inv, f[1] * inv)), nosent(pk2(f[2] * inv, f[3] * inv)), nosent(pk2(f[4] * inv, f[5] * inv)), nosent(pk2(f[6] * inv, f[7] * inv))};
    asm volatile("global_store_dwordx4 %0, %1, %2 sc1\n\ts_nop 1" ::"v"(oo), "v"(ov), "s"(ob + head * 512) : "memory");
    const unsigned ot = nosent(pk2(f[8] * inv, f[9] * inv));
    if (h == 0) asm volatile("global_store_dword %0, %1, %2 offset:2048 sc1" ::"v"(oo), "v"(ot), "s"(ob + head * 4) : "memory");
    STAMP(11);
  } else {
    const int sv = wid - 8, chl = sv >> 1, half = sv & 1, rot = b & 7;
    const unsigned l16 = lane * 16;
    unsigned vo[9];
    const char* wsrc = (const char*)Wq + hg * 144 - 4096;
    if (sv == 3) {
      slice_offsets(vo, lane);
#pragma unroll
      for (int a = 0; a < 3; ++a) glds9(vo, wsrc + a * 64 * ROWB, lds0 + A_W + a * 9216);
      touchs(1024 + (lane & 3) * (2u << 20), ws, lds0 + A_W - 256);
      VMWAIT(1); LSET(4, 3u);
    } else VMWAIT(0);
    const unsigned tag = __builtin_amdgcn_readfirstlane(TAG());
    const float dsk = bf2f(((LAS const u16*)(lds + msc + 16))[kch & 7]);
#define KVC(s0) do { kv_group(ws, lds0, (s0) + chl, rot, 2 * half, l16); kv_group(ws, lds0, (s0) + chl, rot, 2 * half + 1, l16);            \
                     if (half) kv_group(ws, lds0, (s0) + chl, rot, 4, l16); } while (0)
    const char* fp0 = ws + WS_F + (((rot + 2 * sv + 2 + h) & 7) * 32 + r32) * 128;
    u32x4 fl0;
    if (sv < 3) {
      __builtin_amdgcn_s_setprio(1);
      kg_in in; kg_read(in);
      STAMP(1);
      f32x4 d[2][2];
      const lptr tb = lds + A_SCR + 2 * sv * KG_TAB;
      kgen_half(in, tb, lane, d);
      VMWAIT(0);
      STAMP(2);
      lds_wait(&ctrl[16 + sv], 1u);
#pragma unroll
      for (int i = 0; i < 4; ++i) d[i >> 1][i & 1] += *(LAS const f32x4*)(tb + KG_TAB + i * 1024 + lane * 16);
      if (lane == 0) d[0][0][0] += dsk;
      kgen_finish(d, lds + CVB + sv * R_BYTES, &ctrl[5], tb, lane);
      asm volatile("global_load_dwordx4 %0, %1, off sc1\n\ts_waitcnt vmcnt(0)" : "=&v"(fl0) : "v"(fp0) : "memory");
      flag_wait<0>(fp0, tag, fl0);
      if (lane < 56) *(LAS u32x4*)(lds + CVB + CV_U + sv * CV_UCH + (lane < 28 ? lane * 16 : CV_U1 + (lane - 28) * 16)) = (u32x4){0u, 0u, 0u, 0u};
      STAMP(4);
      __builtin_amdgcn_s_setprio(3);
    } else {
      STAMP(1);
      __builtin_amdgcn_s_setprio(3);
#pragma unroll
      for (int a = 3; a < 9; ++a) {
        if (a & 1) lds_wait(&ctrl[12 + (a >> 1)], 2u);
        glds9(vo, wsrc + a * 64 * ROWB, lds0 + A_W + a * 9216);
      }
      STAMP(3);
      VMWAIT(36); LSET(4, 5u);
      VMWAIT(18); LSET(4, 7u);
      VMWAIT(9); LSET(4, 8u);
      VMWAIT(0); LSET(4, 9u);
      STAMP(4);
      const unsigned tg = tag;
      u32x4 fu;
      const char* fup = ws + WS_F + (r32 * 8 + (h ? 7 : b / 36)) * 128;
      asm volatile("global_load_dwordx4 %0, %1, off sc1\n\ts_waitcnt vmcnt(0)" : "=&v"(fl0) : "v"(fp0) : "memory");
#pragma unroll 1
      for (int spin = 0; spin < SPIN_MAX; ++spin) {
        if (__all(h == 1 || fl0[0] == tg)) break;
        __builtin_amdgcn_s_sleep(POLL0);
        if (h == 0 && fl0[0] != tg) asm volatile("global_load_dwordx4 %0, %1, off sc1\n\ts_waitcnt vmcnt(0)" : "+v"(fl0) : "v"(fp0) : "memory");
      }
      STAMP(5);
      lds_wait(&ctrl[12], 2u);
#pragma unroll
      for (int i = 0; i < 5; ++i) kv_group(ws, lds0, 0, rot, i, l16);
      LD16(fu, fup, " sc1");
      VMWAIT(1);
      LSET(7, 1u);
      STAMP(7);
      VMWAIT(0); PIN(fu);
      flag_wait<0>(fp0, tg, fl0);
      STAMP(9);
#pragma unroll
      for (int i = 0; i < 4; ++i) kv_group(ws, lds0, 1, rot, i, l16);
      flag_wait<2>(fup, tg, fu);
      lds_wait(&ctrl[5], 8u);
#pragma unroll
      for (int i = 0; i < 6; ++i)
        glds4c(l16, ws + WS_UT + (size_t)(i < 4 ? 2 * b + (i >> 1) : ce) * UT_ROW + 128 - ((i & 1) ? 0 : 2), lds0 + CVB + CV_U + (i >> 1) * CV_UCH + (i & 1) * CV_U1 + U_PADB);
      lds_wait(&ctrl[6], 8u);
      kv_group(ws, lds0, 1, rot, 4, l16);
      STAMP(10);
    }
    {
      unsigned m = fl0[1];
      m = umax(m, dppu<0x128>(m));
      { const auto t = __builtin_amdgcn_permlane16_swap(m, m, false, false); m = umax(t[0], t[1]); }
      { const auto t = __builtin_amdgcn_permlane32_swap(m, m, false, false); m = umax(t[0], t[1]); }
      if (lane < 8) bctl[-32 + sv * 8 + lane] = m;
    }
    const int rr = lane >> 3;
    const char* wo = (const char*)Wo + hg * 128;
    const unsigned wsw = ((lane & 7) ^ (2 * ((rr >> 1) & 1))) * 16;
    auto wo_group = [&](int gq) {
      const unsigned va = (rr * ROWB + wsw) ^ ((gq & 1) * 64), vb = (va ^ 64) + 8 * ROWB - 1024, vt = ((18 * (rr >> 1) + 16 + (rr & 1)) * ROWB + wsw) ^ ((gq & 1) * 64);
      const char* sb = wo + gq * 72 * ROWB;
      unsigned sv_;
      asm volatile("s_mov_b32 %0, m0\n\ts_add_u32 m0, %8, 0x2000\n\ts_nop 0\n\tglobal_load_lds_dwordx4 %3, %4\n\t"
                   "s_mov_b32 m0, %8\n\ts_nop 0\n\tglobal_load_lds_dwordx4 %1, %4\n\tglobal_load_lds_dwordx4 %2, %4 offset:1024\n\t"
                   "s_add_u32 m0, m0, 0x800\n\ts_nop 0\n\tglobal_load_lds_dwordx4 %1, %5\n\tglobal_load_lds_dwordx4 %2, %5 offset:1024\n\t"
                   "s_add_u32 m0, m0, 0x800\n\ts_nop 0\n\tglobal_load_lds_dwordx4 %1, %6\n\tglobal_load_lds_dwordx4 %2, %6 offset:1024\n\t"
                   "s_add_u32 m0, m0, 0x800\n\ts_nop 0\n\tglobal_load_lds_dwordx4 %1, %7\n\tglobal_load_lds_dwordx4 %2, %7 offset:1024\n\ts_mov_b32 m0, %0"
                   : "=&s"(sv_) : "v"(va), "v"(vb), "v"(vt), "s"(sb), "s"(sb + 18 * ROWB), "s"(sb + 36 * ROWB), "s"(sb + 54 * ROWB), "s"(lds0 + gq * 9216)
                   : "memory", "scc");
    };
    auto wo_rem = [&]() {
#pragma unroll 1
      for (int a = sv; a < 9; a += 4) {
        const int O = 8 * a + rr, gq = O / 9, o = O - gq * 9, e8 = lane & 7;
        gldss((gq * 72 + (o < 8 ? 18 * (o >> 1) + 8 * (o & 1) + e8 : 18 * (e8 >> 1) + 16 + (e8 & 1))) * ROWB, (const char*)Wo + 1024 + hg * 16, lds0 + C_REM + a * 1024);
      }
    };
    const int rofs = CVB + (1023 - l15 + 4 * g) * 4, uofs = CVB + CV_U + (l15 & 1) * CV_U1 + U_PADB + (32 * (l15 >> 1) + 8 * g) * 2;
    f32x4 acc[8], ea = {0.f, 0.f, 0.f, 0.f}, eb = ea;
#pragma unroll
    for (int c = 0; c < 8; ++c) acc[c] = ea;
#pragma unroll 1
    for (int iv = 0; iv < 4; ++iv) {
      STAMP(16);
      BARVM(0);
      STAMP(24);
      if (iv < 2) { KVC(2 + 2 * iv); }
      if (iv == 0) touch((const char*)Wo + ((b >> 3) * 162 + min(sv * 41 + lane, 161)) * 128, lds0 + B_CTRL - 384);
      if (iv == 1 && sv == 3) {
        const int l7 = lane & 7;
        const unsigned m = umax(umax(bctl[-32 + l7], bctl[-24 + l7]), umax(bctl[-16 + l7], bctl[-8 + l7]));
        const float k2 = __uint_as_float(__builtin_amdgcn_readlane(m, 0)) + __uint_as_float(__builtin_amdgcn_readlane(m, 1)) +
                         __uint_as_float(__builtin_amdgcn_readlane(m, 2)) + __uint_as_float(__builtin_amdgcn_readlane(m, 3));
        const float vf = __uint_as_float(umax(umax(__builtin_amdgcn_readlane(m, 4), __builtin_amdgcn_readlane(m, 5)), umax(__builtin_amdgcn_readlane(m, 6), __builtin_amdgcn_readlane(m, 7))));
        if (lane == 0) bctl[8] = (vf <= 1048576.f && vf >= 9.5367431640625e-7f) ? __float_as_uint(k2) : 0x7fc00000u;
      }
      if (iv == 3 && sv < 3) wo_group(3 + sv);
      if (iv == 2) {
        char* p = ws + WS_O + (qb * 4 + sv) * 18432 + 9 * hg * 256 + l16;
        st16c(p, (u32x4){SENT, SENT, SENT, SENT}); st16c(p + 1024, (u32x4){SENT, SENT, SENT, SENT});
        if (lane < 16) st16c(p + 2048, (u32x4){SENT, SENT, SENT, SENT});
      }
      {
        const int m0 = 4 * half + iv;
        const lptr rb = lds + chl * R_BYTES + rofs - 3584 - m0 * 64, ub = lds + chl * CV_UCH + uofs - m0 * 64;
        bf16x8 uf[8], kf[8];
#pragma unroll
        for (int z = 0; z < 8; ++z) {
          LAS const unsigned* rp = (LAS const unsigned*)(rb + 512 * (7 - z));
          kf[z] = __builtin_bit_cast(bf16x8, (u32x4){rp[0], rp[1], rp[2], rp[3]});
          uf[z] = *(LAS const bf16x8*)(ub + z * 512);
        }
        CFENCE();
#pragma unroll
        for (int c = 0; c < 8; ++c)
#pragma unroll
          for (int m1 = 0; m1 <= c; ++m1) acc[c] = MFMA16(uf[c - m1], kf[m1], acc[c]);
      }
      {
        const int m0 = 2 * sv + (iv >> 1), cB = 7 - cq;
        const lptr rb = lds + 2 * R_BYTES + rofs - m0 * 64, ub = lds + 2 * CV_UCH + uofs - m0 * 64;
        if ((iv & 1) == 0) {
#pragma unroll
          for (int i = 0; i < 5; ++i) {
            LAS const unsigned* rp = (LAS const unsigned*)(rb - 512 * i);
            eb = MFMA16(*(LAS const bf16x8*)(ub + 512 * (cB - i)), __builtin_bit_cast(bf16x8, (u32x4){rp[0], rp[1], rp[2], rp[3]}), eb);
          }
        } else {
          bf16x8 uf[4], kf[4];
#pragma unroll
          for (int i = 5; i < 9; ++i) {
            const bool isB = i <= cB;
            const int m1 = isB ? i : i - cB - 1, z = (isB ? cB : cq) - m1;
            LAS const unsigned* rp = (LAS const unsigned*)(rb - 512 * m1);
            kf[i - 5] = __builtin_bit_cast(bf16x8, (u32x4){rp[0], rp[1], rp[2], rp[3]});
            uf[i - 5] = *(LAS const bf16x8*)(ub + 512 * z);
          }
#pragma unroll
          for (int i = 5; i < 9; ++i) {
            if (i < 8 && i <= cB) eb = MFMA16(uf[i - 5], kf[i - 5], eb);
            else ea = MFMA16(uf[i - 5], kf[i - 5], ea);
          }
        }
      }
    }
    STAMP(6);
    const int ec = chl ? 7 - cq : cq;
    const lptr sc = lds + CV_SCR + l16;
    if (half) {
#pragma unroll
      for (int i = 0; i < 4; ++i) { *(LAS f32x4*)(sc + (sv * 4 + i) * 1024) = acc[i]; acc[i] = acc[4 + i]; }
    } else {
#pragma unroll
      for (int i = 0; i < 4; ++i) *(LAS f32x4*)(sc + (sv * 4 + i) * 1024) = acc[4 + i];
    }
    if (sv != 0) *(LAS f32x4*)(sc + 16384 + (sv - 1) * 1024) = ea;
    if (sv != 2) *(LAS f32x4*)(sc + 19456 + (sv > 2 ? 2 : sv) * 1024) = eb;
    STAMP(20);
    BAR();
    STAMP(27);
    KVC(6);
    if (half) wo_group(6 + chl);
    {
      const unsigned yo = g * 128 + l15 * 4;
      const char* yb = ws + WS_YT + (2 * b + chl) * 4096 + half * 2048;
#define YST(base, off, v) asm volatile("global_store_dword %0, %1, %2 offset:" #off " sc1" ::"v"(yo), "v"(v), "s"(base) : "memory")
#define YTILE(i, o0, o1) do { const f32x4 f = acc[i] + *(LAS const f32x4*)(sc + ((sv ^ 1) * 4 + (i)) * 1024); YST(yb, o0, pk2(f[0], f[1])); YST(yb, o1, pk2(f[2], f[3])); } while (0)
      YTILE(0, 0, 64); YTILE(1, 512, 576); YTILE(2, 1024, 1088); YTILE(3, 1536, 1600);
      if (half == 0) {
        f32x4 e = chl ? eb : ea;
#pragma unroll
        for (int s = 0; s < 3; ++s) e += *(LAS const f32x4*)(sc + 16384 + chl * 3072 + s * 1024);
        const char* ye = ws + WS_YT + ce * 4096 + ec * 512;
        YST(ye, 0, pk2(e[0], e[1])); YST(ye, 64, pk2(e[2], e[3]));
      }
    }
    STAMP(21);
    BARVM(0);
    STAMP(28);
    if (wid == 8 && lane == 0) st4c(myflag + 12, tag);
    if (sv) wo_group(sv - 1);
    wo_rem();
    const int tq = (qb >> 2) < 4 ? (qb >> 2) : 7 - (qb >> 2);
    const int pb0 = 32 * hg + (lane < 32 ? lane : 4 * (lane & 7) + tq);
    const char* f2a = ws + WS_F + (lane >= 48 ? qb * 8 + (lane & 7) : ((pb0 & 7) * 4 + (pb0 >> 6)) * 8 + ((pb0 >> 3) & 7)) * 128;
    const char* f2b = ws + WS_F + (sv * 64 + lane) * 128;
    u32x4 fa, fb = {tag, tag, tag, tag};
    LD16(fa, f2a, " sc1");
    if (b == 0) asm volatile("global_load_dwordx4 %0, %1, off sc1" : "+v"(fb) : "v"(f2b) : "memory");
    STAMP(22);
    BAR();
    STAMP(29);
    VMWAIT(0);
    PIN(fa); PIN(fb);
    flag_wait<3>(f2a, tag, fa); flag_wait<3>(f2b, tag, fb);
    {
      const char* yp = ws + WS_YT + hg * 64 * 4096 + qb * 128 + sv * 32;
      const unsigned yv = (lane >> 1) * 4096 + (lane & 1) * 16;
      gldssc(yv, yp, lds0 + C_YT + sv * 2304); gldssc(yv, yp + 32 * 4096, lds0 + C_YT + sv * 2304 + 1024);
      if (lane < 16) gldssc(yv, yp + (512 - hg * 56) * 4096, lds0 + C_YT + sv * 2304 + 2048);
    }
    VMWAIT(0);
    {
      const lptr rp = lds + C_YT + sv * 2304 + (((4 * g + p4) & 7) * 8 + q4) * 32 + (g >> 1) * 8, re = lds + C_YT + sv * 2304 + 2048 + q4 * 32 + p4 * 8;
      u16* orow = out + (size_t)(qb * 64 + sv * 16 + (lane & 3)) * OUTLD;
      u16* om = orow + h * 4 * OUTLD + hg * 64 + ((lane >> 2) & 7) * 8;
      const u32x4 v0 = __builtin_bit_cast(u32x4, trd2(rp, rp + 128)), v1 = __builtin_bit_cast(u32x4, trd2(rp + 16, rp + 144));
      const u32x4 ve = __builtin_bit_cast(u32x4, trd2(re, re + 128));
      ost16(om, v0); ost16(om + 8 * OUTLD, v1);
      if (lane < 16) ost16(orow + q4 * 4 * OUTLD + 512 + hg * 8, ve);
    }
    STAMP(23);
    BAR();
    STAMP(12);
    if (b == 0 && wid == 8 && lane == 0) st4c(ws + WS_E, tag);
    __builtin_amdgcn_s_setprio(0);
    VMWAIT(0);
    STAMP(13);
    BAR();
    if (__builtin_expect(any_bad(), 0)) { BAR(); _Pragma("unroll 1") for (int i = 0; i < 8; i += 2) KVC(i); BARVM(0); BAR(); BAR(); wo_group(sv); wo_group(sv + 4); wo_rem(); BARVM(0); }
  }
  {
    FRESH_LANE();
    const int kt = wid >> 2;
    bf16x8 wf[6][4];
    {
      const int sw = 2 * (((q4 >> 1) & 1) | ((g & 1) << 1));
      const lptr wb = lds + kt * 24576 + (g * 8 + q4) * 128 + (p4 & 1) * 8;
#pragma unroll
      for (int nt = 0; nt < 4; ++nt) {
        const lptr p = wb + (((2 * nt + (p4 >> 1)) ^ sw) * 16);
#pragma unroll
        for (int kk = 0; kk < 6; ++kk) wf[kk][nt] = trd2(p + kk * 4096, p + kk * 4096 + 512);
      }
    }
    bf16x8 wfr[6];
    {
      const lptr wr = lds + C_REM + kt * 3072 + (g * 8 + q4) * 16 + (p4 & 1) * 8;
#pragma unroll
      for (int kk = 0; kk < 6; ++kk) wfr[kk] = trd2(wr + kk * 512, wr + kk * 512 + 64);
    }
    const lptr rd = lds + C_RED2 + mt * 5120 + lane * 16, rd2 = lds + C_RED + mt * 5120 + lane * 16;
    u16* cp = out + (size_t)(qb * 64 + mt * 16 + l15) * OUTLD + DM + g * 4 + (kt < 2 ? hg * 64 + kt * 32 : 512 + hg * 8);
    const char* op = ws + WS_O + (size_t)((qb * 4 + mt) * 18 + kt * 6) * 1024 + lane * 16;
    const char* op2 = op + 3072;
    PIN(cp); PIN(op); PIN(op2);
    VMWAIT(0);
    if (__builtin_expect(wid >= 8, 0)) __builtin_amdgcn_s_sleep(OSLEEP);
    if (CSLEEP) __builtin_amdgcn_s_sleep(CSLEEP);
    STAMP(14);
    u32x4 of0, of1, of2, of3, of4, of5;
    asm volatile("global_load_dwordx4 %0, %6, off sc1\n\tglobal_load_dwordx4 %1, %6, off offset:1024 sc1\n\t"
                 "global_load_dwordx4 %2, %6, off offset:2048 sc1\n\tglobal_load_dwordx4 %3, %7, off sc1\n\t"
                 "global_load_dwordx4 %4, %7, off offset:1024 sc1\n\tglobal_load_dwordx4 %5, %7, off offset:2048 sc1\n\ts_waitcnt vmcnt(0)"
                 : "=&v"(of0), "=&v"(of1), "=&v"(of2), "=&v"(of3), "=&v"(of4), "=&v"(of5) : "v"(op), "v"(op2) : "memory");
#define MX4(x) umax(umax(x[0], x[1]), umax(x[2], x[3]))
    if (__builtin_expect(__any(umax(umax(umax(MX4(of0), MX4(of1)), umax(MX4(of2), MX4(of3))), umax(MX4(of4), MX4(of5))) == SENT), 0))
#pragma unroll 1
    for (int spin = 0; spin < SPIN_MAX; ++spin) {
      const bool m0 = umax(umax(MX4(of0), MX4(of1)), MX4(of2)) == SENT, m1 = umax(umax(MX4(of3), MX4(of4)), MX4(of5)) == SENT;
      if (!__any(m0 || m1)) break;
      __builtin_amdgcn_s_sleep(ORETRY);
      if (m0) asm volatile("global_load_dwordx4 %0, %3, off sc1\n\tglobal_load_dwordx4 %1, %3, off offset:1024 sc1\n\t"
                           "global_load_dwordx4 %2, %3, off offset:2048 sc1\n\ts_waitcnt vmcnt(0)" : "+v"(of0), "+v"(of1), "+v"(of2) : "v"(op) : "memory");
      if (m1) asm volatile("global_load_dwordx4 %0, %3, off sc1\n\tglobal_load_dwordx4 %1, %3, off offset:1024 sc1\n\t"
                           "global_load_dwordx4 %2, %3, off offset:2048 sc1\n\ts_waitcnt vmcnt(0)" : "+v"(of3), "+v"(of4), "+v"(of5) : "v"(op2) : "memory");
    }
    const u32x4 of[6] = {of0, of1, of2, of3, of4, of5};
    STAMP(15);
    f32x4 acc[5];
#pragma unroll
    for (int nt = 0; nt < 5; ++nt) acc[nt] = (f32x4){0.f, 0.f, 0.f, 0.f};
#pragma unroll
    for (int kk = 0; kk < 6; ++kk) {
#pragma unroll
      for (int nt = 0; nt < 4; ++nt) acc[nt] = MFMA16(wf[kk][nt], __builtin_bit_cast(bf16x8, of[kk]), acc[nt]);
      acc[4] = MFMA16(wfr[kk], __builtin_bit_cast(bf16x8, of[kk]), acc[4]);
    }
#define LW(p, v) *(LAS f32x4*)(p) = (v)
#define LR(p) *(LAS const f32x4*)(p)
    if (kt == 0) { LW(rd + 2048, acc[2]); LW(rd + 3072, acc[3]); LW(rd2 + 4096, acc[4]); }
    else if (kt == 1) { LW(rd2, acc[0]); LW(rd2 + 1024, acc[1]); LW(rd + 4096, acc[4]); acc[0] = acc[2]; acc[1] = acc[3]; }
    else { LW(rd, acc[0]); LW(rd + 1024, acc[1]); LW(rd2 + 2048, acc[2]); LW(rd2 + 3072, acc[3]); acc[0] = acc[4]; }
    BAR();
    const f32x4 v = acc[0] + LR(rd + kt * 2048) + LR(rd2 + kt * 2048), w = acc[1] + LR(rd + kt * 2048 + 1024) + LR(rd2 + kt * 2048 + 1024);
    if (kt < 2 || g < 2) ost8(cp, (u32x2){pk2(v[0], v[1]), pk2(v[2], v[3])});
    if (kt < 2) ost8(cp + 16, (u32x2){pk2(w[0], w[1]), pk2(w[2], w[3])});
  }
}

extern "C" void kernel_launch(void* const* d_in, const int* in_sizes, int n_in, void* d_out, int out_size, void* d_ws, size_t ws_size,
                              hipStream_t stream) {
  fused_kernel<<<256, 768, 0, stream>>>((const u16*)d_in[0], (const u16*)d_in[1], (const u16*)d_in[2], (const u16*)d_in[3], (const u16*)d_in[4],
                                        (const u16*)d_in[5], (const u16*)d_in[6], (const u16*)d_in[7], (const u16*)d_in[8], (const u16*)d_in[9],
                                        (const u16*)d_in[10], (const u16*)d_in[11], (const u16*)d_in[12], (const u16*)d_in[13], (u16*)d_out,
                                        (char*)d_ws);
}
```

```cpp
#include <hip/hip_runtime.h>
#include <hip/hip_bf16.h>
#include <stdint.h>

typedef __bf16 bf16x8 __attribute__((ext_vector_type(8)));
typedef __bf16 bf16x16 __attribute__((ext_vector_type(16)));
typedef __bf16 bf16x2 __attribute__((ext_vector_type(2)));
typedef short s16x4 __attribute__((ext_vector_type(4)));
typedef short s16x8 __attribute__((ext_vector_type(8)));
typedef float f32x2 __attribute__((ext_vector_type(2)));
typedef float f32x4 __attribute__((ext_vector_type(4)));
typedef float f32x8 __attribute__((ext_vector_type(8)));
typedef float f32x16 __attribute__((ext_vector_type(16)));
typedef float f32x32 __attribute__((ext_vector_type(32)));
typedef unsigned u32x2 __attribute__((ext_vector_type(2)));
typedef unsigned u32x4 __attribute__((ext_vector_type(4)));
typedef unsigned short u16;
#define LAS __attribute__((address_space(3)))
#define DI __device__ __forceinline__
typedef LAS char* lptr;

constexpr int SEQ = 2048, DM = 576, NS = 128, OUTLD = 2 * DM, ROWB = DM * 2;
constexpr int CH_BYTES = 19456, KV_BYTES = 8 * CH_BYTES, CH_TAIL = 8192, CH_V = 9216, V_OCT = 320;
constexpr int R_BYTES = 4160, CV_U = 3 * 4160, CV_UE = CV_U + 2 * (4640 + 4544), CV_UE1 = 4640 + 1536, UE_PADB = 448 + 1536, CV_UCH = 4640 + 4544, CV_U1 = 4640, U_PADB = 448, UT_ROW = 4224;
constexpr size_t WS_E = 0, WS_F = 4096, WS_KV = WS_F + 256 * 128;
constexpr size_t WS_UT = WS_KV + KV_BYTES;
constexpr size_t WS_YT = WS_UT + (size_t)DM * UT_ROW;
constexpr size_t WS_O = WS_YT + (size_t)DM * SEQ * 2;
constexpr size_t WS_DBG = 64u << 20;
constexpr int LDS_BYTES = 163840, KV_LDS = 0, SPARE = KV_BYTES, CVB = 6 * CH_BYTES;
constexpr int A_UT = 0, A_WKV = 9216, A_X0 = 18432, A_Q = 36864, Q_ROW = 144, A_CTRL = A_Q + 64 * Q_ROW, A_MISC = A_CTRL + 128, A_SCR = A_MISC + 144, A_W = 80896, B_CTRL = LDS_BYTES - 64;
constexpr int CV_SCR = 0, C_REM = 73728, C_YT = 82944, C_RED = C_YT + 9216, C_OX = C_RED + 20480, C_RED2 = C_OX + 24576;
static_assert(A_SCR + 6 * 5712 <= A_W - 256 && A_SCR % 16 == 0 && A_W + 82944 == LDS_BYTES && CVB + CV_UE + CV_UE1 + UE_PADB + 4096 <= B_CTRL - 448 && C_OX + 24576 <= 7 * CH_BYTES + 1024 &&
              C_RED2 + 20480 <= B_CTRL - 448, "LDS map");
constexpr float QSCALE = 0.23570226039551584f * 1.4426950408889634f;
constexpr unsigned SENT = 0xffffffffu;
constexpr int SPIN_MAX = 1 << 16;
#define POLL1 12
#define POLL0 8
#define XLEAD 0u
#define CSLEEP 8
#define KPRIO 3
#define SPRIO 3
#define GPRIO 3
#define FGATE 5
#define FGATEV 1u
#define YLATE 1
#define ROT 0
#define TOUCH2 1
#define OSLEEP 8
#define ORETRY 8

DI float bf2f(u16 x) { return __uint_as_float((unsigned)x << 16); }
DI float bflo(unsigned x) { return __uint_as_float(x << 16); }
DI float bfhi(unsigned x) { return __uint_as_float(x & 0xffff0000u); }
DI unsigned pk2(float lo, float hi) { f32x2 v = {lo, hi}; return __builtin_bit_cast(unsigned, __builtin_convertvector(v, bf16x2)); }
DI float rbf(float x) { return bfhi(pk2(x, x)); }
DI unsigned nosent(unsigned x) { return x < 0xfffffffeu ? x : 0xfffffffeu; }
#define GLDS(SC) "s_mov_b32 %0, m0\n\ts_mov_b32 m0, %2\n\ts_nop 0\n\tglobal_load_lds_dwordx4 %1, off" SC "\n\ts_mov_b32 m0, %0"
DI void glds16(const void* g, unsigned l) { unsigned sv; asm volatile(GLDS("") : "=&s"(sv) : "v"(g), "s"(l) : "memory"); }
DI void glds16c(const void* g, unsigned l) { unsigned sv; asm volatile(GLDS(" sc1") : "=&s"(sv) : "v"(g), "s"(l) : "memory"); }
#define GLDSS(SC) "s_mov_b32 %0, m0\n\ts_mov_b32 m0, %3\n\ts_nop 0\n\tglobal_load_lds_dwordx4 %1, %2" SC "\n\ts_mov_b32 m0, %0"
DI void gldss(unsigned voff, const void* sbase, unsigned l) { unsigned sv; asm volatile(GLDSS("") : "=&s"(sv) : "v"(voff), "s"(sbase), "s"(l) : "memory"); }
DI void gldssc(unsigned voff, const void* sbase, unsigned l) { unsigned sv; asm volatile(GLDSS(" sc1") : "=&s"(sv) : "v"(voff), "s"(sbase), "s"(l) : "memory"); }
DI void glds9(const unsigned (&vo)[9], const void* sbase, unsigned l) {
  unsigned sv;
  asm volatile("s_mov_b32 %0, m0\n\ts_mov_b32 m0, %11\n\ts_nop 0\n\t"
               "global_load_lds_dwordx4 %1, %10\n\tglobal_load_lds_dwordx4 %2, %10 offset:1024\n\t"
               "global_load_lds_dwordx4 %3, %10 offset:2048\n\tglobal_load_lds_dwordx4 %4, %10 offset:3072\n\t"
               "s_add_u32 m0, m0, 0x1000\n\ts_nop 0\n\t"
               "global_load_lds_dwordx4 %5, %10\n\tglobal_load_lds_dwordx4 %6, %10 offset:1024\n\t"
               "global_load_lds_dwordx4 %7, %10 offset:2048\n\tglobal_load_lds_dwordx4 %8, %10 offset:3072\n\t"
               "s_add_u32 m0, m0, 0x1000\n\ts_nop 0\n\tglobal_load_lds_dwordx4 %9, %10\n\ts_mov_b32 m0, %0"
               : "=&s"(sv) : "v"(vo[0]), "v"(vo[1]), "v"(vo[2]), "v"(vo[3]), "v"(vo[4]), "v"(vo[5]), "v"(vo[6]), "v"(vo[7]), "v"(vo[8]),
                 "s"(sbase), "s"(l) : "memory", "scc");
}
DI void glds4c(unsigned voff, const void* sbase, unsigned l) {
  unsigned sv;
  asm volatile("s_mov_b32 %0, m0\n\ts_mov_b32 m0, %3\n\ts_nop 0\n\t"
               "global_load_lds_dwordx4 %1, %2 sc1\n\tglobal_load_lds_dwordx4 %1, %2 offset:1024 sc1\n\t"
               "global_load_lds_dwordx4 %1, %2 offset:2048 sc1\n\tglobal_load_lds_dwordx4 %1, %2 offset:3072 sc1\n\ts_mov_b32 m0, %0"
               : "=&s"(sv) : "v"(voff), "s"(sbase), "s"(l) : "memory");
}
DI void touch(const void* g, unsigned junk) {
  unsigned sv;
  asm volatile("s_mov_b32 %0, m0\n\ts_mov_b32 m0, %2\n\ts_nop 0\n\tglobal_load_lds_dword %1, off sc1\n\ts_mov_b32 m0, %0" : "=&s"(sv) : "v"(g), "s"(junk) : "memory");
}
template <int W> DI void flag_wait(const char* p, unsigned tag, u32x4& v) {
  if (__builtin_expect(!__all(v[W] == tag), 0)) {
#pragma unroll 1
    for (int spin = 0; spin < SPIN_MAX; ++spin) {
      __builtin_amdgcn_s_sleep(POLL1);
      if (v[W] != tag) asm volatile("global_load_dwordx4 %0, %1, off sc1\n\ts_waitcnt vmcnt(0)" : "+v"(v) : "v"(p) : "memory");
      if (__all(v[W] == tag)) break;
    }
  }
}
DI void touchs(unsigned voff, const void* sbase, unsigned junk) {
  unsigned sv;
  asm volatile("s_mov_b32 %0, m0\n\ts_mov_b32 m0, %3\n\ts_nop 0\n\tglobal_load_lds_dword %1, %2 sc1\n\ts_mov_b32 m0, %0" : "=&s"(sv) : "v"(voff), "s"(sbase), "s"(junk) : "memory");
}
DI void slice_offsets(unsigned (&vo)[9], int lane) {
#pragma unroll
  for (int r = 0; r < 9; ++r) {
    const int c = r * 64 + lane, row = c / 9, src = (row & ~7) + 4 * (row & 1) + ((row >> 1) & 3);
    vo[r] = src * ROWB + (c - row * 9) * 16 + 4096 - (r & 3) * 1024;
  }
}
DI void kv_group(const char* ws, unsigned lds0, int slot, int rot, int grp, unsigned l16) {
  const int off = grp < 4 ? grp * 4096 : 15360;
  glds4c(l16, ws + WS_KV + ((slot + rot) & 7) * CH_BYTES + off, lds0 + KV_LDS + slot * CH_BYTES + off);
}
DI void lds_wait(LAS unsigned* p, unsigned n) {
  if (__builtin_expect(__builtin_amdgcn_readfirstlane(__hip_atomic_load(p, __ATOMIC_RELAXED, __HIP_MEMORY_SCOPE_WORKGROUP)) < n, 0)) {
    do __builtin_amdgcn_s_sleep(1); while (__builtin_amdgcn_readfirstlane(__hip_atomic_load(p, __ATOMIC_RELAXED, __HIP_MEMORY_SCOPE_WORKGROUP)) < n);
  }
  asm volatile("" ::: "memory");
}
DI void st16c(void* p, u32x4 v) { asm volatile("global_store_dwordx4 %0, %1, off sc1\n\ts_nop 1" ::"v"(p), "v"(v) : "memory"); }
#define OST ""
DI void ost8(void* p, u32x2 v) { asm volatile("global_store_dwordx2 %0, %1, off" OST ::"v"(p), "v"(v) : "memory"); }
DI void ost16(void* p, u32x4 v) { asm volatile("global_store_dwordx4 %0, %1, off" OST "\n\ts_nop 1" ::"v"(p), "v"(v) : "memory"); }
DI void st8c(void* p, u32x2 v) { asm volatile("global_store_dwordx2 %0, %1, off sc1" ::"v"(p), "v"(v) : "memory"); }
DI void st4c(void* p, unsigned v) { asm volatile("global_store_dword %0, %1, off sc1" ::"v"(p), "v"(v) : "memory"); }
#define LD4(d, p, SC) asm volatile("global_load_dword %0, %1, off" SC : "=v"(d) : "v"(p) : "memory")
#define LD8(d, p, SC) asm volatile("global_load_dwordx2 %0, %1, off" SC : "=v"(d) : "v"(p) : "memory")
#define LD16(d, p, SC) asm volatile("global_load_dwordx4 %0, %1, off" SC : "=v"(d) : "v"(p) : "memory")
#define PIN(x) asm volatile("" : "+v"(x))
#define VMWAIT(n) do { asm volatile("s_waitcnt vmcnt(" #n ")" ::: "memory"); __builtin_amdgcn_sched_barrier(0); } while (0)
#define BAR() asm volatile("s_waitcnt lgkmcnt(0)\n\ts_barrier" ::: "memory")
#define BARVM(n) asm volatile("s_waitcnt vmcnt(" #n ") lgkmcnt(0)\n\ts_barrier" ::: "memory")
#define CFENCE() asm volatile("" ::: "memory")
DI s16x4 trd(lptr p) { return __builtin_amdgcn_ds_read_tr16_b64_v4i16((LAS s16x4*)p); }
DI bf16x8 trd2(lptr a, lptr b) { return __builtin_bit_cast(bf16x8, __builtin_shufflevector(trd(a), trd(b), 0, 1, 2, 3, 4, 5, 6, 7)); }
#define MFMA16(a, b, c) __builtin_amdgcn_mfma_f32_16x16x32_bf16((a), (b), (c), 0, 0, 0)
#define MFMA32(a, b, c) __builtin_amdgcn_mfma_f32_32x32x16_bf16((a), (b), (c), 0, 0, 0)
#define SMF(a, b, c) __builtin_amdgcn_smfmac_f32_32x32x32_bf16((a), (b), (c), (int)0xdddddddd, 0, 0)
template <int CTRL> DI unsigned dppu(unsigned x) { return (unsigned)__builtin_amdgcn_update_dpp((int)x, (int)x, CTRL, 0xf, 0xf, false); }
DI unsigned umax(unsigned a, unsigned b) { return a > b ? a : b; }
DI unsigned max8(unsigned x) { x = umax(x, dppu<0xB1>(x)); x = umax(x, dppu<0x4E>(x)); return umax(x, dppu<0x141>(x)); }
DI unsigned max16(unsigned x) { x = max8(x); return umax(x, dppu<0x140>(x)); }

#define STAMP(i)

DI f32x2 cmul(f32x2 a, f32x2 b) {
  const f32x2 t = (f32x2){a[0], a[0]} * b;
  return __builtin_elementwise_fma((f32x2){a[1], a[1]}, (f32x2){-b[1], b[0]}, t);
}
DI f32x2 cpow(float zr, float zi, float c) {
  const float e = __builtin_amdgcn_exp2f(zr * c), th = __builtin_amdgcn_fractf(zi * c);
  return e * (f32x2){__builtin_amdgcn_cosf(th), __builtin_amdgcn_sinf(th)};
}
constexpr int KG_ROW = 272, KG_TAB = 21 * KG_ROW;
struct kg_in { unsigned are, aim, bre, bim, cre, cim, ldt; };
DI void kgen_half(const kg_in& in, lptr tb, int lane, f32x4 (&d)[2][2]) {
  const float dt = rbf(expf(bflo(in.ldt)));
  const float zr = (dt * bflo(in.are)) * 1.4426950408889634f, zi = (dt * bflo(in.aim)) * 0.15915494309189535f;
  const f32x2 dB = {dt * bflo(in.bre), dt * bflo(in.bim)}, Cc = {bflo(in.cre), bflo(in.cim)};
  lptr tw = tb + lane * 4;
#define PUT(row, v) do { const f32x2 v_ = (v); *(LAS unsigned*)(tw + (row) * KG_ROW) = pk2(v_[0], v_[1]); } while (0)
  const f32x2 w1 = cpow(zr, -zi, 1.f), w8 = cpow(zr, zi, 8.f), w256 = cpow(zr, -zi, 256.f);
  const f32x2 w2 = cmul(w1, w1), w3 = cmul(w2, w1), w4 = cmul(w2, w2), w6 = cmul(w2, w4);
  *(LAS unsigned*)tw = 0x3f80u;
  PUT(1, w1); PUT(2, w2); PUT(3, w3); PUT(4, w4); PUT(5, cmul(w1, w4)); PUT(6, w6); PUT(7, cmul(w3, w4));
  PUT(8, w256); PUT(9, cmul(w2, w256)); PUT(10, cmul(w4, w256)); PUT(11, cmul(w6, w256));
  const f32x2 w512 = cmul(w256, w256), w768 = cmul(w512, w256), w1024 = cmul(w768, w256);
  PUT(12, w512); PUT(13, cmul(w512, w4)); PUT(14, w768); PUT(15, cmul(w768, w4));
  PUT(16, w1024); PUT(17, cmul(w256, w1024)); PUT(18, cmul(w512, w1024)); PUT(19, cmul(w768, w1024)); PUT(20, cmul(w1024, w1024));
  CFENCE();
  const int l15 = lane & 15, g = lane >> 4;
  lptr fb = tb + l15 * KG_ROW + g * 16, fb1 = tb + (16 + min(l15, 4)) * KG_ROW + g * 16;
  bf16x8 a0[4], a1[4];
#pragma unroll
  for (int kk = 0; kk < 4; ++kk) { a0[kk] = *(LAS const bf16x8*)(fb + kk * 64); a1[kk] = *(LAS const bf16x8*)(fb1 + kk * 64); }
  CFENCE();
  const f32x2 w16 = cmul(w8, w8), w32 = cmul(w16, w16), w64 = cmul(w32, w32);
  f32x2 bb[8];
  bb[0] = cmul(Cc, dB); bb[1] = cmul(bb[0], w8); bb[2] = cmul(bb[0], w16); bb[3] = cmul(bb[1], w16);
#pragma unroll
  for (int i = 0; i < 4; ++i) bb[4 + i] = cmul(bb[i], w32);
#pragma unroll
  for (int ct = 0; ct < 2; ++ct) {
#pragma unroll
    for (int i = 0; i < 2; ++i) {
#pragma unroll
      for (int j = 0; j < 8; ++j) PUT(8 * i + j, bb[j]);
      if (ct == 0 || i == 0) {
#pragma unroll
        for (int j = 0; j < 8; ++j) bb[j] = cmul(bb[j], w64);
      }
    }
    CFENCE();
    bf16x8 bf[4];
#pragma unroll
    for (int kk = 0; kk < 4; ++kk) bf[kk] = *(LAS const bf16x8*)(fb + kk * 64);
    CFENCE();
    d[ct][0] = (f32x4){0.f, 0.f, 0.f, 0.f}; d[ct][1] = d[ct][0];
#pragma unroll
    for (int kk = 0; kk < 4; ++kk) { d[ct][0] = MFMA16(a0[kk], bf[kk], d[ct][0]); d[ct][1] = MFMA16(a1[kk], bf[kk], d[ct][1]); }
  }
}
DI void kgen_compact(const f32x4 (&d)[2][2], lptr tb, int lane) {
  const int l15 = lane & 15, g = lane >> 4;
  {
    const int e0 = g < 2 ? 8 * l15 + 4 * g : (g == 2 ? 256 + 4 * l15 : 384 + 2 * l15), es = g < 2 ? 128 : (g == 2 ? 64 : 32), e1 = g == 3 ? 64 : 2;
    const int f0 = g == 0 ? 512 + l15 : (lane == 16 ? 640 : 700);
#pragma unroll
    for (int ct = 0; ct < 2; ++ct) {
      const lptr p = tb + (e0 + ct * es) * 2;
      *(LAS unsigned*)p = pk2(d[ct][0][0], d[ct][0][1]); *(LAS unsigned*)(p + e1 * 2) = pk2(d[ct][0][2], d[ct][0][3]);
      LAS u16* q = (LAS u16*)tb + f0 + ct * 16;
      const unsigned x0 = pk2(d[ct][1][0], d[ct][1][1]), x1 = pk2(d[ct][1][2], d[ct][1][3]);
      q[0] = (u16)x0; q[32] = (u16)(x0 >> 16); q[64] = (u16)x1; q[96] = (u16)(x1 >> 16);
    }
  }
  CFENCE();
}
DI void kgen_expand(lptr tb, lptr img, int lane) {
  LAS const u16* C = (LAS const u16*)tb;
  const int rl = 63 - lane;
#pragma unroll
  for (int r = 0; r < 2; ++r) {
    const unsigned a = C[512 + 64 * (1 - r) + rl], bb = C[513 + 64 * (1 - r) + rl];
    const unsigned aa = a | (a << 16), b2x = bb | (bb << 16);
    *(LAS u32x4*)(img + r * 1024 + lane * 16) = (u32x4){b2x, (rl & 1) ? b2x : (bb | (a << 16)), aa, aa};
  }
  {
    const unsigned ab = *(LAS const unsigned*)(C + 384 + 2 * rl), c = C[386 + 2 * rl];
    *(LAS u32x4*)(img + 2048 + lane * 16) = (u32x4){c | (c << 16), __builtin_amdgcn_perm(ab, ab, 0x03020302u), __builtin_amdgcn_perm(ab, ab, 0x01000302u),
                                                __builtin_amdgcn_perm(ab, ab, 0x01000100u)};
  }
  {
    const bool hi = lane < 32;
    const int e = hi ? 128 + 4 * rl : 8 * rl;
    const u32x2 w01 = *(LAS const u32x2*)(C + e), w23 = *(LAS const u32x2*)(C + e + 4);
    const unsigned o0 = hi ? __builtin_amdgcn_perm(w01[1], w23[0], 0x07060100u) : __builtin_amdgcn_perm(w23[1], w23[1], 0x01000302u);
    const unsigned o1 = __builtin_amdgcn_perm(w23[0], w01[1], hi ? 0x01000100u : 0x05040706u);
    const unsigned o2 = __builtin_amdgcn_perm(w01[1], w01[0], hi ? 0x03020504u : 0x05040706u);
    const unsigned o3 = __builtin_amdgcn_perm(w01[0], w01[0], hi ? 0x01000100u : 0x01000302u);
    *(LAS u32x4*)(img + 3072 + lane * 16) = (u32x4){o0, o1, o2, o3};
  }
  if (lane < 4) *(LAS u32x4*)(img + 4096 + lane * 16) = (u32x4){0u, 0u, 0u, 0u};
}

template <int NT> DI void qgemm(lptr lds, LAS unsigned* ctrl, const bf16x8 (&xa)[18], int mt, int lane) {
  const int l15 = lane & 15, g = lane >> 4;
  constexpr int nt0 = NT == 2 ? 3 : 0;
  f32x4 acc[NT];
#pragma unroll
  for (int i = 0; i < NT; ++i) acc[i] = (f32x4){0.f, 0.f, 0.f, 0.f};
  const lptr wq = lds + A_W + (g * 8 + (l15 >> 2) * 2) * 144 + (l15 & 3) * 8 + nt0 * 32;
  constexpr int PD = 4;
  bf16x8 wf[PD + 1][NT];
  unsigned stage = __builtin_amdgcn_readfirstlane(__hip_atomic_load(&ctrl[4], __ATOMIC_RELAXED, __HIP_MEMORY_SCOPE_WORKGROUP));
#define WNEED(n) do { if (__builtin_expect(stage < (n), 0)) {                                                                             \
      do stage = __builtin_amdgcn_readfirstlane(__hip_atomic_load(&ctrl[4], __ATOMIC_RELAXED, __HIP_MEMORY_SCOPE_WORKGROUP)); while (stage < (n)); } \
    CFENCE(); } while (0)
#define QRD(kk) _Pragma("unroll") for (int i = 0; i < NT; ++i) wf[(kk) % (PD + 1)][i] = trd2(wq + (kk) * 4608 + i * 32, wq + (kk) * 4608 + 144 + i * 32)
#define QMM(kk) _Pragma("unroll") for (int i = 0; i < NT; ++i) acc[i] = MFMA16(wf[(kk) % (PD + 1)][i], xa[kk], acc[i])
#define SB() __builtin_amdgcn_sched_barrier(0)
  WNEED(3u);
#pragma unroll
  for (int kk = 0; kk < 6 + PD; ++kk) { if (kk >= PD) { QMM(kk - PD); } SB(); if (kk < 6) { QRD(kk); } SB(); }
  WNEED(5u); QRD(6); QRD(7); QRD(8); QRD(9); SB(); QMM(6); QMM(7); QMM(8); QMM(9); SB();
  WNEED(7u); QRD(10); QRD(11); QRD(12); QRD(13); SB(); QMM(10); QMM(11); QMM(12); QMM(13); SB();
  WNEED(8u); QRD(14); QRD(15); SB(); QMM(14); QMM(15); SB();
  WNEED(9u); QRD(16); QRD(17); SB(); QMM(16); QMM(17); SB();
  LAS unsigned* qw = (LAS unsigned*)(lds + A_Q + (mt * 16 + l15) * Q_ROW + nt0 * 32 + g * 8);
#pragma unroll
  for (int i = 0; i < NT; ++i)
    if (nt0 + i < 4 || g < 2) *(LAS u32x2*)(qw + 8 * i) = (u32x2){pk2(acc[i][0] * QSCALE, acc[i][1] * QSCALE), pk2(acc[i][2] * QSCALE, acc[i][3] * QSCALE)};
  CFENCE();
  if (lane == 0) __hip_atomic_fetch_add(&ctrl[5], 1u, __ATOMIC_RELAXED, __HIP_MEMORY_SCOPE_WORKGROUP);
}

DI f32x32 qk_tile(lptr kp, const bf16x8& q0, const bf16x8& q1, unsigned kt, u32x2 qa) {
  const bf16x8 k = *(LAS const bf16x8*)kp;
  f32x16 z;
#pragma unroll
  for (int j = 0; j < 16; ++j) z[j] = 0.f;
  const f32x16 a = MFMA32(k, q0, z), b = MFMA32(k, q1, z);
  f32x32 s = __builtin_shufflevector(a, b, 0, 1, 2, 3, 4, 5, 6, 7, 8, 9, 10, 11, 12, 13, 14, 15, 16, 17, 18, 19, 20, 21, 22, 23, 24, 25, 26, 27, 28, 29, 30, 31);
  return __builtin_amdgcn_mfma_f32_32x32x4bf16_1k(__builtin_bit_cast(s16x4, (u32x2){kt, 0u}), __builtin_bit_cast(s16x4, qa), s, 0, 0, 0);
}
DI bf16x16 sub8(const f32x32& s, int o) {
  f32x8 t = {s[o], s[o + 1], s[o + 2], s[o + 3], s[o + 4], s[o + 5], s[o + 6], s[o + 7]};
  return __builtin_bit_cast(bf16x16, t);
}

__global__ __launch_bounds__(768, 3) void fused_kernel(const u16* __restrict__ U, const u16* __restrict__ Are, const u16* __restrict__ Aim,
                                                       const u16* __restrict__ Bre, const u16* __restrict__ Bim, const u16* __restrict__ Cre,
                                                       const u16* __restrict__ Cim, const u16* __restrict__ logdt, const u16* __restrict__ Dskip,
                                                       const u16* __restrict__ X, const u16* __restrict__ Wq, const u16* __restrict__ Wk,
                                                       const u16* __restrict__ Wv, const u16* __restrict__ Wo, u16* __restrict__ out,
                                                       char* __restrict__ ws) {
  __shared__ __attribute__((aligned(16))) char lds_raw[LDS_BYTES];
  const lptr lds = (lptr)lds_raw;
  const unsigned lds0 = (unsigned)(uintptr_t)lds_raw;
  STAMP(0);
  const int tid = threadIdx.x, lane = tid & 63, wid = __builtin_amdgcn_readfirstlane(tid >> 6);
  const int l15 = lane & 15, g = lane >> 4, r32 = lane & 31, h = lane >> 5, q4 = l15 >> 2, p4 = l15 & 3;
  const int b = blockIdx.x, qb = (b & 7) * 4 + (b >> 6), hg = (b >> 3) & 7;
  const int ce = 512 + (b >> 2), cq = b & 3, mt = wid & 3;
  char* const myflag = ws + WS_F + (qb * 8 + hg) * 128;
  LAS unsigned* ctrl = (LAS unsigned*)(lds + A_CTRL);
  LAS unsigned* bctl = (LAS unsigned*)(lds + B_CTRL);
#define LADD(i) do { if (lane == 0) __hip_atomic_fetch_add(&ctrl[i], 1u, __ATOMIC_RELAXED, __HIP_MEMORY_SCOPE_WORKGROUP); } while (0)
#define LSET(i, v) do { if (lane == 0) __hip_atomic_store(&ctrl[i], (v), __ATOMIC_RELAXED, __HIP_MEMORY_SCOPE_WORKGROUP); } while (0)
  {
    int tw = wid + (ROT ? (b >> 3) % 12 : 0); tw = tw >= 12 ? tw - 12 : tw;
    const int tm = tw & 3;
    const int arg = tw >= 6 ? tw - 5 : (tm == 0 ? 9 : (tw < 4 ? 10 : 11 + (hg >> 2)));
    const char* base = ((const char* const __attribute__((address_space(4)))*)__builtin_amdgcn_kernarg_segment_ptr())[arg];
    const int o0 = tw >= 6 ? b * 512 : (tm == 0 ? qb * 73728 + hg * 9216 + tw * 1152 : (tw < 4 ? ((b >> 3) * 3 + tw - 1) * 6912 : (b >> 6) * 5184));
    const int st = tw >= 6 ? 16 : (tm == 0 ? 72 : (tw < 4 ? 108 : 80));
    unsigned vo = lane * st + o0;
    if (tw >= 6 && lane >= 32) vo = ce * 256 + r32 * 8;
    const char* base2 = ((const char* const __attribute__((address_space(4)))*)__builtin_amdgcn_kernarg_segment_ptr())[(0x087F >> (4 * tm)) & 15];
    const unsigned vo2 = tm == 3 ? qb * 73728 + (tw >> 2) * 36000 : 0;
    if (ROT == 2 && (b >> 6) == 3) { touchs(vo2, base2, lds0 + A_W - 256); touchs(vo, base, lds0 + A_W - 256); }
    else { touchs(vo, base, lds0 + A_W - 256); if (TOUCH2) touchs(vo2, base2, lds0 + A_W - 256); }
  }
  if (tid < 32) ctrl[tid] = 0u;
  BAR();
  asm volatile("" :: "s"(U), "s"(Are), "s"(Aim), "s"(Bre), "s"(Bim), "s"(Cre), "s"(Cim), "s"(logdt), "s"(Dskip), "s"(X), "s"(Wq), "s"(Wk), "s"(Wv), "s"(Wo),
               "s"(out), "s"(ws));
  const int xoff = mt == 0 ? A_X0 : A_W + 9216 + mt * 18432;
  const unsigned xv0 = (lane >> 3) * ROWB + (((lane & 7) ^ (lane >> 4)) * 16), xv1 = (8 + (lane >> 3)) * ROWB + (((lane & 7) ^ (4 + (lane >> 4))) * 16);
  const char* xs = (const char*)X + (size_t)(qb * 64 + mt * 16) * ROWB;
  const bool kgw = mt != (wid < 4 ? 0 : 3) && (wid < 4 || wid >= 8);
  const int kc = wid < 4 ? wid - 1 : wid - 8, kch = kc < 2 ? 2 * b + kc : ce;
  int msc = kgw ? A_SCR + (2 * kc + (wid < 4 ? 1 : 0)) * KG_TAB + 49 * 16 : A_MISC + (wid == 11 ? 80 : (wid == 0 ? 64 : mt * 16));
  asm volatile("" : "+v"(msc)); msc = __builtin_amdgcn_readfirstlane(msc);
  if (wid >= 4 && wid < 8) {
    __builtin_amdgcn_s_setprio(3);
#pragma unroll
    for (int kb = 0; kb < 2; ++kb) { gldss(xv0, xs + kb * 128, lds0 + xoff + kb * 2048); gldss(xv1, xs + kb * 128, lds0 + xoff + kb * 2048 + 1024); }
  } else if (wid == 0) {
    __builtin_amdgcn_s_setprio(3);
    const char* wx = (const char*)(hg < 4 ? Wk : Wv) + 8 * (hg & 3);
#pragma unroll
    for (int i = 0; i < 8; ++i) gldss(lane * 36, wx + i * 64 * 36, lds0 + A_WKV + i * 1024);
    if (lane < 63) gldss(lane * 36, wx + 512 * 36, lds0 + A_WKV + 8192);
    if (lane == 0) {
      unsigned sv_;
      asm volatile("s_mov_b32 %0, m0\n\ts_mov_b32 m0, %3\n\ts_nop 0\n\tglobal_load_lds_dwordx3 %1, %2\n\ts_mov_b32 m0, %0"
                   : "=&s"(sv_) : "v"(575 * 36), "s"(wx), "s"(lds0 + A_WKV + 575 * 16) : "memory");
    }
  }
  {
    const int a6 = lane >> 3;
    const u16* pa = a6 < 2 ? (a6 == 0 ? Are : Aim) : (a6 < 4 ? (a6 == 2 ? Bre : Bim) : (a6 == 4 ? Cre : Cim));
    pa += kch * NS + (wid < 4 ? 64 : 0) + (lane & 7) * 8;
    if (lane == 48) pa = logdt + (kch & ~7);
    if (lane == 49) pa = (const u16*)(ws + WS_E);
    if (lane >= 50) pa = Dskip + (kch & ~7);
    if (lane >= (kgw ? 0 : 49) && lane < (kgw ? 51 : 50)) glds16c(pa, lds0 + msc - 49 * 16);
  }
  STAMP(17);
#define TAG() (*(LAS const unsigned*)(lds + msc) * 2654435761u + 0x9e3779b9u)
  auto kg_read = [&](kg_in& in) {
    LAS const u16* p = (LAS const u16*)(lds + msc - 49 * 16) + lane;
    in.are = p[0]; in.aim = p[64]; in.bre = p[128]; in.bim = p[192]; in.cre = p[256]; in.cim = p[320];
    in.ldt = ((LAS const u16*)(lds + msc - 16))[kch & 7];
    CFENCE();
  };
#define FRESH_LANE() int tid_ = threadIdx.x; asm volatile("" : "+v"(tid_));                                                          \
  const int lane = tid_ & 63, l15 = lane & 15, g = lane >> 4, r32 = lane & 31, h = lane >> 5, q4 = l15 >> 2, p4 = l15 & 3;             \
  (void)r32; (void)h; (void)q4; (void)p4; (void)g
  auto any_bad = [&]() { return __builtin_amdgcn_readfirstlane((int)bctl[8]); };

  if (wid < 8) {
    {
      const lptr xb0 = lds + xoff + l15 * 128 + ((g ^ (l15 >> 1)) * 16), xb1 = lds + xoff + l15 * 128 + (((4 + g) ^ (l15 >> 1)) * 16);
      bf16x8 xa[18];
      if (wid >= 4) {
#pragma unroll
        for (int kb = 2; kb < 9; ++kb) {
          gldss(xv0, xs + kb * 128, lds0 + xoff + kb * 2048); gldss(xv1, xs + kb * 128, lds0 + xoff + kb * 2048 + 1024);
        }
        STAMP(1);
        char* cbp = ws + WS_KV + (qb >> 2) * CH_BYTES;
        const int key0 = (qb & 3) * 64 + mt * 16;
        char* sp;
        if (hg < 4) {
          const int key = key0 + l15;
          sp = g == 0 ? cbp + (key >> 5) * 1024 + (hg >> 1) * 512 + (key & 31) * 16 + (hg & 1) * 8 : cbp + CH_TAIL + (key & 63) * 16 + (key >> 6) * 4;
        } else {
          const int key = key0 + 4 * g, dim = 4 * (hg - 4) + l15;
          const int vrow = l15 < 4 ? ((dim & 3) | ((dim & 4) << 1) | ((dim & 8) >> 1)) : 12 + l15;
          sp = cbp + CH_V + (key >> 3) * V_OCT + (key & 4) * 2 + vrow * 16;
        }
        PIN(sp);
        lds_wait(&ctrl[0], 1u);
        const lptr wb = lds + A_WKV + (g * 8 + q4) * 16 + (l15 & 1) * 8;
        bf16x8 wk[18];
#pragma unroll
        for (int i = 0; i < 18; ++i) wk[i] = trd2(wb + i * 512, wb + i * 512 + 64);
        STAMP(2);
        f32x4 acc = {0.f, 0.f, 0.f, 0.f}, acc2 = acc;
#define XGRP(kb0, n, KTYPE)                                                                                                    \
        { VMWAIT(n);                                                                                                           \
          if ((n) == 0) LSET(8 + mt, 1u);                                                                                      \
          _Pragma("unroll") for (int i = 0; i < 6; ++i) xa[2 * (kb0) + i] = *(LAS const bf16x8*)(((i & 1) ? xb1 : xb0) + ((kb0) + (i >> 1)) * 2048); \
          CFENCE();                                                                                                            \
          if ((n) == 0) LADD(12 + mt);                                                                                         \
          _Pragma("unroll") for (int i = 2 * (kb0); i < 2 * (kb0) + 6; i += 2) {                                                \
            if (KTYPE) { acc = MFMA16(wk[i], xa[i], acc); acc2 = MFMA16(wk[i + 1], xa[i + 1], acc2); }                          \
            else { acc = MFMA16(xa[i], wk[i], acc); acc2 = MFMA16(xa[i + 1], wk[i + 1], acc2); } } }
        if (hg < 4) { XGRP(0, 12, 1) XGRP(3, 6, 1) XGRP(6, 0, 1) } else { XGRP(0, 12, 0) XGRP(3, 6, 0) XGRP(6, 0, 0) }
        STAMP(3);
        acc += acc2;
        u32x2 v = {pk2(acc[0], acc[1]), pk2(acc[2], acc[3])};
        unsigned mv;
        if (hg < 4) {
          if (g == 0) st8c(sp, v);
          if (g == 1 && hg == 3) st4c(sp, v[0]);
          float n2 = bflo(v[0]) * bflo(v[0]) + bfhi(v[0]) * bfhi(v[0]);
          if (g == 0) n2 += bflo(v[1]) * bflo(v[1]) + bfhi(v[1]) * bfhi(v[1]);
          const unsigned m16 = max16(__float_as_uint(n2) & 0x7fffffffu);
          const float mq = __uint_as_float(__builtin_amdgcn_readlane(m16, 0)), mtl = __uint_as_float(__builtin_amdgcn_readlane(m16, 16));
          mv = __float_as_uint(hg == 3 ? mq + mtl : mq) & 0x7fffffffu;
        } else {
          unsigned am = umax(umax(v[0] << 16 & 0x7fffffffu, v[0] & 0x7fff0000u), umax(v[1] << 16 & 0x7fffffffu, v[1] & 0x7fff0000u));
          if (l15 >= 6) am = 0u;
          if (l15 == 6) v = (u32x2){0x3f803f80u, 0x3f803f80u};
          if (l15 == 7) v = (u32x2){0u, 0u};
          if (l15 < 4 || (hg == 7 && l15 < 8)) st8c(sp, v);
          am = max16(am);
          mv = umax(umax(__builtin_amdgcn_readlane(am, 0), __builtin_amdgcn_readlane(am, 16)), umax(__builtin_amdgcn_readlane(am, 32), __builtin_amdgcn_readlane(am, 48)));
        }
        if (lane == 0) __hip_atomic_fetch_max(&ctrl[2], mv, __ATOMIC_RELAXED, __HIP_MEMORY_SCOPE_WORKGROUP);
        STAMP(4);
      } else {
        if (wid == 0) {
          __builtin_amdgcn_s_setprio(2);
          unsigned vo[9];
          slice_offsets(vo, lane);
          VMWAIT(1);
          LSET(0, 1u);
          STAMP(1);
          glds9(vo, (const char*)U + (size_t)qb * 64 * ROWB + hg * 144 - 4096, lds0 + A_UT);
          const lptr rp = lds + A_UT + (((4 * g + p4) & 7) * 8 + 2 * q4) * 144 + (g >> 1) * 8;
          char* up = ws + WS_UT + (size_t)(hg * 72 + (lane >> 5) * 4 + (lane & 3)) * UT_ROW + 128 + qb * 128 + ((lane >> 2) & 7) * 16;
          VMWAIT(0);
          bf16x8 ut[9];
#pragma unroll
          for (int i = 0; i < 9; ++i) ut[i] = trd2(rp + i * 16, rp + i * 16 + 144);
          CFENCE();
#pragma unroll
          for (int i = 0; i < 9; ++i) st16c(up + i * 8 * UT_ROW, __builtin_bit_cast(u32x4, ut[i]));
          if (__builtin_expect(qb == 0, 0)) {
            st16c(ws + WS_UT + (size_t)(hg * 72 + lane) * UT_ROW + 112, (u32x4){0u, 0u, 0u, 0u});
            st16c(ws + WS_UT + (size_t)(hg * 72 + 64 + (lane & 7)) * UT_ROW + 112, (u32x4){0u, 0u, 0u, 0u});
          }
          STAMP(2);
        } else {
          __builtin_amdgcn_s_setprio(2);
          VMWAIT(0);
          kg_in in; kg_read(in);
          STAMP(1);
          f32x4 d[2][2];
          const lptr tb = lds + A_SCR + (2 * wid - 1) * KG_TAB;
          kgen_half(in, tb, lane, d);
#pragma unroll
          for (int i = 0; i < 4; ++i) *(LAS f32x4*)(tb + i * 1024 + lane * 16) = d[i >> 1][i & 1];
          CFENCE();
          LSET(15 + wid, 1u);
          VMWAIT(0);
          STAMP(2);
        }
        lds_wait(&ctrl[8 + mt], 1u);
#pragma unroll
        for (int kk = 0; kk < 18; ++kk) xa[kk] = *(LAS const bf16x8*)(((kk & 1) ? xb1 : xb0) + (kk >> 1) * 2048);
        CFENCE();
        LADD(12 + mt);
        STAMP(3);
        STAMP(4);
      }
      if (wid >= 4) {
        VMWAIT(0);
        if (lane == 0 && __hip_atomic_fetch_add(&ctrl[1], 1u, __ATOMIC_RELAXED, __HIP_MEMORY_SCOPE_WORKGROUP) == 3u)
          st8c(myflag, (u32x2){TAG(), __hip_atomic_load(&ctrl[2], __ATOMIC_RELAXED, __HIP_MEMORY_SCOPE_WORKGROUP)});
        __builtin_amdgcn_s_setprio(2);
        STAMP(5);
        qgemm<2>(lds, ctrl, xa, mt, lane);
      } else {
        VMWAIT(0);
        if (wid == 0 && lane == 0) st4c(myflag + 8, TAG());
        STAMP(5);
        qgemm<3>(lds, ctrl, xa, mt, lane);
      }
    }
    STAMP(8);
    FRESH_LANE();
    const int kp = wid >> 2, head = wid & 3;
    f32x16 o0, o1;
#pragma unroll
    for (int j = 0; j < 16; ++j) { o0[j] = 0.f; o1[j] = 0.f; }
    const unsigned ka0 = lds0 + KV_LDS + kp * 1024 + lane * 16, ta0 = lds0 + KV_LDS + CH_TAIL + kp * 512 + r32 * 16,
                   va0 = lds0 + KV_LDS + CH_V + kp * 4 * V_OCT + h * V_OCT + (r32 < 19 ? r32 : 19) * 16;
    unsigned oo = (r32 >> 4) * 18432 + l15 * 16 + h * 256;
    PIN(oo); PIN(o0); PIN(o1);
    u32x4 q0, q1; unsigned qtl;
    lds_wait(&ctrl[5], 8u);
    {
      LAS const unsigned *qp0 = (LAS const unsigned*)(lds + A_Q + (kp * 32 + r32) * Q_ROW + head * 36 + h * 16), *qp1 = qp0 + (32 - 64 * kp) * (Q_ROW / 4);
#pragma unroll
      for (int j = 0; j < 4; ++j) { q0[j] = qp0[j]; q1[j] = qp1[j]; }
      qtl = *(LAS const unsigned*)(lds + A_Q + (lane ^ (kp * 32)) * Q_ROW + head * 36 + 32);
    }
    CFENCE();
    LADD(6);
    const u32x2 qa = {qtl, 0u}, qc = {0u, qtl};
    STAMP(9);
    lds_wait(&ctrl[7], 1u);
    STAMP(10);
    {
      int it = 8; unsigned ka = ka0, va = va0, ta = ta0;
      asm volatile(
      "ds_read_b128 v[128:131], %[ka]\n"
      "ds_read_b128 v[140:143], %[ta]\n"
      "v_mov_b32 v120, 0\n"
      "v_mov_b32 v121, 0\n"
      "v_mov_b32 v122, 0\n"
      "v_mov_b32 v123, 0\n"
      "v_mov_b32 v124, 0\n"
      "v_mov_b32 v125, 0\n"
      "v_mov_b32 v126, 0\n"
      "v_mov_b32 v127, 0\n"
      "v_mov_b32 v136, 0\n"
      "v_mov_b32 v137, 0\n"
      "v_mov_b32 v138, 0\n"
      "v_mov_b32 v139, 0\n"
      "s_waitcnt lgkmcnt(1)\n"
      "v_mfma_f32_32x32x16_bf16 v[64:79], v[128:131], %[q0], 0\n"
      "v_mfma_f32_32x32x16_bf16 v[80:95], v[128:131], %[q1], 0\n"
      "ds_read_b128 v[128:131], %[ka] offset:2048\n"
      "ds_read_b128 v[132:135], %[va]\n"
      "s_nop 7\n"
      "s_waitcnt lgkmcnt(2)\n"
      "v_mfma_f32_32x32x4_2b_bf16 v[64:95], v[140:141], %[qa], v[64:95]\n"
      "s_nop 15\n"
      "s_nop 1\n"
      "1:\n"
      "s_setprio 1\n"
      "s_waitcnt lgkmcnt(1)\n"
      "v_mfma_f32_32x32x16_bf16 v[96:111], v[128:131], %[q0], 0\n"
      "v_exp_f32 v64, v64\n"
      "v_exp_f32 v65, v65\n"
      "v_exp_f32 v66, v66\n"
      "v_smfmac_f32_32x32x32_bf16 %[o1], v[136:139], v[120:127], %[ix]\n"
      "ds_read_b128 v[136:139], %[va] offset:640\n"
      "v_exp_f32 v67, v67\n"
      "v_exp_f32 v68, v68\n"
      "v_exp_f32 v69, v69\n"
      "v_mfma_f32_32x32x16_bf16 v[112:127], v[128:131], %[q1], 0\n"
      "ds_read_b128 v[128:131], %[ka] offset:4096\n"
      "v_exp_f32 v70, v70\n"
      "v_exp_f32 v71, v71\n"
      "v_exp_f32 v72, v72\n"
      "v_exp_f32 v73, v73\n"
      "s_waitcnt lgkmcnt(2)\n"
      "v_smfmac_f32_32x32x32_bf16 %[o0], v[132:135], v[64:71], %[ix]\n"
      "v_exp_f32 v74, v74\n"
      "v_exp_f32 v75, v75\n"
      "v_exp_f32 v76, v76\n"
      "v_exp_f32 v77, v77\n"
      "v_exp_f32 v78, v78\n"
      "v_mfma_f32_32x32x4_2b_bf16 v[96:127], v[140:141], %[qc], v[96:127]\n"
      "v_exp_f32 v79, v79\n"
      "v_exp_f32 v80, v80\n"
      "v_exp_f32 v81, v81\n"
      "v_exp_f32 v82, v82\n"
      "v_exp_f32 v83, v83\n"
      "v_exp_f32 v84, v84\n"
      "v_exp_f32 v85, v85\n"
      "v_exp_f32 v86, v86\n"
      "s_waitcnt lgkmcnt(1)\n"
      "v_smfmac_f32_32x32x32_bf16 %[o0], v[136:139], v[72:79], %[ix]\n"
      "v_exp_f32 v87, v87\n"
      "v_exp_f32 v88, v88\n"
      "v_exp_f32 v89, v89\n"
      "v_exp_f32 v90, v90\n"
      "v_smfmac_f32_32x32x32_bf16 %[o1], v[132:135], v[80:87], %[ix]\n"
      "ds_read_b128 v[132:135], %[va] offset:2560\n"
      "v_exp_f32 v91, v91\n"
      "v_exp_f32 v92, v92\n"
      "v_exp_f32 v93, v93\n"
      "v_exp_f32 v94, v94\n"
      "v_exp_f32 v95, v95\n"
      "s_setprio 0\n"
      "s_waitcnt lgkmcnt(1)\n"
      "v_mfma_f32_32x32x16_bf16 v[64:79], v[128:131], %[q0], 0\n"
      "v_exp_f32 v96, v96\n"
      "v_exp_f32 v97, v97\n"
      "v_exp_f32 v98, v98\n"
      "v_smfmac_f32_32x32x32_bf16 %[o1], v[136:139], v[88:95], %[ix]\n"
      "ds_read_b128 v[136:139], %[va] offset:3200\n"
      "v_exp_f32 v99, v99\n"
      "v_exp_f32 v100, v100\n"
      "v_exp_f32 v101, v101\n"
      "v_mfma_f32_32x32x16_bf16 v[80:95], v[128:131], %[q1], 0\n"
      "ds_read_b128 v[128:131], %[ka] offset:6144\n"
      "v_exp_f32 v102, v102\n"
      "v_exp_f32 v103, v103\n"
      "v_exp_f32 v104, v104\n"
      "v_exp_f32 v105, v105\n"
      "s_waitcnt lgkmcnt(2)\n"
      "v_smfmac_f32_32x32x32_bf16 %[o0], v[132:135], v[96:103], %[ix]\n"
      "v_exp_f32 v106, v106\n"
      "v_exp_f32 v107, v107\n"
      "v_exp_f32 v108, v108\n"
      "v_exp_f32 v109, v109\n"
      "v_exp_f32 v110, v110\n"
      "v_mfma_f32_32x32x4_2b_bf16 v[64:95], v[142:143], %[qa], v[64:95]\n"
      "v_exp_f32 v111, v111\n"
      "v_exp_f32 v112, v112\n"
      "v_exp_f32 v113, v113\n"
      "v_exp_f32 v114, v114\n"
      "v_exp_f32 v115, v115\n"
      "v_exp_f32 v116, v116\n"
      "v_exp_f32 v117, v117\n"
      "v_exp_f32 v118, v118\n"
      "s_waitcnt lgkmcnt(1)\n"
      "v_smfmac_f32_32x32x32_bf16 %[o0], v[136:139], v[104:111], %[ix]\n"
      "v_exp_f32 v119, v119\n"
      "v_exp_f32 v120, v120\n"
      "v_exp_f32 v121, v121\n"
      "v_exp_f32 v122, v122\n"
      "v_smfmac_f32_32x32x32_bf16 %[o1], v[132:135], v[112:119], %[ix]\n"
      "ds_read_b128 v[132:135], %[va] offset:5120\n"
      "v_exp_f32 v123, v123\n"
      "v_exp_f32 v124, v124\n"
      "v_exp_f32 v125, v125\n"
      "v_exp_f32 v126, v126\n"
      "v_exp_f32 v127, v127\n"
      "s_setprio 3\n"
      "s_waitcnt lgkmcnt(1)\n"
      "v_mfma_f32_32x32x16_bf16 v[96:111], v[128:131], %[q0], 0\n"
      "v_exp_f32 v64, v64\n"
      "v_exp_f32 v65, v65\n"
      "v_exp_f32 v66, v66\n"
      "v_smfmac_f32_32x32x32_bf16 %[o1], v[136:139], v[120:127], %[ix]\n"
      "ds_read_b128 v[136:139], %[va] offset:5760\n"
      "v_exp_f32 v67, v67\n"
      "v_exp_f32 v68, v68\n"
      "v_exp_f32 v69, v69\n"
      "v_mfma_f32_32x32x16_bf16 v[112:127], v[128:131], %[q1], 0\n"
      "s_setprio 2\n"
      "s_waitcnt lgkmcnt(1)\n"
      "v_mfma_f32_32x32x16_bf16 v[64:79], v[128:131], %[q0], 0\n"
      "v_exp_f32 v96, v96\n"
      "v_exp_f32 v97, v97\n"
      "v_exp_f32 v98, v98\n"
      "v_smfmac_f32_32x32x32_bf16 %[o1], v[136:139], v[88:95], %[ix]\n"
      "ds_read_b128 v[136:139], %[va] offset:8320\n"
      "v_exp_f32 v99, v99\n"
      "v_exp_f32 v100, v100\n"
      "v_exp_f32 v101, v101\n"
      "v_mfma_f32_32x32x16_bf16 v[80:95], v[128:131], %[q1], 0\n"
      "ds_read_b128 v[128:131], %[ka] offset:21504\n"
      "v_exp_f32 v102, v102\n"
      "v_exp_f32 v103, v103\n"
      "v_exp_f32 v104, v104\n"
      "v_exp_f32 v105, v105\n"
      "s_waitcnt lgkmcnt(2)\n"
      "v_smfmac_f32_32x32x32_bf16 %[o0], v[132:135], v[96:103], %[ix]\n"
      "v_exp_f32 v106, v106\n"
      "v_exp_f32 v107, v107\n"
      "v_exp_f32 v108, v108\n"
      "v_exp_f32 v109, v109\n"
      "v_exp_f32 v110, v110\n"
      "v_mfma_f32_32x32x4_2b_bf16 v[64:95], v[140:141], %[qa], v[64:95]\n"
      "v_exp_f32 v111, v111\n"
      "v_exp_f32 v112, v112\n"
      "v_exp_f32 v113, v113\n"
      "v_exp_f32 v114, v114\n"
      "v_exp_f32 v115, v115\n"
      "v_exp_f32 v116, v116\n"
      "v_exp_f32 v117, v117\n"
      "v_exp_f32 v118, v118\n"
      "s_waitcnt lgkmcnt(1)\n"
      "v_smfmac_f32_32x32x32_bf16 %[o0], v[136:139], v[104:111], %[ix]\n"
      "v_exp_f32 v119, v119\n"
      "v_exp_f32 v120, v120\n"
      "v_exp_f32 v121, v121\n"
      "v_exp_f32 v122, v122\n"
      "v_smfmac_f32_32x32x32_bf16 %[o1], v[132:135], v[112:119], %[ix]\n"
      "ds_read_b128 v[132:135], %[va] offset:19456\n"
      "v_exp_f32 v123, v123\n"
      "v_exp_f32 v124, v124\n"
      "v_exp_f32 v125, v125\n"
      "v_exp_f32 v126, v126\n"
      "v_exp_f32 v127, v127\n"
      "v_add_u32 %[ka], 19456, %[ka]\n"
      "v_add_u32 %[va], 19456, %[va]\n"
      "v_add_u32 %[ta], 19456, %[ta]\n"
      "s_sub_u32 %[it], %[it], 1\n"
      "s_cmp_lg_u32 %[it], 0\n"
      "s_cbranch_scc1 1b\n"
      "v_smfmac_f32_32x32x32_bf16 %[o1], v[136:139], v[120:127], %[ix]\n"
      "s_setprio 0\n"
      "s_waitcnt lgkmcnt(0)\n"
      "s_nop 15\n"
          : [o0] "+v"(o0), [o1] "+v"(o1), [ka] "+v"(ka), [va] "+v"(va), [ta] "+v"(ta), [it] "+s"(it)
          : [q0] "v"(q0), [q1] "v"(q1), [qa] "v"(qa), [qc] "v"(qc), [ix] "v"(0xdddddddd)
          : "v64", "v65", "v66", "v67", "v68", "v69", "v70", "v71", "v72", "v73", "v74", "v75", "v76", "v77", "v78", "v79", "v80", "v81", "v82", "v83", "v84", "v85", "v86", "v87", "v88", "v89", "v90", "v91", "v92", "v93", "v94", "v95", "v96", "v97", "v98", "v99", "v100", "v101", "v102", "v103", "v104", "v105", "v106", "v107", "v108", "v109", "v110", "v111", "v112", "v113", "v114", "v115", "v116", "v117", "v118", "v119", "v120", "v121", "v122", "v123", "v124", "v125", "v126", "v127", "v128", "v129", "v130", "v131", "v132", "v133", "v134", "v135", "v136", "v137", "v138", "v139", "v140", "v141", "v142", "v143", "scc", "memory");
    }
    STAMP(13);
    const lptr xo = lds + C_OX + lane * 16;
    auto put_half = [&]() {
#pragma unroll
      for (int i = 0; i < 3; ++i) {
        *(LAS f32x4*)(xo + (wid * 3 + i) * 1024) = (f32x4){o1[4 * i], o1[4 * i + 1], o1[4 * i + 2], o1[4 * i + 3]};
      }
    };
    put_half();
    BAR();
    f32x4 pp[3];
#pragma unroll
    for (int i = 0; i < 3; ++i) pp[i] = *(LAS const f32x4*)(xo + ((wid ^ 4) * 3 + i) * 1024);
    if (__builtin_expect(any_bad(), 0)) {
      BAR(); BAR();
      const bf16x8 qf0 = __builtin_bit_cast(bf16x8, q0), qf1 = __builtin_bit_cast(bf16x8, q1);
      float m0 = -INFINITY, m1 = -INFINITY;
#pragma unroll 1
      for (int i = 0; i < 32; ++i) {
        const int off = (i >> 2) * CH_BYTES;
        const f32x32 s = qk_tile((lptr)(uintptr_t)(ka0 + off + (i & 3) * 2048), qf0, qf1, *(LAS const unsigned*)(uintptr_t)(ta0 + off + (i & 3) * 4), qa);
#pragma unroll
        for (int j = 0; j < 16; ++j) { m0 = fmaxf(m0, s[j]); m1 = fmaxf(m1, s[16 + j]); }
      }
      m0 = fmaxf(m0, __shfl_xor(m0, 32)); m1 = fmaxf(m1, __shfl_xor(m1, 32));
      *(LAS f32x2*)(lds + SPARE + tid * 8) = (f32x2){m0, m1};
      BAR();
      const f32x2 mo = *(LAS const f32x2*)(lds + SPARE + (tid ^ 256) * 8);
      m0 = fmaxf(m0, mo[1]); m1 = fmaxf(m1, mo[0]);
#pragma unroll
      for (int j = 0; j < 16; ++j) { o0[j] = 0.f; o1[j] = 0.f; }
#pragma unroll 1
      for (int i = 0; i < 32; ++i) {
        const int off = (i >> 2) * CH_BYTES;
        f32x32 s = qk_tile((lptr)(uintptr_t)(ka0 + off + (i & 3) * 2048), qf0, qf1, *(LAS const unsigned*)(uintptr_t)(ta0 + off + (i & 3) * 4), qa);
#pragma unroll
        for (int j = 0; j < 16; ++j) { s[j] = __builtin_amdgcn_exp2f(s[j] - m0); s[16 + j] = __builtin_amdgcn_exp2f(s[16 + j] - m1); }
        const bf16x8 v0 = *(LAS const bf16x8*)(uintptr_t)(va0 + off + (i & 3) * 8 * V_OCT), v1 = *(LAS const bf16x8*)(uintptr_t)(va0 + off + ((i & 3) * 8 + 2) * V_OCT);
        o0 = SMF(v0, sub8(s, 0), o0); o0 = SMF(v1, sub8(s, 8), o0); o1 = SMF(v0, sub8(s, 16), o1); o1 = SMF(v1, sub8(s, 24), o1);
      }
      BAR();
      put_half();
      BAR();
#pragma unroll
      for (int i = 0; i < 3; ++i) pp[i] = *(LAS const f32x4*)(xo + ((wid ^ 4) * 3 + i) * 1024);
    }
    float f[12];
#pragma unroll
    for (int i = 0; i < 12; ++i) f[i] = o0[i] + pp[i >> 2][i & 3];
    const auto sw = __builtin_amdgcn_permlane32_swap(__float_as_uint(f[10]), __float_as_uint(f[10]), false, false);
    const float inv = __builtin_amdgcn_rcpf(__uint_as_float(sw[0]));
    const char* ob = ws + WS_O + (qb * 4 + kp * 2) * 18432 + hg * 2304;
    const u32x4 ov = {nosent(pk2(f[0] * inv, f[1] * inv)), nosent(pk2(f[2] * inv, f[3] * inv)), nosent(pk2(f[4] * inv, f[5] * inv)), nosent(pk2(f[6] * inv, f[7] * inv))};
    asm volatile("global_store_dwordx4 %0, %1, %2 sc1\n\ts_nop 1" ::"v"(oo), "v"(ov), "s"(ob + head * 512) : "memory");
    const unsigned ot = nosent(pk2(f[8] * inv, f[9] * inv));
    if (h == 0) asm volatile("global_store_dword %0, %1, %2 offset:2048 sc1" ::"v"(oo), "v"(ot), "s"(ob + head * 4) : "memory");
    STAMP(11);
  } else {
    const int sv = wid - 8, chl = sv >> 1, half = sv & 1, rot = b & 7;
    const unsigned l16 = lane * 16;
    unsigned vo[9];
    const char* wsrc = (const char*)Wq + hg * 144 - 4096;
    if (sv == 3) {
      slice_offsets(vo, lane);
#pragma unroll
      for (int a = 0; a < 3; ++a) glds9(vo, wsrc + a * 64 * ROWB, lds0 + A_W + a * 9216);
      touchs(1024 + (lane & 3) * (2u << 20), ws, lds0 + A_W - 256);
    }
    const int rr = lane >> 3;
    unsigned rem0, rem1, rem2, wva, wvt, f2o, yrd, yre, oom, ooe;
    {
      auto remoff = [&](int a) { const int O = 8 * a + rr, gq = O / 9, o = O - gq * 9, e8 = lane & 7;
                                 return (unsigned)((gq * 72 + (o < 8 ? 18 * (o >> 1) + 8 * (o & 1) + e8 : 18 * (e8 >> 1) + 16 + (e8 & 1))) * ROWB); };
      rem0 = remoff(sv); rem1 = remoff(sv + 4); rem2 = remoff(8);
      const unsigned wsw = ((lane & 7) ^ (2 * ((rr >> 1) & 1))) * 16;
      wva = rr * ROWB + wsw; wvt = (18 * (rr >> 1) + 16 + (rr & 1)) * ROWB + wsw;
      const int pb0 = 32 * hg + r32;
      f2o = (h ? qb * 8 + (lane & 7) : ((pb0 & 7) * 4 + (pb0 >> 6)) * 8 + ((pb0 >> 3) & 7)) * 128;
      yrd = lds0 + C_YT + sv * 2304 + (((4 * g + p4) & 7) * 8 + q4) * 32 + (g >> 1) * 8; yre = lds0 + C_YT + sv * 2304 + 2048 + q4 * 32 + p4 * 8;
      oom = (((lane & 3) + h * 4) * OUTLD + ((lane >> 2) & 7) * 8) * 2; ooe = ((lane & 3) + q4 * 4) * OUTLD * 2;
      PIN(rem0); PIN(rem1); PIN(rem2); PIN(wva); PIN(wvt); PIN(f2o); PIN(yrd); PIN(yre); PIN(oom); PIN(ooe);
    }
    if (sv == 3) { VMWAIT(1); LSET(4, 3u); } else VMWAIT(0);
    const unsigned tag = __builtin_amdgcn_readfirstlane(TAG());
    const float dsk = bf2f(((LAS const u16*)(lds + msc + 16))[kch & 7]);
#define KVC(s0) do { kv_group(ws, lds0, (s0) + chl, rot, 2 * half, l16); kv_group(ws, lds0, (s0) + chl, rot, 2 * half + 1, l16);            \
                     if (half) kv_group(ws, lds0, (s0) + chl, rot, 4, l16); } while (0)
    const char* fp0 = ws + WS_F + (((rot + 2 * sv + 2 + h) & 7) * 32 + r32) * 128;
    u32x4 fl0;
    unsigned qn2 = 0;
    if (sv < 3) {
      __builtin_amdgcn_s_setprio(KPRIO);
      kg_in in; kg_read(in);
      STAMP(1);
      f32x4 d[2][2];
      const lptr tb = lds + A_SCR + 2 * sv * KG_TAB;
      kgen_half(in, tb, lane, d);
      VMWAIT(0);
      STAMP(2);
      lds_wait(&ctrl[16 + sv], 1u);
#pragma unroll
      for (int i = 0; i < 4; ++i) d[i >> 1][i & 1] += *(LAS const f32x4*)(tb + KG_TAB + i * 1024 + lane * 16);
      if (lane == 0) d[0][0][0] += dsk;
      kgen_compact(d, tb, lane);
      __builtin_amdgcn_s_setprio(GPRIO);
      lds_wait(&ctrl[FGATE], FGATEV);
      {
        const char* fup = ws + WS_F + (r32 * 8 + (sv == 2 ? 7 : b / 36)) * 128;
        u32x4 fu;
        asm volatile("global_load_dwordx4 %0, %2, off sc1\n\tglobal_load_dwordx4 %1, %3, off sc1\n\ts_waitcnt vmcnt(0)" : "=&v"(fu), "=&v"(fl0) : "v"(fup), "v"(fp0) : "memory");
        flag_wait<2>(fup, tag, fu);
        lds_wait(&ctrl[5], 8u);
        const char* ur = ws + WS_UT + (size_t)kch * UT_ROW + 128;
        const int upad = sv == 2 ? UE_PADB : U_PADB, u1 = sv == 2 ? CV_UE1 : CV_U1;
        glds4c(l16, ur - 2, lds0 + CVB + CV_U + sv * CV_UCH + upad); glds4c(l16, ur, lds0 + CVB + CV_U + sv * CV_UCH + u1 + upad);
      }
      kgen_expand(tb, lds + CVB + sv * R_BYTES, lane);
      flag_wait<0>(fp0, tag, fl0);
      if (sv < 2) { if (lane < 56) *(LAS u32x4*)(lds + CVB + CV_U + sv * CV_UCH + (lane < 28 ? lane * 16 : CV_U1 + (lane - 28) * 16)) = (u32x4){0u, 0u, 0u, 0u}; }
      else if (lane < 62) {
#pragma unroll
        for (int i = 0; i < 4; ++i) *(LAS u32x4*)(lds + CVB + CV_UE + (i >> 1) * CV_UE1 + (i & 1) * 992 + l16) = (u32x4){0u, 0u, 0u, 0u};
      }
      STAMP(4);
      __builtin_amdgcn_s_setprio(3);
    } else {
      STAMP(1);
      __builtin_amdgcn_s_setprio(3);
#pragma unroll
      for (int a = 3; a < 9; ++a) {
        if (a & 1) lds_wait(&ctrl[12 + (a >> 1)], 2u);
        glds9(vo, wsrc + a * 64 * ROWB, lds0 + A_W + a * 9216);
      }
      STAMP(3);
      VMWAIT(36); LSET(4, 5u);
      VMWAIT(18); LSET(4, 7u);
      VMWAIT(9); LSET(4, 8u);
      VMWAIT(0); LSET(4, 9u);
      STAMP(4);
      const unsigned tg = tag;
      asm volatile("global_load_dwordx4 %0, %1, off sc1\n\ts_waitcnt vmcnt(0)" : "=&v"(fl0) : "v"(fp0) : "memory");
#pragma unroll 1
      for (int spin = 0; spin < SPIN_MAX; ++spin) {
        if (__all(h == 1 || fl0[0] == tg)) break;
        __builtin_amdgcn_s_sleep(POLL0);
        if (h == 0 && fl0[0] != tg) asm volatile("global_load_dwordx4 %0, %1, off sc1\n\ts_waitcnt vmcnt(0)" : "+v"(fl0) : "v"(fp0) : "memory");
      }
      STAMP(5);
      lds_wait(&ctrl[12], 2u);
#pragma unroll
      for (int i = 0; i < 5; ++i) kv_group(ws, lds0, 0, rot, i, l16);
      VMWAIT(0);
      LSET(7, 1u);
      STAMP(7);
      flag_wait<0>(fp0, tg, fl0);
      STAMP(9);
#pragma unroll
      for (int i = 0; i < 4; ++i) kv_group(ws, lds0, 1, rot, i, l16);
      lds_wait(&ctrl[5], 8u);
      {
#define DOT2(x, c) __builtin_amdgcn_fdot2_f32_bf16(__builtin_bit_cast(bf16x2, x), __builtin_bit_cast(bf16x2, x), (c), false)
        u32x4 qr[9];
#pragma unroll
        for (int i = 0; i < 9; ++i) qr[i] = *(LAS const u32x4*)(lds + A_Q + lane * Q_ROW + i * 16);
        CFENCE();
        lds_wait(&ctrl[6], 8u);
        kv_group(ws, lds0, 1, rot, 4, l16);
        float n[4] = {0.f, 0.f, 0.f, 0.f};
#pragma unroll
        for (int i = 0; i < 36; ++i) n[i / 9] = DOT2(qr[i >> 2][i & 3], n[i / 9]);
        qn2 = umax(umax(__float_as_uint(n[0]) & 0x7fffffffu, __float_as_uint(n[1]) & 0x7fffffffu), umax(__float_as_uint(n[2]) & 0x7fffffffu, __float_as_uint(n[3]) & 0x7fffffffu));
        PIN(qn2);
      }
      STAMP(10);
    }
    {
      unsigned m = fl0[1];
      m = umax(m, dppu<0x128>(m));
      { const auto t = __builtin_amdgcn_permlane16_swap(m, m, false, false); m = umax(t[0], t[1]); }
      { const auto t = __builtin_amdgcn_permlane32_swap(m, m, false, false); m = umax(t[0], t[1]); }
      if (lane < 8) bctl[-32 + sv * 8 + lane] = m;
    }
    const char* wo = (const char*)Wo + hg * 128;
    auto wo_group = [&](int gq) {
      const unsigned va = wva ^ ((gq & 1) * 64), vb = (va ^ 64) + 8 * ROWB - 1024, vt = wvt ^ ((gq & 1) * 64);
      const char* sb = wo + gq * 72 * ROWB;
      unsigned sv_;
      asm volatile("s_mov_b32 %0, m0\n\ts_add_u32 m0, %8, 0x2000\n\ts_nop 0\n\tglobal_load_lds_dwordx4 %3, %4\n\t"
                   "s_mov_b32 m0, %8\n\ts_nop 0\n\tglobal_load_lds_dwordx4 %1, %4\n\tglobal_load_lds_dwordx4 %2, %4 offset:1024\n\t"
                   "s_add_u32 m0, m0, 0x800\n\ts_nop 0\n\tglobal_load_lds_dwordx4 %1, %5\n\tglobal_load_lds_dwordx4 %2, %5 offset:1024\n\t"
                   "s_add_u32 m0, m0, 0x800\n\ts_nop 0\n\tglobal_load_lds_dwordx4 %1, %6\n\tglobal_load_lds_dwordx4 %2, %6 offset:1024\n\t"
                   "s_add_u32 m0, m0, 0x800\n\ts_nop 0\n\tglobal_load_lds_dwordx4 %1, %7\n\tglobal_load_lds_dwordx4 %2, %7 offset:1024\n\ts_mov_b32 m0, %0"
                   : "=&s"(sv_) : "v"(va), "v"(vb), "v"(vt), "s"(sb), "s"(sb + 18 * ROWB), "s"(sb + 36 * ROWB), "s"(sb + 54 * ROWB), "s"(lds0 + gq * 9216)
                   : "memory", "scc");
    };
    auto wo_rem = [&]() {
      const char* wr = (const char*)Wo + 1024 + hg * 16;
      gldss(rem0, wr, lds0 + C_REM + sv * 1024); gldss(rem1, wr, lds0 + C_REM + 4096 + sv * 1024);
      if (sv == 0) gldss(rem2, wr, lds0 + C_REM + 8192);
    };
    __builtin_amdgcn_s_setprio(SPRIO);
    const int rofs = CVB + (1023 - l15 + 4 * g) * 4, uofs = CVB + CV_U + (l15 & 1) * CV_U1 + U_PADB + (32 * (l15 >> 1) + 8 * g) * 2;
    f32x4 acc[8], ea = {0.f, 0.f, 0.f, 0.f}, eb = ea;
#pragma unroll
    for (int c = 0; c < 8; ++c) acc[c] = ea;
    {
      const lptr rb = lds + chl * R_BYTES + rofs - 3584 - 192 - half * 256, ub = lds + chl * CV_UCH + uofs - 192 - half * 256;
      const lptr rbe = lds + 2 * R_BYTES + rofs - 3584 - 64 - sv * 128;
      const lptr ube = lds + CVB + CV_UE + (l15 & 1) * CV_UE1 + UE_PADB + (32 * (l15 >> 1) + 8 * g) * 2 - 64 - sv * 128 - cq * 512, uae = ube + (2 * cq - 3) * 512;
      bf16x8 uf[8], kf[8];
#define LDK(base, z, d) do { LAS const unsigned* rp = (LAS const unsigned*)((base) + 512 * (7 - (z)) + 64 * (d)); kf[z] = __builtin_bit_cast(bf16x8, (u32x4){rp[0], rp[1], rp[2], rp[3]}); } while (0)
#define LDU(base, z, d) uf[z] = *(LAS const bf16x8*)((base) + (z) * 512 + 64 * (d))
#pragma unroll
      for (int iv = 0; iv < 4; ++iv) {
        STAMP(16);
        if (iv == 0) { BARVM(0); } else asm volatile("s_waitcnt vmcnt(0)\n\ts_barrier" ::: "memory");
        STAMP(24);
        if (iv == 0) {
#pragma unroll
          for (int z = 0; z < 8; ++z) { LDK(rb, z, 3); LDU(ub, 7 - z, 3); }
        }
        if (iv < 2) { KVC(2 + 2 * iv); }
        if (iv == 0) touchs(lane * 80, (const char*)Wo + ((b >> 3) * 4 + sv) * 5184, lds0 + B_CTRL - 448);
        if (iv == 1 && sv == 3) {
          const int l7 = lane & 7;
          const unsigned m = umax(umax(bctl[-32 + l7], bctl[-24 + l7]), umax(bctl[-16 + l7], bctl[-8 + l7]));
          const float k2 = __uint_as_float(__builtin_amdgcn_readlane(m, 0)) + __uint_as_float(__builtin_amdgcn_readlane(m, 1)) +
                           __uint_as_float(__builtin_amdgcn_readlane(m, 2)) + __uint_as_float(__builtin_amdgcn_readlane(m, 3));
          const float vf = __uint_as_float(umax(umax(__builtin_amdgcn_readlane(m, 4), __builtin_amdgcn_readlane(m, 5)), umax(__builtin_amdgcn_readlane(m, 6), __builtin_amdgcn_readlane(m, 7))));
          const bool bad = __any(!(__uint_as_float(qn2) * k2 <= 8000.f)) || !(vf <= 1048576.f && vf >= 9.5367431640625e-7f);
          if (lane < 16) bctl[lane] = (lane == 8 && bad) ? 1u : 0u;
        }
        if (iv == 3 && sv < 3) wo_group(3 + sv);
        if (iv == 2) {
          const char* p = ws + WS_O + (qb * 4 + sv) * 18432 + 9 * hg * 256;
          const u32x4 sv4 = {SENT, SENT, SENT, SENT};
          asm volatile("global_store_dwordx4 %0, %1, %2 sc1\n\tglobal_store_dwordx4 %0, %1, %2 offset:1024 sc1\n\t"
                       "global_store_dwordx4 %0, %1, %2 offset:1280 sc1\n\ts_nop 1" ::"v"(l16), "v"(sv4), "s"(p) : "memory");
        }
        const bool ex = iv & 1;
#pragma unroll
        for (int m1 = 0; m1 < 8; ++m1) {
          __builtin_amdgcn_sched_barrier(0);
#pragma unroll
          for (int c = 7; c >= m1; --c) acc[c] = MFMA16(uf[c - m1], kf[m1], acc[c]);
          __builtin_amdgcn_sched_barrier(0);
          if (ex) { LDK(rbe, m1, 1 - (iv >> 1)); LDU(ube, 7 - m1, 1 - (iv >> 1)); }
          else { LDK(rb, m1, 2 - iv); LDU(ub, 7 - m1, 2 - iv); }
        }
        if (ex) {
          bf16x8 ua[4];
#pragma unroll
          for (int i = 0; i < 4; ++i) ua[i] = *(LAS const bf16x8*)(uae + (3 - i) * 512 + 64 * (1 - (iv >> 1)));
#pragma unroll
          for (int m1 = 0; m1 < 8; ++m1) {
            __builtin_amdgcn_sched_barrier(0);
            eb = MFMA16(uf[7 - m1], kf[m1], eb);
            if (m1 >= 4) ea = MFMA16(ua[m1 - 4], kf[m1 - 4], ea);
            __builtin_amdgcn_sched_barrier(0);
            if (iv == 1) { LDU(ub, 7 - m1, 1); if (m1 >= 4) { LDK(rb, m1 - 4, 1); LDK(rb, m1, 1); } }
          }
        }
      }
    }
    STAMP(6);
    const int ec = chl ? 7 - cq : cq;
    const lptr sc = lds + CV_SCR + l16;
    if (half) {
#pragma unroll
      for (int i = 0; i < 4; ++i) { *(LAS f32x4*)(sc + (sv * 4 + i) * 1024) = acc[i]; acc[i] = acc[4 + i]; }
    } else {
#pragma unroll
      for (int i = 0; i < 4; ++i) *(LAS f32x4*)(sc + (sv * 4 + i) * 1024) = acc[4 + i];
    }
    if (sv != 0) *(LAS f32x4*)(sc + 16384 + (sv - 1) * 1024) = ea;
    if (sv != 2) *(LAS f32x4*)(sc + 19456 + (sv > 2 ? 2 : sv) * 1024) = eb;
    STAMP(20);
    BAR();
    STAMP(27);
    KVC(6);
    if (half) wo_group(6 + chl);
    {
      const unsigned yo = g * 128 + l15 * 4;
      const char* yb = ws + WS_YT + (2 * b + chl) * 4096 + half * 2048;
#define YST(base, off, v) asm volatile("global_store_dword %0, %1, %2 offset:" #off " sc1" ::"v"(yo), "v"(v), "s"(base) : "memory")
#define YTILE(i, o0, o1) do { const f32x4 f = acc[i] + *(LAS const f32x4*)(sc + ((sv ^ 1) * 4 + (i)) * 1024); YST(yb, o0, pk2(f[0], f[1])); YST(yb, o1, pk2(f[2], f[3])); } while (0)
      YTILE(0, 0, 64); YTILE(1, 512, 576); YTILE(2, 1024, 1088); YTILE(3, 1536, 1600);
      if (half == 0) {
        f32x4 e = chl ? eb : ea;
#pragma unroll
        for (int s = 0; s < 3; ++s) e += *(LAS const f32x4*)(sc + 16384 + chl * 3072 + s * 1024);
        const char* ye = ws + WS_YT + ce * 4096 + ec * 512;
        YST(ye, 0, pk2(e[0], e[1])); YST(ye, 64, pk2(e[2], e[3]));
      }
    }
    STAMP(21);
    BARVM(0);
    STAMP(28);
    if (wid == 8 && lane == 0) st4c(myflag + 12, tag);
    if (sv) wo_group(sv - 1);
    wo_rem();
    const char* f2a = ws + WS_F + f2o;
    const char* f2b = ws + WS_F + (sv * 64 + lane) * 128;
    STAMP(22);
    BAR();
    STAMP(29);
    u32x4 fa, fb = {tag, tag, tag, tag};
    asm volatile("global_load_dwordx4 %0, %1, off sc1\n\ts_waitcnt vmcnt(0)" : "=&v"(fa) : "v"(f2a) : "memory");
    if (__builtin_expect(b == 0, 0)) asm volatile("global_load_dwordx4 %0, %1, off sc1\n\ts_waitcnt vmcnt(0)" : "+v"(fb) : "v"(f2b) : "memory");
    flag_wait<3>(f2a, tag, fa); flag_wait<3>(f2b, tag, fb);
    {
      const char* yp = ws + WS_YT + hg * 64 * 4096 + qb * 128 + sv * 32;
      const unsigned yv = (lane >> 1) * 4096 + (lane & 1) * 16;
      gldssc(yv, yp, lds0 + C_YT + sv * 2304); gldssc(yv, yp + 32 * 4096, lds0 + C_YT + sv * 2304 + 1024);
      if (lane < 16) gldssc(yv, yp + (512 - hg * 56) * 4096, lds0 + C_YT + sv * 2304 + 2048);
    }
    STAMP(23);
    BAR();
    STAMP(12);
    VMWAIT(0);
    {
      const lptr rp = (lptr)(uintptr_t)yrd, re = (lptr)(uintptr_t)yre;
      const char* ob = (const char*)(out + (size_t)(qb * 64 + sv * 16) * OUTLD);
      const u32x4 v0 = __builtin_bit_cast(u32x4, trd2(rp, rp + 128)), v1 = __builtin_bit_cast(u32x4, trd2(rp + 16, rp + 144));
      const u32x4 ve = __builtin_bit_cast(u32x4, trd2(re, re + 128));
#define OST16S(vo, v, base) asm volatile("global_store_dwordx4 %0, %1, %2\n\ts_nop 1" ::"v"(vo), "v"(v), "s"(base) : "memory")
      OST16S(oom, v0, ob + hg * 128); OST16S(oom, v1, ob + hg * 128 + 8 * OUTLD * 2);
      if (lane < 16) OST16S(ooe, ve, ob + 1024 + hg * 16);
    }
    if (b == 0 && wid == 8 && lane == 0) st4c(ws + WS_E, tag);
    __builtin_amdgcn_s_setprio(0);
    STAMP(13);
    BAR();
    if (__builtin_expect(any_bad(), 0)) { BAR(); _Pragma("unroll 1") for (int i = 0; i < 8; i += 2) KVC(i); BARVM(0); BAR(); BAR(); wo_group(sv); wo_group(sv + 4); wo_rem(); BARVM(0); }
  }
  {
    FRESH_LANE();
    const int kt = wid >> 2;
    bf16x8 wf[6][4];
    {
      const int sw = 2 * (((q4 >> 1) & 1) | ((g & 1) << 1));
      const lptr wb = lds + kt * 24576 + (g * 8 + q4) * 128 + (p4 & 1) * 8;
#pragma unroll
      for (int nt = 0; nt < 4; ++nt) {
        const lptr p = wb + (((2 * nt + (p4 >> 1)) ^ sw) * 16);
#pragma unroll
        for (int kk = 0; kk < 6; ++kk) wf[kk][nt] = trd2(p + kk * 4096, p + kk * 4096 + 512);
      }
    }
    bf16x8 wfr[6];
    {
      const lptr wr = lds + C_REM + kt * 3072 + (g * 8 + q4) * 16 + (p4 & 1) * 8;
#pragma unroll
      for (int kk = 0; kk < 6; ++kk) wfr[kk] = trd2(wr + kk * 512, wr + kk * 512 + 64);
    }
    const lptr rd = lds + C_RED2 + mt * 5120 + lane * 16, rd2 = lds + C_RED + mt * 5120 + lane * 16;
    u16* cp = out + (size_t)(qb * 64 + mt * 16 + l15) * OUTLD + DM + g * 4 + (kt < 2 ? hg * 64 + kt * 32 : 512 + hg * 8);
    const char* op = ws + WS_O + (size_t)((qb * 4 + mt) * 18 + kt * 6) * 1024 + lane * 16;
    const char* op2 = op + 3072;
    PIN(cp); PIN(op); PIN(op2);
    VMWAIT(0);
    if (__builtin_expect(wid >= 8, 0)) __builtin_amdgcn_s_sleep(OSLEEP);
    if (CSLEEP) __builtin_amdgcn_s_sleep(CSLEEP);
    STAMP(14);
    u32x4 of0, of1, of2, of3, of4, of5;
    asm volatile("global_load_dwordx4 %0, %6, off sc1\n\tglobal_load_dwordx4 %1, %6, off offset:1024 sc1\n\t"
                 "global_load_dwordx4 %2, %6, off offset:2048 sc1\n\tglobal_load_dwordx4 %3, %7, off sc1\n\t"
                 "global_load_dwordx4 %4, %7, off offset:1024 sc1\n\tglobal_load_dwordx4 %5, %7, off offset:2048 sc1\n\ts_waitcnt vmcnt(0)"
                 : "=&v"(of0), "=&v"(of1), "=&v"(of2), "=&v"(of3), "=&v"(of4), "=&v"(of5) : "v"(op), "v"(op2) : "memory");
#define MX4(x) umax(umax(x[0], x[1]), umax(x[2], x[3]))
    if (__builtin_expect(__any(umax(umax(umax(MX4(of0), MX4(of1)), umax(MX4(of2), MX4(of3))), umax(MX4(of4), MX4(of5))) == SENT), 0))
#pragma unroll 1
    for (int spin = 0; spin < SPIN_MAX; ++spin) {
      const bool m0 = umax(umax(MX4(of0), MX4(of1)), MX4(of2)) == SENT, m1 = umax(umax(MX4(of3), MX4(of4)), MX4(of5)) == SENT;
      if (!__any(m0 || m1)) break;
      __builtin_amdgcn_s_sleep(ORETRY);
      if (m0) asm volatile("global_load_dwordx4 %0, %3, off sc1\n\tglobal_load_dwordx4 %1, %3, off offset:1024 sc1\n\t"
                           "global_load_dwordx4 %2, %3, off offset:2048 sc1\n\ts_waitcnt vmcnt(0)" : "+v"(of0), "+v"(of1), "+v"(of2) : "v"(op) : "memory");
      if (m1) asm volatile("global_load_dwordx4 %0, %3, off sc1\n\tglobal_load_dwordx4 %1, %3, off offset:1024 sc1\n\t"
                           "global_load_dwordx4 %2, %3, off offset:2048 sc1\n\ts_waitcnt vmcnt(0)" : "+v"(of3), "+v"(of4), "+v"(of5) : "v"(op2) : "memory");
    }
    const u32x4 of[6] = {of0, of1, of2, of3, of4, of5};
    STAMP(15);
    f32x4 acc[5];
#pragma unroll
    for (int nt = 0; nt < 5; ++nt) acc[nt] = (f32x4){0.f, 0.f, 0.f, 0.f};
#pragma unroll
    for (int kk = 0; kk < 6; ++kk) {
#pragma unroll
      for (int nt = 0; nt < 4; ++nt) acc[nt] = MFMA16(wf[kk][nt], __builtin_bit_cast(bf16x8, of[kk]), acc[nt]);
      acc[4] = MFMA16(wfr[kk], __builtin_bit_cast(bf16x8, of[kk]), acc[4]);
    }
#define LW(p, v) *(LAS f32x4*)(p) = (v)
#define LR(p) *(LAS const f32x4*)(p)
    if (kt == 0) { LW(rd + 2048, acc[2]); LW(rd + 3072, acc[3]); LW(rd2 + 4096, acc[4]); }
    else if (kt == 1) { LW(rd2, acc[0]); LW(rd2 + 1024, acc[1]); LW(rd + 4096, acc[4]); acc[0] = acc[2]; acc[1] = acc[3]; }
    else { LW(rd, acc[0]); LW(rd + 1024, acc[1]); LW(rd2 + 2048, acc[2]); LW(rd2 + 3072, acc[3]); acc[0] = acc[4]; }
    BAR();
    const f32x4 v = acc[0] + LR(rd + kt * 2048) + LR(rd2 + kt * 2048), w = acc[1] + LR(rd + kt * 2048 + 1024) + LR(rd2 + kt * 2048 + 1024);
    if (kt < 2 || g < 2) ost8(cp, (u32x2){pk2(v[0], v[1]), pk2(v[2], v[3])});
    if (kt < 2) ost8(cp + 16, (u32x2){pk2(w[0], w[1]), pk2(w[2], w[3])});
  }
}

extern "C" void kernel_launch(void* const* d_in, const int* in_sizes, int n_in, void* d_out, int out_size, void* d_ws, size_t ws_size,
                              hipStream_t stream) {
  fused_kernel<<<256, 768, 0, stream>>>((const u16*)d_in[0], (const u16*)d_in[1], (const u16*)d_in[2], (const u16*)d_in[3], (const u16*)d_in[4],
                                        (const u16*)d_in[5], (const u16*)d_in[6], (const u16*)d_in[7], (const u16*)d_in[8], (const u16*)d_in[9],
                                        (const u16*)d_in[10], (const u16*)d_in[11], (const u16*)d_in[12], (const u16*)d_in[13], (u16*)d_out,
                                        (char*)d_ws);
}
```

```cpp
#include <hip/hip_runtime.h>
#include <hip/hip_bf16.h>
#include <stdint.h>

typedef __bf16 bf16x8 __attribute__((ext_vector_type(8)));
typedef __bf16 bf16x16 __attribute__((ext_vector_type(16)));
typedef __bf16 bf16x2 __attribute__((ext_vector_type(2)));
typedef short s16x4 __attribute__((ext_vector_type(4)));
typedef float f32x2 __attribute__((ext_vector_type(2)));
typedef float f32x4 __attribute__((ext_vector_type(4)));
typedef float f32x8 __attribute__((ext_vector_type(8)));
typedef float f32x16 __attribute__((ext_vector_type(16)));
typedef float f32x32 __attribute__((ext_vector_type(32)));
typedef unsigned u32x2 __attribute__((ext_vector_type(2)));
typedef unsigned u32x4 __attribute__((ext_vector_type(4)));
typedef unsigned short u16;
#define LAS __attribute__((address_space(3)))
#define DI __device__ __forceinline__
typedef LAS char* lptr;

constexpr int SEQ = 2048, DM = 576, NS = 128, OUTLD = 2 * DM, ROWB = DM * 2;
constexpr int CH_BYTES = 19456, KV_BYTES = 8 * CH_BYTES, CH_TAIL = 8192, CH_V = 9216, V_OCT = 320;
constexpr int R_BYTES = 4160, CV_U = 3 * 4160, CV_UE = CV_U + 2 * (4640 + 4544), CV_UE1 = 4640 + 1536, UE_PADB = 448 + 1536, CV_UCH = 4640 + 4544, CV_U1 = 4640, U_PADB = 448, UT_ROW = 4224;
constexpr size_t WS_E = 0, WS_F = 4096, WS_KV = WS_F + 256 * 128;
constexpr size_t WS_UT = WS_KV + KV_BYTES;
constexpr size_t WS_YT = WS_UT + (size_t)DM * UT_ROW;
constexpr size_t WS_O = WS_YT + (size_t)DM * SEQ * 2;
constexpr int LDS_BYTES = 163840, KV_LDS = 0, SPARE = KV_BYTES, CVB = 6 * CH_BYTES;
constexpr int A_UT = 0, A_WKV = 9216, A_X0 = 18432, A_Q = 36864, Q_ROW = 144, A_CTRL = A_Q + 64 * Q_ROW, A_MISC = A_CTRL + 128, A_SCR = A_MISC + 144, A_W = 80896, B_CTRL = LDS_BYTES - 64;
constexpr int CV_SCR = 0, C_REM = 73728, C_YT = 82944, C_RED = C_YT + 9216, C_OX = C_RED + 20480, C_RED2 = C_OX + 24576;
static_assert(A_SCR + 6 * 5712 <= A_W - 256 && A_SCR % 16 == 0 && A_W + 82944 == LDS_BYTES && CVB + CV_UE + CV_UE1 + UE_PADB + 4096 <= B_CTRL - 448 && C_OX + 24576 <= 7 * CH_BYTES + 1024 &&
              C_RED2 + 20480 <= B_CTRL - 448, "LDS map");
constexpr float QSCALE = 0.23570226039551584f * 1.4426950408889634f;
constexpr unsigned SENT = 0xffffffffu;
constexpr int SPIN_MAX = 1 << 16;
constexpr int POLL0 = 8, POLL1 = 12, CSLEEP = 8, OSLEEP = 8;

DI float bf2f(u16 x) { return __uint_as_float((unsigned)x << 16); }
DI float bflo(unsigned x) { return __uint_as_float(x << 16); }
DI float bfhi(unsigned x) { return __uint_as_float(x & 0xffff0000u); }
DI unsigned pk2(float lo, float hi) { f32x2 v = {lo, hi}; return __builtin_bit_cast(unsigned, __builtin_convertvector(v, bf16x2)); }
DI float rbf(float x) { return bfhi(pk2(x, x)); }
DI unsigned nosent(unsigned x) { return x < 0xfffffffeu ? x : 0xfffffffeu; }
#define GLDS(SC) "s_mov_b32 %0, m0\n\ts_mov_b32 m0, %2\n\ts_nop 0\n\tglobal_load_lds_dwordx4 %1, off" SC "\n\ts_mov_b32 m0, %0"
DI void glds16c(const void* g, unsigned l) { unsigned sv; asm volatile(GLDS(" sc1") : "=&s"(sv) : "v"(g), "s"(l) : "memory"); }
#define GLDSS(SC) "s_mov_b32 %0, m0\n\ts_mov_b32 m0, %3\n\ts_nop 0\n\tglobal_load_lds_dwordx4 %1, %2" SC "\n\ts_mov_b32 m0, %0"
DI void gldss(unsigned voff, const void* sbase, unsigned l) { unsigned sv; asm volatile(GLDSS("") : "=&s"(sv) : "v"(voff), "s"(sbase), "s"(l) : "memory"); }
DI void gldssc(unsigned voff, const void* sbase, unsigned l) { unsigned sv; asm volatile(GLDSS(" sc1") : "=&s"(sv) : "v"(voff), "s"(sbase), "s"(l) : "memory"); }
DI void glds9(const unsigned (&vo)[9], const void* sbase, unsigned l) {
  unsigned sv;
  asm volatile("s_mov_b32 %0, m0\n\ts_mov_b32 m0, %11\n\ts_nop 0\n\t"
               "global_load_lds_dwordx4 %1, %10\n\tglobal_load_lds_dwordx4 %2, %10 offset:1024\n\t"
               "global_load_lds_dwordx4 %3, %10 offset:2048\n\tglobal_load_lds_dwordx4 %4, %10 offset:3072\n\t"
               "s_add_u32 m0, m0, 0x1000\n\ts_nop 0\n\t"
               "global_load_lds_dwordx4 %5, %10\n\tglobal_load_lds_dwordx4 %6, %10 offset:1024\n\t"
               "global_load_lds_dwordx4 %7, %10 offset:2048\n\tglobal_load_lds_dwordx4 %8, %10 offset:3072\n\t"
               "s_add_u32 m0, m0, 0x1000\n\ts_nop 0\n\tglobal_load_lds_dwordx4 %9, %10\n\ts_mov_b32 m0, %0"
               : "=&s"(sv) : "v"(vo[0]), "v"(vo[1]), "v"(vo[2]), "v"(vo[3]), "v"(vo[4]), "v"(vo[5]), "v"(vo[6]), "v"(vo[7]), "v"(vo[8]),
                 "s"(sbase), "s"(l) : "memory", "scc");
}
DI void glds4c(unsigned voff, const void* sbase, unsigned l) {
  unsigned sv;
  asm volatile("s_mov_b32 %0, m0\n\ts_mov_b32 m0, %3\n\ts_nop 0\n\t"
               "global_load_lds_dwordx4 %1, %2 sc1\n\tglobal_load_lds_dwordx4 %1, %2 offset:1024 sc1\n\t"
               "global_load_lds_dwordx4 %1, %2 offset:2048 sc1\n\tglobal_load_lds_dwordx4 %1, %2 offset:3072 sc1\n\ts_mov_b32 m0, %0"
               : "=&s"(sv) : "v"(voff), "s"(sbase), "s"(l) : "memory");
}
template <int W> DI void flag_wait(const char* p, unsigned tag, u32x4& v) {
  if (__builtin_expect(!__all(v[W] == tag), 0)) {
#pragma unroll 1
    for (int spin = 0; spin < SPIN_MAX; ++spin) {
      __builtin_amdgcn_s_sleep(POLL1);
      if (v[W] != tag) asm volatile("global_load_dwordx4 %0, %1, off sc1\n\ts_waitcnt vmcnt(0)" : "+v"(v) : "v"(p) : "memory");
      if (__all(v[W] == tag)) break;
    }
  }
}
DI void touchs(unsigned voff, const void* sbase, unsigned junk) {
  unsigned sv;
  asm volatile("s_mov_b32 %0, m0\n\ts_mov_b32 m0, %3\n\ts_nop 0\n\tglobal_load_lds_dword %1, %2 sc1\n\ts_mov_b32 m0, %0" : "=&s"(sv) : "v"(voff), "s"(sbase), "s"(junk) : "memory");
}
DI void slice_offsets(unsigned (&vo)[9], int lane) {
#pragma unroll
  for (int r = 0; r < 9; ++r) {
    const int c = r * 64 + lane, row = c / 9, src = (row & ~7) + 4 * (row & 1) + ((row >> 1) & 3);
    vo[r] = src * ROWB + (c - row * 9) * 16 + 4096 - (r & 3) * 1024;
  }
}
DI void kv_group(const char* ws, unsigned lds0, int slot, int rot, int grp, unsigned l16) {
  const int off = grp < 4 ? grp * 4096 : 15360;
  glds4c(l16, ws + WS_KV + ((slot + rot) & 7) * CH_BYTES + off, lds0 + KV_LDS + slot * CH_BYTES + off);
}
DI void lds_wait(LAS unsigned* p, unsigned n) {
  if (__builtin_expect(__builtin_amdgcn_readfirstlane(__hip_atomic_load(p, __ATOMIC_RELAXED, __HIP_MEMORY_SCOPE_WORKGROUP)) < n, 0)) {
    do __builtin_amdgcn_s_sleep(1); while (__builtin_amdgcn_readfirstlane(__hip_atomic_load(p, __ATOMIC_RELAXED, __HIP_MEMORY_SCOPE_WORKGROUP)) < n);
  }
  asm volatile("" ::: "memory");
}
DI void st16c(void* p, u32x4 v) { asm volatile("global_store_dwordx4 %0, %1, off sc1\n\ts_nop 1" ::"v"(p), "v"(v) : "memory"); }
DI void ost8(void* p, u32x2 v) { asm volatile("global_store_dwordx2 %0, %1, off" ::"v"(p), "v"(v) : "memory"); }
DI void st8c(void* p, u32x2 v) { asm volatile("global_store_dwordx2 %0, %1, off sc1" ::"v"(p), "v"(v) : "memory"); }
DI void st4c(void* p, unsigned v) { asm volatile("global_store_dword %0, %1, off sc1" ::"v"(p), "v"(v) : "memory"); }
#define PIN(x) asm volatile("" : "+v"(x))
#define VMWAIT(n) do { asm volatile("s_waitcnt vmcnt(" #n ")" ::: "memory"); __builtin_amdgcn_sched_barrier(0); } while (0)
#define BAR() asm volatile("s_waitcnt lgkmcnt(0)\n\ts_barrier" ::: "memory")
#define BARVM(n) asm volatile("s_waitcnt vmcnt(" #n ") lgkmcnt(0)\n\ts_barrier" ::: "memory")
#define CFENCE() asm volatile("" ::: "memory")
DI s16x4 trd(lptr p) { return __builtin_amdgcn_ds_read_tr16_b64_v4i16((LAS s16x4*)p); }
DI bf16x8 trd2(lptr a, lptr b) { return __builtin_bit_cast(bf16x8, __builtin_shufflevector(trd(a), trd(b), 0, 1, 2, 3, 4, 5, 6, 7)); }
#define MFMA16(a, b, c) __builtin_amdgcn_mfma_f32_16x16x32_bf16((a), (b), (c), 0, 0, 0)
#define MFMA32(a, b, c) __builtin_amdgcn_mfma_f32_32x32x16_bf16((a), (b), (c), 0, 0, 0)
#define SMF(a, b, c) __builtin_amdgcn_smfmac_f32_32x32x32_bf16((a), (b), (c), (int)0xdddddddd, 0, 0)
template <int CTRL> DI unsigned dppu(unsigned x) { return (unsigned)__builtin_amdgcn_update_dpp((int)x, (int)x, CTRL, 0xf, 0xf, false); }
DI unsigned umax(unsigned a, unsigned b) { return a > b ? a : b; }
DI unsigned max8(unsigned x) { x = umax(x, dppu<0xB1>(x)); x = umax(x, dppu<0x4E>(x)); return umax(x, dppu<0x141>(x)); }
DI unsigned max16(unsigned x) { x = max8(x); return umax(x, dppu<0x140>(x)); }


DI f32x2 cmul(f32x2 a, f32x2 b) {
  const f32x2 t = (f32x2){a[0], a[0]} * b;
  return __builtin_elementwise_fma((f32x2){a[1], a[1]}, (f32x2){-b[1], b[0]}, t);
}
DI f32x2 cpow(float zr, float zi, float c) {
  const float e = __builtin_amdgcn_exp2f(zr * c), th = __builtin_amdgcn_fractf(zi * c);
  return e * (f32x2){__builtin_amdgcn_cosf(th), __builtin_amdgcn_sinf(th)};
}
constexpr int KG_ROW = 272, KG_TAB = 21 * KG_ROW;
struct kg_in { unsigned are, aim, bre, bim, cre, cim, ldt; };
DI void kgen_half(const kg_in& in, lptr tb, int lane, f32x4 (&d)[2][2]) {
  const float dt = rbf(expf(bflo(in.ldt)));
  const float zr = (dt * bflo(in.are)) * 1.4426950408889634f, zi = (dt * bflo(in.aim)) * 0.15915494309189535f;
  const f32x2 dB = {dt * bflo(in.bre), dt * bflo(in.bim)}, Cc = {bflo(in.cre), bflo(in.cim)};
  lptr tw = tb + lane * 4;
#define PUT(row, v) do { const f32x2 v_ = (v); *(LAS unsigned*)(tw + (row) * KG_ROW) = pk2(v_[0], v_[1]); } while (0)
  const f32x2 w1 = cpow(zr, -zi, 1.f), w8 = cpow(zr, zi, 8.f), w256 = cpow(zr, -zi, 256.f);
  const f32x2 w2 = cmul(w1, w1), w3 = cmul(w2, w1), w4 = cmul(w2, w2), w6 = cmul(w2, w4);
  *(LAS unsigned*)tw = 0x3f80u;
  PUT(1, w1); PUT(2, w2); PUT(3, w3); PUT(4, w4); PUT(5, cmul(w1, w4)); PUT(6, w6); PUT(7, cmul(w3, w4));
  PUT(8, w256); PUT(9, cmul(w2, w256)); PUT(10, cmul(w4, w256)); PUT(11, cmul(w6, w256));
  const f32x2 w512 = cmul(w256, w256), w768 = cmul(w512, w256), w1024 = cmul(w768, w256);
  PUT(12, w512); PUT(13, cmul(w512, w4)); PUT(14, w768); PUT(15, cmul(w768, w4));
  PUT(16, w1024); PUT(17, cmul(w256, w1024)); PUT(18, cmul(w512, w1024)); PUT(19, cmul(w768, w1024)); PUT(20, cmul(w1024, w1024));
  CFENCE();
  const int l15 = lane & 15, g = lane >> 4;
  lptr fb = tb + l15 * KG_ROW + g * 16, fb1 = tb + (16 + min(l15, 4)) * KG_ROW + g * 16;
  bf16x8 a0[4], a1[4];
#pragma unroll
  for (int kk = 0; kk < 4; ++kk) { a0[kk] = *(LAS const bf16x8*)(fb + kk * 64); a1[kk] = *(LAS const bf16x8*)(fb1 + kk * 64); }
  CFENCE();
  const f32x2 w16 = cmul(w8, w8), w32 = cmul(w16, w16), w64 = cmul(w32, w32);
  f32x2 bb[8];
  bb[0] = cmul(Cc, dB); bb[1] = cmul(bb[0], w8); bb[2] = cmul(bb[0], w16); bb[3] = cmul(bb[1], w16);
#pragma unroll
  for (int i = 0; i < 4; ++i) bb[4 + i] = cmul(bb[i], w32);
#pragma unroll
  for (int ct = 0; ct < 2; ++ct) {
#pragma unroll
    for (int i = 0; i < 2; ++i) {
#pragma unroll
      for (int j = 0; j < 8; ++j) PUT(8 * i + j, bb[j]);
      if (ct == 0 || i == 0) {
#pragma unroll
        for (int j = 0; j < 8; ++j) bb[j] = cmul(bb[j], w64);
      }
    }
    CFENCE();
    bf16x8 bf[4];
#pragma unroll
    for (int kk = 0; kk < 4; ++kk) bf[kk] = *(LAS const bf16x8*)(fb + kk * 64);
    CFENCE();
    d[ct][0] = (f32x4){0.f, 0.f, 0.f, 0.f}; d[ct][1] = d[ct][0];
#pragma unroll
    for (int kk = 0; kk < 4; ++kk) { d[ct][0] = MFMA16(a0[kk], bf[kk], d[ct][0]); d[ct][1] = MFMA16(a1[kk], bf[kk], d[ct][1]); }
  }
}
DI void kgen_compact(const f32x4 (&d)[2][2], lptr tb, int lane) {
  const int l15 = lane & 15, g = lane >> 4;
  {
    const int e0 = g < 2 ? 8 * l15 + 4 * g : (g == 2 ? 256 + 4 * l15 : 384 + 2 * l15), es = g < 2 ? 128 : (g == 2 ? 64 : 32), e1 = g == 3 ? 64 : 2;
    const int f0 = g == 0 ? 512 + l15 : (lane == 16 ? 640 : 700);
#pragma unroll
    for (int ct = 0; ct < 2; ++ct) {
      const lptr p = tb + (e0 + ct * es) * 2;
      *(LAS unsigned*)p = pk2(d[ct][0][0], d[ct][0][1]); *(LAS unsigned*)(p + e1 * 2) = pk2(d[ct][0][2], d[ct][0][3]);
      LAS u16* q = (LAS u16*)tb + f0 + ct * 16;
      const unsigned x0 = pk2(d[ct][1][0], d[ct][1][1]), x1 = pk2(d[ct][1][2], d[ct][1][3]);
      q[0] = (u16)x0; q[32] = (u16)(x0 >> 16); q[64] = (u16)x1; q[96] = (u16)(x1 >> 16);
    }
  }
  CFENCE();
}
DI void kgen_expand(lptr tb, lptr img, int lane) {
  LAS const u16* C = (LAS const u16*)tb;
  const int rl = 63 - lane;
#pragma unroll
  for (int r = 0; r < 2; ++r) {
    const unsigned a = C[512 + 64 * (1 - r) + rl], bb = C[513 + 64 * (1 - r) + rl];
    const unsigned aa = a | (a << 16), b2x = bb | (bb << 16);
    *(LAS u32x4*)(img + r * 1024 + lane * 16) = (u32x4){b2x, (rl & 1) ? b2x : (bb | (a << 16)), aa, aa};
  }
  {
    const unsigned ab = *(LAS const unsigned*)(C + 384 + 2 * rl), c = C[386 + 2 * rl];
    *(LAS u32x4*)(img + 2048 + lane * 16) = (u32x4){c | (c << 16), __builtin_amdgcn_perm(ab, ab, 0x03020302u), __builtin_amdgcn_perm(ab, ab, 0x01000302u),
                                                __builtin_amdgcn_perm(ab, ab, 0x01000100u)};
  }
  {
    const bool hi = lane < 32;
    const int e = hi ? 128 + 4 * rl : 8 * rl;
    const u32x2 w01 = *(LAS const u32x2*)(C + e), w23 = *(LAS const u32x2*)(C + e + 4);
    const unsigned o0 = hi ? __builtin_amdgcn_perm(w01[1], w23[0], 0x07060100u) : __builtin_amdgcn_perm(w23[1], w23[1], 0x01000302u);
    const unsigned o1 = __builtin_amdgcn_perm(w23[0], w01[1], hi ? 0x01000100u : 0x05040706u);
    const unsigned o2 = __builtin_amdgcn_perm(w01[1], w01[0], hi ? 0x03020504u : 0x05040706u);
    const unsigned o3 = __builtin_amdgcn_perm(w01[0], w01[0], hi ? 0x01000100u : 0x01000302u);
    *(LAS u32x4*)(img + 3072 + lane * 16) = (u32x4){o0, o1, o2, o3};
  }
  if (lane < 4) *(LAS u32x4*)(img + 4096 + lane * 16) = (u32x4){0u, 0u, 0u, 0u};
}

template <int NT> DI void qgemm(lptr lds, LAS unsigned* ctrl, const bf16x8 (&xa)[18], int mt, int lane) {
  const int l15 = lane & 15, g = lane >> 4;
  constexpr int nt0 = NT == 2 ? 3 : 0;
  f32x4 acc[NT];
#pragma unroll
  for (int i = 0; i < NT; ++i) acc[i] = (f32x4){0.f, 0.f, 0.f, 0.f};
  const lptr wq = lds + A_W + (g * 8 + (l15 >> 2) * 2) * 144 + (l15 & 3) * 8 + nt0 * 32;
  constexpr int PD = 4;
  bf16x8 wf[PD + 1][NT];
  unsigned stage = __builtin_amdgcn_readfirstlane(__hip_atomic_load(&ctrl[4], __ATOMIC_RELAXED, __HIP_MEMORY_SCOPE_WORKGROUP));
#define WNEED(n) do { if (__builtin_expect(stage < (n), 0)) {                                                                             \
      do stage = __builtin_amdgcn_readfirstlane(__hip_atomic_load(&ctrl[4], __ATOMIC_RELAXED, __HIP_MEMORY_SCOPE_WORKGROUP)); while (stage < (n)); } \
    CFENCE(); } while (0)
#define QRD(kk) _Pragma("unroll") for (int i = 0; i < NT; ++i) wf[(kk) % (PD + 1)][i] = trd2(wq + (kk) * 4608 + i * 32, wq + (kk) * 4608 + 144 + i * 32)
#define QMM(kk) _Pragma("unroll") for (int i = 0; i < NT; ++i) acc[i] = MFMA16(wf[(kk) % (PD + 1)][i], xa[kk], acc[i])
#define SB() __builtin_amdgcn_sched_barrier(0)
  WNEED(3u);
#pragma unroll
  for (int kk = 0; kk < 6 + PD; ++kk) { if (kk >= PD) { QMM(kk - PD); } SB(); if (kk < 6) { QRD(kk); } SB(); }
  WNEED(5u); QRD(6); QRD(7); QRD(8); QRD(9); SB(); QMM(6); QMM(7); QMM(8); QMM(9); SB();
  WNEED(7u); QRD(10); QRD(11); QRD(12); QRD(13); SB(); QMM(10); QMM(11); QMM(12); QMM(13); SB();
  WNEED(8u); QRD(14); QRD(15); SB(); QMM(14); QMM(15); SB();
  WNEED(9u); QRD(16); QRD(17); SB(); QMM(16); QMM(17); SB();
  LAS unsigned* qw = (LAS unsigned*)(lds + A_Q + (mt * 16 + l15) * Q_ROW + nt0 * 32 + g * 8);
#pragma unroll
  for (int i = 0; i < NT; ++i)
    if (nt0 + i < 4 || g < 2) *(LAS u32x2*)(qw + 8 * i) = (u32x2){pk2(acc[i][0] * QSCALE, acc[i][1] * QSCALE), pk2(acc[i][2] * QSCALE, acc[i][3] * QSCALE)};
  CFENCE();
  if (lane == 0) __hip_atomic_fetch_add(&ctrl[5], 1u, __ATOMIC_RELAXED, __HIP_MEMORY_SCOPE_WORKGROUP);
}

DI f32x32 qk_tile(lptr kp, const bf16x8& q0, const bf16x8& q1, unsigned kt, u32x2 qa) {
  const bf16x8 k = *(LAS const bf16x8*)kp;
  f32x16 z;
#pragma unroll
  for (int j = 0; j < 16; ++j) z[j] = 0.f;
  const f32x16 a = MFMA32(k, q0, z), b = MFMA32(k, q1, z);
  f32x32 s = __builtin_shufflevector(a, b, 0, 1, 2, 3, 4, 5, 6, 7, 8, 9, 10, 11, 12, 13, 14, 15, 16, 17, 18, 19, 20, 21, 22, 23, 24, 25, 26, 27, 28, 29, 30, 31);
  return __builtin_amdgcn_mfma_f32_32x32x4bf16_1k(__builtin_bit_cast(s16x4, (u32x2){kt, 0u}), __builtin_bit_cast(s16x4, qa), s, 0, 0, 0);
}
DI bf16x16 sub8(const f32x32& s, int o) {
  f32x8 t = {s[o], s[o + 1], s[o + 2], s[o + 3], s[o + 4], s[o + 5], s[o + 6], s[o + 7]};
  return __builtin_bit_cast(bf16x16, t);
}

__global__ __launch_bounds__(768, 3) void fused_kernel(const u16* __restrict__ U, const u16* __restrict__ Are, const u16* __restrict__ Aim,
                                                       const u16* __restrict__ Bre, const u16* __restrict__ Bim, const u16* __restrict__ Cre,
                                                       const u16* __restrict__ Cim, const u16* __restrict__ logdt, const u16* __restrict__ Dskip,
                                                       const u16* __restrict__ X, const u16* __restrict__ Wq, const u16* __restrict__ Wk,
                                                       const u16* __restrict__ Wv, const u16* __restrict__ Wo, u16* __restrict__ out,
                                                       char* __restrict__ ws) {
  __shared__ __attribute__((aligned(16))) char lds_raw[LDS_BYTES];
  const lptr lds = (lptr)lds_raw;
  const unsigned lds0 = (unsigned)(uintptr_t)lds_raw;
  const int tid = threadIdx.x, lane = tid & 63, wid = __builtin_amdgcn_readfirstlane(tid >> 6);
  const int l15 = lane & 15, g = lane >> 4, r32 = lane & 31, h = lane >> 5, q4 = l15 >> 2, p4 = l15 & 3;
  const int b = blockIdx.x, qb = (b & 7) * 4 + (b >> 6), hg = (b >> 3) & 7;
  const int ce = 512 + (b >> 2), cq = b & 3, mt = wid & 3;
  char* const myflag = ws + WS_F + (qb * 8 + hg) * 128;
  LAS unsigned* ctrl = (LAS unsigned*)(lds + A_CTRL);
  LAS unsigned* bctl = (LAS unsigned*)(lds + B_CTRL);
#define LADD(i) do { if (lane == 0) __hip_atomic_fetch_add(&ctrl[i], 1u, __ATOMIC_RELAXED, __HIP_MEMORY_SCOPE_WORKGROUP); } while (0)
#define LSET(i, v) do { if (lane == 0) __hip_atomic_store(&ctrl[i], (v), __ATOMIC_RELAXED, __HIP_MEMORY_SCOPE_WORKGROUP); } while (0)
  {
    const unsigned o1 = (((unsigned)(0x654321B9AAA9ull >> (4 * wid)) & 15) + (wid == 5 ? hg >> 2 : 0)) * 8, o2 = ((0x087F >> (4 * mt)) & 15) * 8;
    unsigned long long p1, p2; unsigned sa, sc, sd, se, sf, v1, v2;
    asm volatile("s_load_dwordx2 %[p1], %[ka], %[o1]\n\ts_load_dwordx2 %[p2], %[ka], %[o2]\n\t"
                 "s_and_b32 %[a], %[b], 7\n\ts_lshl_b32 %[a], %[a], 2\n\ts_lshr_b32 %[c], %[b], 6\n\ts_add_u32 %[a], %[a], %[c]\n\t"
                 "s_mul_i32 %[a], %[a], 0x12000\n\ts_lshr_b32 %[d], %[b], 3\n\t"
                 "s_and_b32 %[e], %[d], 7\n\ts_lshl_b32 %[e], %[e], 3\n\ts_add_u32 %[e], %[e], %[w]\n\t"
                 "s_mul_i32 %[e], %[e], 0x480\n\ts_add_u32 %[e], %[e], %[a]\n\t"
                 "s_mul_i32 %[d], %[d], 3\n\ts_add_u32 %[d], %[d], %[w]\n\ts_sub_u32 %[d], %[d], 1\n\ts_mul_i32 %[d], %[d], 0x1b00\n\t"
                 "s_mul_i32 %[c], %[c], 0x1440\n\ts_movk_i32 %[f], 80\n\t"
                 "s_cmp_lt_u32 %[w], 4\n\ts_cselect_b32 %[c], %[d], %[c]\n\ts_cselect_b32 %[d], 0x6c, %[f]\n\t"
                 "s_and_b32 %[f], %[w], 3\n\ts_cmp_eq_u32 %[f], 0\n\ts_cselect_b32 %[c], %[e], %[c]\n\ts_cselect_b32 %[d], 0x48, %[d]\n\t"
                 "s_lshr_b32 %[e], %[w], 2\n\ts_mul_i32 %[e], %[e], 0x8ca0\n\ts_add_u32 %[a], %[a], %[e]\n\t"
                 "s_cmp_eq_u32 %[f], 3\n\ts_cselect_b32 %[a], %[a], 0\n\tv_mov_b32 %[v2], %[a]\n\t"
                 "s_lshl_b32 %[e], %[b], 9\n\ts_lshr_b32 %[a], %[b], 2\n\ts_lshl_b32 %[a], %[a], 8\n\ts_add_u32 %[a], %[a], 0x1fd00\n\ts_sub_u32 %[a], %[a], %[e]\n\t"
                 "s_cmp_ge_u32 %[w], 6\n\ts_cselect_b32 %[c], %[e], %[c]\n\ts_cselect_b32 %[d], 16, %[d]\n\ts_cselect_b32 %[a], %[a], 0\n\t"
                 "v_mul_u32_u24 %[v1], %[d], %[ln]\n\tv_add_u32 %[v1], %[c], %[v1]\n\tv_mad_i32_i24 %[v1], %[h], %[a], %[v1]\n\t"
                 "s_mov_b32 %[f], m0\n\ts_mov_b32 m0, %[jk]\n\ts_waitcnt lgkmcnt(0)\n\t"
                 "global_load_lds_dword %[v1], %[p1] sc1\n\tglobal_load_lds_dword %[v2], %[p2] sc1\n\ts_mov_b32 m0, %[f]"
                 : [p1] "=&s"(p1), [p2] "=&s"(p2), [a] "=&s"(sa), [c] "=&s"(sc), [d] "=&s"(sd), [e] "=&s"(se), [f] "=&s"(sf), [v1] "=&v"(v1), [v2] "=&v"(v2)
                 : [ka] "s"(__builtin_amdgcn_kernarg_segment_ptr()), [o1] "s"(o1), [o2] "s"(o2), [b] "s"(b), [w] "s"(wid), [ln] "v"(lane), [h] "v"(h),
                   [jk] "s"(lds0 + A_W - 256)
                 : "memory", "scc");
  }
  asm volatile("" :: "s"(U), "s"(Are), "s"(Aim), "s"(Bre), "s"(Bim), "s"(Cre), "s"(Cim), "s"(logdt), "s"(Dskip), "s"(X), "s"(Wq), "s"(Wk), "s"(Wv), "s"(Wo),
               "s"(out), "s"(ws));
  const int xoff = mt == 0 ? A_X0 : A_W + 9216 + mt * 18432;
  const unsigned xv0 = (lane >> 3) * ROWB + (((lane & 7) ^ (lane >> 4)) * 16), xv1 = (8 + (lane >> 3)) * ROWB + (((lane & 7) ^ (4 + (lane >> 4))) * 16);
  const char* xs = (const char*)X + (size_t)(qb * 64 + mt * 16) * ROWB;
  const bool kgw = mt != (wid < 4 ? 0 : 3) && (wid < 4 || wid >= 8);
  const int kc = wid < 4 ? wid - 1 : wid - 8, kch = kc < 2 ? 2 * b + kc : ce;
  int msc = kgw ? A_SCR + (2 * kc + (wid < 4 ? 1 : 0)) * KG_TAB + 49 * 16 : A_MISC + (wid == 11 ? 80 : (wid == 0 ? 64 : mt * 16));
  asm volatile("" : "+v"(msc)); msc = __builtin_amdgcn_readfirstlane(msc);
  if (wid >= 4 && wid < 8) {
    __builtin_amdgcn_s_setprio(3);
#pragma unroll
    for (int kb = 0; kb < 2; ++kb) { gldss(xv0, xs + kb * 128, lds0 + xoff + kb * 2048); gldss(xv1, xs + kb * 128, lds0 + xoff + kb * 2048 + 1024); }
  } else if (wid == 0) {
    __builtin_amdgcn_s_setprio(3);
    const char* wx = (const char*)(hg < 4 ? Wk : Wv) + 8 * (hg & 3);
#pragma unroll
    for (int i = 0; i < 8; ++i) gldss(lane * 36, wx + i * 64 * 36, lds0 + A_WKV + i * 1024);
    if (lane < 63) gldss(lane * 36, wx + 512 * 36, lds0 + A_WKV + 8192);
    if (lane == 0) {
      unsigned sv_;
      asm volatile("s_mov_b32 %0, m0\n\ts_mov_b32 m0, %3\n\ts_nop 0\n\tglobal_load_lds_dwordx3 %1, %2\n\ts_mov_b32 m0, %0"
                   : "=&s"(sv_) : "v"(575 * 36), "s"(wx), "s"(lds0 + A_WKV + 575 * 16) : "memory");
    }
  }
  {
    const int a6 = lane >> 3;
    const u16* pa = a6 < 2 ? (a6 == 0 ? Are : Aim) : (a6 < 4 ? (a6 == 2 ? Bre : Bim) : (a6 == 4 ? Cre : Cim));
    pa += kch * NS + (wid < 4 ? 64 : 0) + (lane & 7) * 8;
    if (lane == 48) pa = logdt + (kch & ~7);
    if (lane == 49) pa = (const u16*)(ws + WS_E);
    if (lane >= 50) pa = Dskip + (kch & ~7);
    if (lane >= (kgw ? 0 : 49) && lane < (kgw ? 51 : 50)) glds16c(pa, lds0 + msc - 49 * 16);
  }
  if (tid < 32) ctrl[tid] = 0u;
  BAR();
#define TAG() (*(LAS const unsigned*)(lds + msc) * 2654435761u + 0x9e3779b9u)
  auto kg_read = [&](kg_in& in) {
    LAS const u16* p = (LAS const u16*)(lds + msc - 49 * 16) + lane;
    in.are = p[0]; in.aim = p[64]; in.bre = p[128]; in.bim = p[192]; in.cre = p[256]; in.cim = p[320];
    in.ldt = ((LAS const u16*)(lds + msc - 16))[kch & 7];
    CFENCE();
  };
#define FRESH_LANE() int tid_ = threadIdx.x; asm volatile("" : "+v"(tid_));                                                          \
  const int lane = tid_ & 63, l15 = lane & 15, g = lane >> 4, r32 = lane & 31, h = lane >> 5, q4 = l15 >> 2, p4 = l15 & 3;             \
  (void)r32; (void)h; (void)q4; (void)p4; (void)g
  auto any_bad = [&]() { return __builtin_amdgcn_readfirstlane((int)bctl[8]); };

  if (wid < 8) {
    {
      const lptr xb0 = lds + xoff + l15 * 128 + ((g ^ (l15 >> 1)) * 16), xb1 = lds + xoff + l15 * 128 + (((4 + g) ^ (l15 >> 1)) * 16);
      bf16x8 xa[18];
      if (wid >= 4) {
#pragma unroll
        for (int kb = 2; kb < 9; ++kb) {
          gldss(xv0, xs + kb * 128, lds0 + xoff + kb * 2048); gldss(xv1, xs + kb * 128, lds0 + xoff + kb * 2048 + 1024);
        }
        char* cbp = ws + WS_KV + (qb >> 2) * CH_BYTES;
        const int key0 = (qb & 3) * 64 + mt * 16;
        char* sp;
        if (hg < 4) {
          const int key = key0 + l15;
          sp = g == 0 ? cbp + (key >> 5) * 1024 + (hg >> 1) * 512 + (key & 31) * 16 + (hg & 1) * 8 : cbp + CH_TAIL + (key & 63) * 16 + (key >> 6) * 4;
        } else {
          const int key = key0 + 4 * g, dim = 4 * (hg - 4) + l15;
          const int vrow = l15 < 4 ? ((dim & 3) | ((dim & 4) << 1) | ((dim & 8) >> 1)) : 12 + l15;
          sp = cbp + CH_V + (key >> 3) * V_OCT + (key & 4) * 2 + vrow * 16;
        }
        PIN(sp);
        lds_wait(&ctrl[0], 1u);
        const lptr wb = lds + A_WKV + (g * 8 + q4) * 16 + (l15 & 1) * 8;
        bf16x8 wk[18];
#pragma unroll
        for (int i = 0; i < 18; ++i) wk[i] = trd2(wb + i * 512, wb + i * 512 + 64);
        f32x4 acc = {0.f, 0.f, 0.f, 0.f}, acc2 = acc;
#define XGRP(kb0, n, KTYPE)                                                                                                    \
        { VMWAIT(n);                                                                                                           \
          if ((n) == 0) LSET(8 + mt, 1u);                                                                                      \
          _Pragma("unroll") for (int i = 0; i < 6; ++i) xa[2 * (kb0) + i] = *(LAS const bf16x8*)(((i & 1) ? xb1 : xb0) + ((kb0) + (i >> 1)) * 2048); \
          CFENCE();                                                                                                            \
          if ((n) == 0) LADD(12 + mt);                                                                                         \
          _Pragma("unroll") for (int i = 2 * (kb0); i < 2 * (kb0) + 6; i += 2) {                                                \
            if (KTYPE) { acc = MFMA16(wk[i], xa[i], acc); acc2 = MFMA16(wk[i + 1], xa[i + 1], acc2); }                          \
            else { acc = MFMA16(xa[i], wk[i], acc); acc2 = MFMA16(xa[i + 1], wk[i + 1], acc2); } } }
        if (hg < 4) { XGRP(0, 12, 1) XGRP(3, 6, 1) XGRP(6, 0, 1) } else { XGRP(0, 12, 0) XGRP(3, 6, 0) XGRP(6, 0, 0) }
        acc += acc2;
        u32x2 v = {pk2(acc[0], acc[1]), pk2(acc[2], acc[3])};
        unsigned mv;
        if (hg < 4) {
          if (g == 0) st8c(sp, v);
          if (g == 1 && hg == 3) st4c(sp, v[0]);
          float n2 = bflo(v[0]) * bflo(v[0]) + bfhi(v[0]) * bfhi(v[0]);
          if (g == 0) n2 += bflo(v[1]) * bflo(v[1]) + bfhi(v[1]) * bfhi(v[1]);
          const unsigned m16 = max16(__float_as_uint(n2) & 0x7fffffffu);
          const float mq = __uint_as_float(__builtin_amdgcn_readlane(m16, 0)), mtl = __uint_as_float(__builtin_amdgcn_readlane(m16, 16));
          mv = __float_as_uint(hg == 3 ? mq + mtl : mq) & 0x7fffffffu;
        } else {
          unsigned am = umax(umax(v[0] << 16 & 0x7fffffffu, v[0] & 0x7fff0000u), umax(v[1] << 16 & 0x7fffffffu, v[1] & 0x7fff0000u));
          if (l15 >= 6) am = 0u;
          if (l15 == 6) v = (u32x2){0x3f803f80u, 0x3f803f80u};
          if (l15 == 7) v = (u32x2){0u, 0u};
          if (l15 < 4 || (hg == 7 && l15 < 8)) st8c(sp, v);
          am = max16(am);
          mv = umax(umax(__builtin_amdgcn_readlane(am, 0), __builtin_amdgcn_readlane(am, 16)), umax(__builtin_amdgcn_readlane(am, 32), __builtin_amdgcn_readlane(am, 48)));
        }
        if (lane == 0) __hip_atomic_fetch_max(&ctrl[2], mv, __ATOMIC_RELAXED, __HIP_MEMORY_SCOPE_WORKGROUP);
      } else {
        if (wid == 0) {
          __builtin_amdgcn_s_setprio(2);
          unsigned vo[9];
          slice_offsets(vo, lane);
          glds9(vo, (const char*)U + (size_t)qb * 64 * ROWB + hg * 144 - 4096, lds0 + A_UT);
          VMWAIT(10);
          LSET(0, 1u);
          const lptr rp = lds + A_UT + (((4 * g + p4) & 7) * 8 + 2 * q4) * 144 + (g >> 1) * 8;
          char* up = ws + WS_UT + (size_t)(hg * 72 + (lane >> 5) * 4 + (lane & 3)) * UT_ROW + 128 + qb * 128 + ((lane >> 2) & 7) * 16;
          VMWAIT(0);
          bf16x8 ut[9];
#pragma unroll
          for (int i = 0; i < 9; ++i) ut[i] = trd2(rp + i * 16, rp + i * 16 + 144);
          CFENCE();
#pragma unroll
          for (int i = 0; i < 9; ++i) st16c(up + i * 8 * UT_ROW, __builtin_bit_cast(u32x4, ut[i]));
          if (__builtin_expect(qb == 0, 0)) {
            st16c(ws + WS_UT + (size_t)(hg * 72 + lane) * UT_ROW + 112, (u32x4){0u, 0u, 0u, 0u});
            st16c(ws + WS_UT + (size_t)(hg * 72 + 64 + (lane & 7)) * UT_ROW + 112, (u32x4){0u, 0u, 0u, 0u});
          }
        } else {
          __builtin_amdgcn_s_setprio(2);
          VMWAIT(0);
          kg_in in; kg_read(in);
          f32x4 d[2][2];
          const lptr tb = lds + A_SCR + (2 * wid - 1) * KG_TAB;
          kgen_half(in, tb, lane, d);
#pragma unroll
          for (int i = 0; i < 4; ++i) *(LAS f32x4*)(tb + i * 1024 + lane * 16) = d[i >> 1][i & 1];
          CFENCE();
          LSET(15 + wid, 1u);
          VMWAIT(0);
        }
        lds_wait(&ctrl[8 + mt], 1u);
#pragma unroll
        for (int kk = 0; kk < 18; ++kk) xa[kk] = *(LAS const bf16x8*)(((kk & 1) ? xb1 : xb0) + (kk >> 1) * 2048);
        CFENCE();
        LADD(12 + mt);
      }
      if (wid >= 4) {
        VMWAIT(0);
        if (lane == 0 && __hip_atomic_fetch_add(&ctrl[1], 1u, __ATOMIC_RELAXED, __HIP_MEMORY_SCOPE_WORKGROUP) == 3u)
          st8c(myflag, (u32x2){TAG(), __hip_atomic_load(&ctrl[2], __ATOMIC_RELAXED, __HIP_MEMORY_SCOPE_WORKGROUP)});
        __builtin_amdgcn_s_setprio(2);
      } else {
        VMWAIT(0);
        if (wid == 0 && lane == 0) st4c(myflag + 8, TAG());
      }
      if (wid >= 4) qgemm<2>(lds, ctrl, xa, mt, lane); else qgemm<3>(lds, ctrl, xa, mt, lane);
    }
    FRESH_LANE();
    const int kp = wid >> 2, head = wid & 3;
    f32x16 o0, o1;
#pragma unroll
    for (int j = 0; j < 16; ++j) { o0[j] = 0.f; o1[j] = 0.f; }
    const unsigned ka0 = lds0 + KV_LDS + kp * 1024 + lane * 16, ta0 = lds0 + KV_LDS + CH_TAIL + kp * 512 + r32 * 16,
                   va0 = lds0 + KV_LDS + CH_V + kp * 4 * V_OCT + h * V_OCT + (r32 < 19 ? r32 : 19) * 16;
    unsigned oo = (r32 >> 4) * 18432 + l15 * 16 + h * 256;
    PIN(oo); PIN(o0); PIN(o1);
    u32x4 q0, q1; unsigned qtl;
    lds_wait(&ctrl[5], 8u);
    {
      LAS const unsigned *qp0 = (LAS const unsigned*)(lds + A_Q + (kp * 32 + r32) * Q_ROW + head * 36 + h * 16), *qp1 = qp0 + (32 - 64 * kp) * (Q_ROW / 4);
#pragma unroll
      for (int j = 0; j < 4; ++j) { q0[j] = qp0[j]; q1[j] = qp1[j]; }
      qtl = *(LAS const unsigned*)(lds + A_Q + (lane ^ (kp * 32)) * Q_ROW + head * 36 + 32);
    }
    CFENCE();
    LADD(6);
    const u32x2 qa = {qtl, 0u}, qc = {0u, qtl};
    lds_wait(&ctrl[7], 1u);
    {
      int it = 8; unsigned ka = ka0, va = va0, ta = ta0;
      asm volatile(
      "ds_read_b128 v[128:131], %[ka]\n"
      "ds_read_b128 v[140:143], %[ta]\n"
      "s_waitcnt lgkmcnt(1)\n"
      "v_mfma_f32_32x32x16_bf16 v[64:79], v[128:131], %[q0], 0\n"
      "v_mfma_f32_32x32x16_bf16 v[80:95], v[128:131], %[q1], 0\n"
      "ds_read_b128 v[128:131], %[ka] offset:2048\n"
      "ds_read_b128 v[132:135], %[va]\n"
      "s_nop 7\n"
      "s_waitcnt lgkmcnt(2)\n"
      "v_mfma_f32_32x32x4_2b_bf16 v[64:95], v[140:141], %[qa], v[64:95]\n"
      "s_nop 15\n"
      "s_nop 1\n"
      "s_setprio 1\n"
      "s_waitcnt lgkmcnt(1)\n"
      "v_mfma_f32_32x32x16_bf16 v[96:111], v[128:131], %[q0], 0\n"
      "v_exp_f32 v64, v64\n"
      "v_exp_f32 v65, v65\n"
      "v_exp_f32 v66, v66\n"
      "ds_read_b128 v[136:139], %[va] offset:640\n"
      "v_exp_f32 v67, v67\n"
      "v_exp_f32 v68, v68\n"
      "v_exp_f32 v69, v69\n"
      "v_mfma_f32_32x32x16_bf16 v[112:127], v[128:131], %[q1], 0\n"
      "ds_read_b128 v[128:131], %[ka] offset:4096\n"
      "v_exp_f32 v70, v70\n"
      "v_exp_f32 v71, v71\n"
      "v_exp_f32 v72, v72\n"
      "v_exp_f32 v73, v73\n"
      "s_waitcnt lgkmcnt(2)\n"
      "v_smfmac_f32_32x32x32_bf16 %[o0], v[132:135], v[64:71], %[ix]\n"
      "v_exp_f32 v74, v74\n"
      "v_exp_f32 v75, v75\n"
      "v_exp_f32 v76, v76\n"
      "v_exp_f32 v77, v77\n"
      "v_exp_f32 v78, v78\n"
      "v_mfma_f32_32x32x4_2b_bf16 v[96:127], v[140:141], %[qc], v[96:127]\n"
      "v_exp_f32 v79, v79\n"
      "v_exp_f32 v80, v80\n"
      "v_exp_f32 v81, v81\n"
      "v_exp_f32 v82, v82\n"
      "v_exp_f32 v83, v83\n"
      "v_exp_f32 v84, v84\n"
      "v_exp_f32 v85, v85\n"
      "v_exp_f32 v86, v86\n"
      "s_waitcnt lgkmcnt(1)\n"
      "v_smfmac_f32_32x32x32_bf16 %[o0], v[136:139], v[72:79], %[ix]\n"
      "v_exp_f32 v87, v87\n"
      "v_exp_f32 v88, v88\n"
      "v_exp_f32 v89, v89\n"
      "v_exp_f32 v90, v90\n"
      "v_smfmac_f32_32x32x32_bf16 %[o1], v[132:135], v[80:87], %[ix]\n"
      "ds_read_b128 v[132:135], %[va] offset:2560\n"
      "v_exp_f32 v91, v91\n"
      "v_exp_f32 v92, v92\n"
      "v_exp_f32 v93, v93\n"
      "v_exp_f32 v94, v94\n"
      "v_exp_f32 v95, v95\n"
      "s_setprio 0\n"
      "s_waitcnt lgkmcnt(1)\n"
      "v_mfma_f32_32x32x16_bf16 v[64:79], v[128:131], %[q0], 0\n"
      "v_exp_f32 v96, v96\n"
      "v_exp_f32 v97, v97\n"
      "v_exp_f32 v98, v98\n"
      "v_smfmac_f32_32x32x32_bf16 %[o1], v[136:139], v[88:95], %[ix]\n"
      "ds_read_b128 v[136:139], %[va] offset:3200\n"
      "v_exp_f32 v99, v99\n"
      "v_exp_f32 v100, v100\n"
      "v_exp_f32 v101, v101\n"
      "v_mfma_f32_32x32x16_bf16 v[80:95], v[128:131], %[q1], 0\n"
      "ds_read_b128 v[128:131], %[ka] offset:6144\n"
      "v_exp_f32 v102, v102\n"
      "v_exp_f32 v103, v103\n"
      "v_exp_f32 v104, v104\n"
      "v_exp_f32 v105, v105\n"
      "s_waitcnt lgkmcnt(2)\n"
      "v_smfmac_f32_32x32x32_bf16 %[o0], v[132:135], v[96:103], %[ix]\n"
      "v_exp_f32 v106, v106\n"
      "v_exp_f32 v107, v107\n"
      "v_exp_f32 v108, v108\n"
      "v_exp_f32 v109, v109\n"
      "v_exp_f32 v110, v110\n"
      "v_mfma_f32_32x32x4_2b_bf16 v[64:95], v[142:143], %[qa], v[64:95]\n"
      "v_exp_f32 v111, v111\n"
      "v_exp_f32 v112, v112\n"
      "v_exp_f32 v113, v113\n"
      "v_exp_f32 v114, v114\n"
      "v_exp_f32 v115, v115\n"
      "v_exp_f32 v116, v116\n"
      "v_exp_f32 v117, v117\n"
      "v_exp_f32 v118, v118\n"
      "s_waitcnt lgkmcnt(1)\n"
      "v_smfmac_f32_32x32x32_bf16 %[o0], v[136:139], v[104:111], %[ix]\n"
      "v_exp_f32 v119, v119\n"
      "v_exp_f32 v120, v120\n"
      "v_exp_f32 v121, v121\n"
      "v_exp_f32 v122, v122\n"
      "v_smfmac_f32_32x32x32_bf16 %[o1], v[132:135], v[112:119], %[ix]\n"
      "ds_read_b128 v[132:135], %[va] offset:5120\n"
      "v_exp_f32 v123, v123\n"
      "v_exp_f32 v124, v124\n"
      "v_exp_f32 v125, v125\n"
      "v_exp_f32 v126, v126\n"
      "v_exp_f32 v127, v127\n"
      "s_setprio 3\n"
      "s_waitcnt lgkmcnt(1)\n"
      "v_mfma_f32_32x32x16_bf16 v[96:111], v[128:131], %[q0], 0\n"
      "v_exp_f32 v64, v64\n"
      "v_exp_f32 v65, v65\n"
      "v_exp_f32 v66, v66\n"
      "v_smfmac_f32_32x32x32_bf16 %[o1], v[136:139], v[120:127], %[ix]\n"
      "ds_read_b128 v[136:139], %[va] offset:5760\n"
      "v_exp_f32 v67, v67\n"
      "v_exp_f32 v68, v68\n"
      "v_exp_f32 v69, v69\n"
      "v_mfma_f32_32x32x16_bf16 v[112:127], v[128:131], %[q1], 0\n"
      "s_barrier\n"
      "ds_read_b128 v[128:131], %[ka] offset:19456\n"
      "v_exp_f32 v70, v70\n"
      "v_exp_f32 v71, v71\n"
      "v_exp_f32 v72, v72\n"
      "v_exp_f32 v73, v73\n"
      "s_waitcnt lgkmcnt(2)\n"
      "v_smfmac_f32_32x32x32_bf16 %[o0], v[132:135], v[64:71], %[ix]\n"
      "v_exp_f32 v74, v74\n"
      "v_exp_f32 v75, v75\n"
      "v_exp_f32 v76, v76\n"
      "v_exp_f32 v77, v77\n"
      "v_exp_f32 v78, v78\n"
      "v_mfma_f32_32x32x4_2b_bf16 v[96:127], v[142:143], %[qc], v[96:127]\n"
      "ds_read_b128 v[140:143], %[ta] offset:19456\n"
      "v_exp_f32 v79, v79\n"
      "v_exp_f32 v80, v80\n"
      "v_exp_f32 v81, v81\n"
      "v_exp_f32 v82, v82\n"
      "v_exp_f32 v83, v83\n"
      "v_exp_f32 v84, v84\n"
      "v_exp_f32 v85, v85\n"
      "v_exp_f32 v86, v86\n"
      "s_waitcnt lgkmcnt(2)\n"
      "v_smfmac_f32_32x32x32_bf16 %[o0], v[136:139], v[72:79], %[ix]\n"
      "v_exp_f32 v87, v87\n"
      "v_exp_f32 v88, v88\n"
      "v_exp_f32 v89, v89\n"
      "v_exp_f32 v90, v90\n"
      "v_smfmac_f32_32x32x32_bf16 %[o1], v[132:135], v[80:87], %[ix]\n"
      "ds_read_b128 v[132:135], %[va] offset:7680\n"
      "v_exp_f32 v91, v91\n"
      "v_exp_f32 v92, v92\n"
      "v_exp_f32 v93, v93\n"
      "v_exp_f32 v94, v94\n"
      "v_exp_f32 v95, v95\n"
      "s_setprio 2\n"
      "s_waitcnt lgkmcnt(1)\n"
      "v_mfma_f32_32x32x16_bf16 v[64:79], v[128:131], %[q0], 0\n"
      "v_exp_f32 v96, v96\n"
      "v_exp_f32 v97, v97\n"
      "v_exp_f32 v98, v98\n"
      "v_smfmac_f32_32x32x32_bf16 %[o1], v[136:139], v[88:95], %[ix]\n"
      "ds_read_b128 v[136:139], %[va] offset:8320\n"
      "v_exp_f32 v99, v99\n"
      "v_exp_f32 v100, v100\n"
      "v_exp_f32 v101, v101\n"
      "v_mfma_f32_32x32x16_bf16 v[80:95], v[128:131], %[q1], 0\n"
      "ds_read_b128 v[128:131], %[ka] offset:21504\n"
      "v_exp_f32 v102, v102\n"
      "v_exp_f32 v103, v103\n"
      "v_exp_f32 v104, v104\n"
      "v_exp_f32 v105, v105\n"
      "s_waitcnt lgkmcnt(2)\n"
      "v_smfmac_f32_32x32x32_bf16 %[o0], v[132:135], v[96:103], %[ix]\n"
      "v_exp_f32 v106, v106\n"
      "v_exp_f32 v107, v107\n"
      "v_exp_f32 v108, v108\n"
      "v_exp_f32 v109, v109\n"
      "v_exp_f32 v110, v110\n"
      "v_mfma_f32_32x32x4_2b_bf16 v[64:95], v[140:141], %[qa], v[64:95]\n"
      "v_exp_f32 v111, v111\n"
      "v_exp_f32 v112, v112\n"
      "v_exp_f32 v113, v113\n"
      "v_exp_f32 v114, v114\n"
      "v_exp_f32 v115, v115\n"
      "v_exp_f32 v116, v116\n"
      "v_exp_f32 v117, v117\n"
      "v_exp_f32 v118, v118\n"
      "s_waitcnt lgkmcnt(1)\n"
      "v_smfmac_f32_32x32x32_bf16 %[o0], v[136:139], v[104:111], %[ix]\n"
      "v_exp_f32 v119, v119\n"
      "v_exp_f32 v120, v120\n"
      "v_exp_f32 v121, v121\n"
      "v_exp_f32 v122, v122\n"
      "v_smfmac_f32_32x32x32_bf16 %[o1], v[132:135], v[112:119], %[ix]\n"
      "ds_read_b128 v[132:135], %[va] offset:19456\n"
      "v_exp_f32 v123, v123\n"
      "v_exp_f32 v124, v124\n"
      "v_exp_f32 v125, v125\n"
      "v_exp_f32 v126, v126\n"
      "v_exp_f32 v127, v127\n"
      "s_setprio 1\n"
      "s_waitcnt lgkmcnt(1)\n"
      "v_mfma_f32_32x32x16_bf16 v[96:111], v[128:131], %[q0], 0\n"
      "v_exp_f32 v64, v64\n"
      "v_exp_f32 v65, v65\n"
      "v_exp_f32 v66, v66\n"
      "v_smfmac_f32_32x32x32_bf16 %[o1], v[136:139], v[120:127], %[ix]\n"
      "ds_read_b128 v[136:139], %[va] offset:20096\n"
      "v_exp_f32 v67, v67\n"
      "v_exp_f32 v68, v68\n"
      "v_exp_f32 v69, v69\n"
      "v_mfma_f32_32x32x16_bf16 v[112:127], v[128:131], %[q1], 0\n"
      "ds_read_b128 v[128:131], %[ka] offset:23552\n"
      "v_exp_f32 v70, v70\n"
      "v_exp_f32 v71, v71\n"
      "v_exp_f32 v72, v72\n"
      "v_exp_f32 v73, v73\n"
      "s_waitcnt lgkmcnt(2)\n"
      "v_smfmac_f32_32x32x32_bf16 %[o0], v[132:135], v[64:71], %[ix]\n"
      "v_exp_f32 v74, v74\n"
      "v_exp_f32 v75, v75\n"
      "v_exp_f32 v76, v76\n"
      "v_exp_f32 v77, v77\n"
      "v_exp_f32 v78, v78\n"
      "v_mfma_f32_32x32x4_2b_bf16 v[96:127], v[140:141], %[qc], v[96:127]\n"
      "v_exp_f32 v79, v79\n"
      "v_exp_f32 v80, v80\n"
      "v_exp_f32 v81, v81\n"
      "v_exp_f32 v82, v82\n"
      "v_exp_f32 v83, v83\n"
      "v_exp_f32 v84, v84\n"
      "v_exp_f32 v85, v85\n"
      "v_exp_f32 v86, v86\n"
      "s_waitcnt lgkmcnt(1)\n"
      "v_smfmac_f32_32x32x32_bf16 %[o0], v[136:139], v[72:79], %[ix]\n"
      "v_exp_f32 v87, v87\n"
      "v_exp_f32 v88, v88\n"
      "v_exp_f32 v89, v89\n"
      "v_exp_f32 v90, v90\n"
      "v_smfmac_f32_32x32x32_bf16 %[o1], v[132:135], v[80:87], %[ix]\n"
      "ds_read_b128 v[132:135], %[va] offset:22016\n"
      "v_exp_f32 v91, v91\n"
      "v_exp_f32 v92, v92\n"
      "v_exp_f32 v93, v93\n"
      "v_exp_f32 v94, v94\n"
      "v_exp_f32 v95, v95\n"
      "s_setprio 0\n"
      "s_waitcnt lgkmcnt(1)\n"
      "v_mfma_f32_32x32x16_bf16 v[64:79], v[128:131], %[q0], 0\n"
      "v_exp_f32 v96, v96\n"
      "v_exp_f32 v97, v97\n"
      "v_exp_f32 v98, v98\n"
      "v_smfmac_f32_32x32x32_bf16 %[o1], v[136:139], v[88:95], %[ix]\n"
      "ds_read_b128 v[136:139], %[va] offset:22656\n"
      "v_exp_f32 v99, v99\n"
      "v_exp_f32 v100, v100\n"
      "v_exp_f32 v101, v101\n"
      "v_mfma_f32_32x32x16_bf16 v[80:95], v[128:131], %[q1], 0\n"
      "ds_read_b128 v[128:131], %[ka] offset:25600\n"
      "v_exp_f32 v102, v102\n"
      "v_exp_f32 v103, v103\n"
      "v_exp_f32 v104, v104\n"
      "v_exp_f32 v105, v105\n"
      "s_waitcnt lgkmcnt(2)\n"
      "v_smfmac_f32_32x32x32_bf16 %[o0], v[132:135], v[96:103], %[ix]\n"
      "v_exp_f32 v106, v106\n"
      "v_exp_f32 v107, v107\n"
      "v_exp_f32 v108, v108\n"
      "v_exp_f32 v109, v109\n"
      "v_exp_f32 v110, v110\n"
      "v_mfma_f32_32x32x4_2b_bf16 v[64:95], v[142:143], %[qa], v[64:95]\n"
      "v_exp_f32 v111, v111\n"
      "v_exp_f32 v112, v112\n"
      "v_exp_f32 v113, v113\n"
      "v_exp_f32 v114, v114\n"
      "v_exp_f32 v115, v115\n"
      "v_exp_f32 v116, v116\n"
      "v_exp_f32 v117, v117\n"
      "v_exp_f32 v118, v118\n"
      "s_waitcnt lgkmcnt(1)\n"
      "v_smfmac_f32_32x32x32_bf16 %[o0], v[136:139], v[104:111], %[ix]\n"
      "v_exp_f32 v119, v119\n"
      "v_exp_f32 v120, v120\n"
      "v_exp_f32 v121, v121\n"
      "v_exp_f32 v122, v122\n"
      "v_smfmac_f32_32x32x32_bf16 %[o1], v[132:135], v[112:119], %[ix]\n"
      "ds_read_b128 v[132:135], %[va] offset:24576\n"
      "v_exp_f32 v123, v123\n"
      "v_exp_f32 v124, v124\n"
      "v_exp_f32 v125, v125\n"
      "v_exp_f32 v126, v126\n"
      "v_exp_f32 v127, v127\n"
      "s_setprio 3\n"
      "s_waitcnt lgkmcnt(1)\n"
      "v_mfma_f32_32x32x16_bf16 v[96:111], v[128:131], %[q0], 0\n"
      "v_exp_f32 v64, v64\n"
      "v_exp_f32 v65, v65\n"
      "v_exp_f32 v66, v66\n"
      "v_smfmac_f32_32x32x32_bf16 %[o1], v[136:139], v[120:127], %[ix]\n"
      "ds_read_b128 v[136:139], %[va] offset:25216\n"
      "v_exp_f32 v67, v67\n"
      "v_exp_f32 v68, v68\n"
      "v_exp_f32 v69, v69\n"
      "v_mfma_f32_32x32x16_bf16 v[112:127], v[128:131], %[q1], 0\n"
      "s_barrier\n"
      "ds_read_b128 v[128:131], %[ka] offset:38912\n"
      "v_exp_f32 v70, v70\n"
      "v_exp_f32 v71, v71\n"
      "v_exp_f32 v72, v72\n"
      "v_exp_f32 v73, v73\n"
      "s_waitcnt lgkmcnt(2)\n"
      "v_smfmac_f32_32x32x32_bf16 %[o0], v[132:135], v[64:71], %[ix]\n"
      "v_exp_f32 v74, v74\n"
      "v_exp_f32 v75, v75\n"
      "v_exp_f32 v76, v76\n"
      "v_exp_f32 v77, v77\n"
      "v_exp_f32 v78, v78\n"
      "v_mfma_f32_32x32x4_2b_bf16 v[96:127], v[142:143], %[qc], v[96:127]\n"
      "ds_read_b128 v[140:143], %[ta] offset:38912\n"
      "v_exp_f32 v79, v79\n"
      "v_exp_f32 v80, v80\n"
      "v_exp_f32 v81, v81\n"
      "v_exp_f32 v82, v82\n"
      "v_exp_f32 v83, v83\n"
      "v_exp_f32 v84, v84\n"
      "v_exp_f32 v85, v85\n"
      "v_exp_f32 v86, v86\n"
      "s_waitcnt lgkmcnt(2)\n"
      "v_smfmac_f32_32x32x32_bf16 %[o0], v[136:139], v[72:79], %[ix]\n"
      "v_exp_f32 v87, v87\n"
      "v_exp_f32 v88, v88\n"
      "v_exp_f32 v89, v89\n"
      "v_exp_f32 v90, v90\n"
      "v_smfmac_f32_32x32x32_bf16 %[o1], v[132:135], v[80:87], %[ix]\n"
      "ds_read_b128 v[132:135], %[va] offset:27136\n"
      "v_exp_f32 v91, v91\n"
      "v_exp_f32 v92, v92\n"
      "v_exp_f32 v93, v93\n"
      "v_exp_f32 v94, v94\n"
      "v_exp_f32 v95, v95\n"
      "s_setprio 2\n"
      "s_waitcnt lgkmcnt(1)\n"
      "v_mfma_f32_32x32x16_bf16 v[64:79], v[128:131], %[q0], 0\n"
      "v_exp_f32 v96, v96\n"
      "v_exp_f32 v97, v97\n"
      "v_exp_f32 v98, v98\n"
      "v_smfmac_f32_32x32x32_bf16 %[o1], v[136:139], v[88:95], %[ix]\n"
      "ds_read_b128 v[136:139], %[va] offset:27776\n"
      "v_exp_f32 v99, v99\n"
      "v_exp_f32 v100, v100\n"
      "v_exp_f32 v101, v101\n"
      "v_mfma_f32_32x32x16_bf16 v[80:95], v[128:131], %[q1], 0\n"
      "ds_read_b128 v[128:131], %[ka] offset:40960\n"
      "v_exp_f32 v102, v102\n"
      "v_exp_f32 v103, v103\n"
      "v_exp_f32 v104, v104\n"
      "v_exp_f32 v105, v105\n"
      "s_waitcnt lgkmcnt(2)\n"
      "v_smfmac_f32_32x32x32_bf16 %[o0], v[132:135], v[96:103], %[ix]\n"
      "v_exp_f32 v106, v106\n"
      "v_exp_f32 v107, v107\n"
      "v_exp_f32 v108, v108\n"
      "v_exp_f32 v109, v109\n"
      "v_exp_f32 v110, v110\n"
      "v_mfma_f32_32x32x4_2b_bf16 v[64:95], v[140:141], %[qa], v[64:95]\n"
      "v_exp_f32 v111, v111\n"
      "v_exp_f32 v112, v112\n"
      "v_exp_f32 v113, v113\n"
      "v_exp_f32 v114, v114\n"
      "v_exp_f32 v115, v115\n"
      "v_exp_f32 v116, v116\n"
      "v_exp_f32 v117, v117\n"
      "v_exp_f32 v118, v118\n"
      "s_waitcnt lgkmcnt(1)\n"
      "v_smfmac_f32_32x32x32_bf16 %[o0], v[136:139], v[104:111], %[ix]\n"
      "v_exp_f32 v119, v119\n"
      "v_exp_f32 v120, v120\n"
      "v_exp_f32 v121, v121\n"
      "v_exp_f32 v122, v122\n"
      "v_smfmac_f32_32x32x32_bf16 %[o1], v[132:135], v[112:119], %[ix]\n"
      "ds_read_b128 v[132:135], %[va] offset:38912\n"
      "v_exp_f32 v123, v123\n"
      "v_exp_f32 v124, v124\n"
      "v_exp_f32 v125, v125\n"
      "v_exp_f32 v126, v126\n"
      "v_exp_f32 v127, v127\n"
      "s_setprio 1\n"
      "s_waitcnt lgkmcnt(1)\n"
      "v_mfma_f32_32x32x16_bf16 v[96:111], v[128:131], %[q0], 0\n"
      "v_exp_f32 v64, v64\n"
      "v_exp_f32 v65, v65\n"
      "v_exp_f32 v66, v66\n"
      "v_smfmac_f32_32x32x32_bf16 %[o1], v[136:139], v[120:127], %[ix]\n"
      "ds_read_b128 v[136:139], %[va] offset:39552\n"
      "v_exp_f32 v67, v67\n"
      "v_exp_f32 v68, v68\n"
      "v_exp_f32 v69, v69\n"
      "v_mfma_f32_32x32x16_bf16 v[112:127], v[128:131], %[q1], 0\n"
      "ds_read_b128 v[128:131], %[ka] offset:43008\n"
      "v_exp_f32 v70, v70\n"
      "v_exp_f32 v71, v71\n"
      "v_exp_f32 v72, v72\n"
      "v_exp_f32 v73, v73\n"
      "s_waitcnt lgkmcnt(2)\n"
      "v_smfmac_f32_32x32x32_bf16 %[o0], v[132:135], v[64:71], %[ix]\n"
      "v_exp_f32 v74, v74\n"
      "v_exp_f32 v75, v75\n"
      "v_exp_f32 v76, v76\n"
      "v_exp_f32 v77, v77\n"
      "v_exp_f32 v78, v78\n"
      "v_mfma_f32_32x32x4_2b_bf16 v[96:127], v[140:141], %[qc], v[96:127]\n"
      "v_exp_f32 v79, v79\n"
      "v_exp_f32 v80, v80\n"
      "v_exp_f32 v81, v81\n"
      "v_exp_f32 v82, v82\n"
      "v_exp_f32 v83, v83\n"
      "v_exp_f32 v84, v84\n"
      "v_exp_f32 v85, v85\n"
      "v_exp_f32 v86, v86\n"
      "s_waitcnt lgkmcnt(1)\n"
      "v_smfmac_f32_32x32x32_bf16 %[o0], v[136:139], v[72:79], %[ix]\n"
      "v_exp_f32 v87, v87\n"
      "v_exp_f32 v88, v88\n"
      "v_exp_f32 v89, v89\n"
      "v_exp_f32 v90, v90\n"
      "v_smfmac_f32_32x32x32_bf16 %[o1], v[132:135], v[80:87], %[ix]\n"
      "ds_read_b128 v[132:135], %[va] offset:41472\n"
      "v_exp_f32 v91, v91\n"
      "v_exp_f32 v92, v92\n"
      "v_exp_f32 v93, v93\n"
      "v_exp_f32 v94, v94\n"
      "v_exp_f32 v95, v95\n"
      "s_setprio 0\n"
      "s_waitcnt lgkmcnt(1)\n"
      "v_mfma_f32_32x32x16_bf16 v[64:79], v[128:131], %[q0], 0\n"
      "v_exp_f32 v96, v96\n"
      "v_exp_f32 v97, v97\n"
      "v_exp_f32 v98, v98\n"
      "v_smfmac_f32_32x32x32_bf16 %[o1], v[136:139], v[88:95], %[ix]\n"
      "ds_read_b128 v[136:139], %[va] offset:42112\n"
      "v_exp_f32 v99, v99\n"
      "v_exp_f32 v100, v100\n"
      "v_exp_f32 v101, v101\n"
      "v_mfma_f32_32x32x16_bf16 v[80:95], v[128:131], %[q1], 0\n"
      "ds_read_b128 v[128:131], %[ka] offset:45056\n"
      "v_exp_f32 v102, v102\n"
      "v_exp_f32 v103, v103\n"
      "v_exp_f32 v104, v104\n"
      "v_exp_f32 v105, v105\n"
      "s_waitcnt lgkmcnt(2)\n"
      "v_smfmac_f32_32x32x32_bf16 %[o0], v[132:135], v[96:103], %[ix]\n"
      "v_exp_f32 v106, v106\n"
      "v_exp_f32 v107, v107\n"
      "v_exp_f32 v108, v108\n"
      "v_exp_f32 v109, v109\n"
      "v_exp_f32 v110, v110\n"
      "v_mfma_f32_32x32x4_2b_bf16 v[64:95], v[142:143], %[qa], v[64:95]\n"
      "v_exp_f32 v111, v111\n"
      "v_exp_f32 v112, v112\n"
      "v_exp_f32 v113, v113\n"
      "v_exp_f32 v114, v114\n"
      "v_exp_f32 v115, v115\n"
      "v_exp_f32 v116, v116\n"
      "v_exp_f32 v117, v117\n"
      "v_exp_f32 v118, v118\n"
      "s_waitcnt lgkmcnt(1)\n"
      "v_smfmac_f32_32x32x32_bf16 %[o0], v[136:139], v[104:111], %[ix]\n"
      "v_exp_f32 v119, v119\n"
      "v_exp_f32 v120, v120\n"
      "v_exp_f32 v121, v121\n"
      "v_exp_f32 v122, v122\n"
      "v_smfmac_f32_32x32x32_bf16 %[o1], v[132:135], v[112:119], %[ix]\n"
      "ds_read_b128 v[132:135], %[va] offset:44032\n"
      "v_exp_f32 v123, v123\n"
      "v_exp_f32 v124, v124\n"
      "v_exp_f32 v125, v125\n"
      "v_exp_f32 v126, v126\n"
      "v_exp_f32 v127, v127\n"
      "s_setprio 3\n"
      "s_waitcnt lgkmcnt(1)\n"
      "v_mfma_f32_32x32x16_bf16 v[96:111], v[128:131], %[q0], 0\n"
      "v_exp_f32 v64, v64\n"
      "v_exp_f32 v65, v65\n"
      "v_exp_f32 v66, v66\n"
      "v_smfmac_f32_32x32x32_bf16 %[o1], v[136:139], v[120:127], %[ix]\n"
      "ds_read_b128 v[136:139], %[va] offset:44672\n"
      "v_exp_f32 v67, v67\n"
      "v_exp_f32 v68, v68\n"
      "v_exp_f32 v69, v69\n"
      "v_mfma_f32_32x32x16_bf16 v[112:127], v[128:131], %[q1], 0\n"
      "s_barrier\n"
      "ds_read_b128 v[128:131], %[ka] offset:58368\n"
      "v_exp_f32 v70, v70\n"
      "v_exp_f32 v71, v71\n"
      "v_exp_f32 v72, v72\n"
      "v_exp_f32 v73, v73\n"
      "s_waitcnt lgkmcnt(2)\n"
      "v_smfmac_f32_32x32x32_bf16 %[o0], v[132:135], v[64:71], %[ix]\n"
      "v_exp_f32 v74, v74\n"
      "v_exp_f32 v75, v75\n"
      "v_exp_f32 v76, v76\n"
      "v_exp_f32 v77, v77\n"
      "v_exp_f32 v78, v78\n"
      "v_mfma_f32_32x32x4_2b_bf16 v[96:127], v[142:143], %[qc], v[96:127]\n"
      "ds_read_b128 v[140:143], %[ta] offset:58368\n"
      "v_exp_f32 v79, v79\n"
      "v_exp_f32 v80, v80\n"
      "v_exp_f32 v81, v81\n"
      "v_exp_f32 v82, v82\n"
      "v_exp_f32 v83, v83\n"
      "v_exp_f32 v84, v84\n"
      "v_exp_f32 v85, v85\n"
      "v_exp_f32 v86, v86\n"
      "s_waitcnt lgkmcnt(2)\n"
      "v_smfmac_f32_32x32x32_bf16 %[o0], v[136:139], v[72:79], %[ix]\n"
      "v_exp_f32 v87, v87\n"
      "v_exp_f32 v88, v88\n"
      "v_exp_f32 v89, v89\n"
      "v_exp_f32 v90, v90\n"
      "v_smfmac_f32_32x32x32_bf16 %[o1], v[132:135], v[80:87], %[ix]\n"
      "ds_read_b128 v[132:135], %[va] offset:46592\n"
      "v_exp_f32 v91, v91\n"
      "v_exp_f32 v92, v92\n"
      "v_exp_f32 v93, v93\n"
      "v_exp_f32 v94, v94\n"
      "v_exp_f32 v95, v95\n"
      "s_setprio 2\n"
      "s_waitcnt lgkmcnt(1)\n"
      "v_mfma_f32_32x32x16_bf16 v[64:79], v[128:131], %[q0], 0\n"
      "v_exp_f32 v96, v96\n"
      "v_exp_f32 v97, v97\n"
      "v_exp_f32 v98, v98\n"
      "v_smfmac_f32_32x32x32_bf16 %[o1], v[136:139], v[88:95], %[ix]\n"
      "ds_read_b128 v[136:139], %[va] offset:47232\n"
      "v_exp_f32 v99, v99\n"
      "v_exp_f32 v100, v100\n"
      "v_exp_f32 v101, v101\n"
      "v_mfma_f32_32x32x16_bf16 v[80:95], v[128:131], %[q1], 0\n"
      "ds_read_b128 v[128:131], %[ka] offset:60416\n"
      "v_exp_f32 v102, v102\n"
      "v_exp_f32 v103, v103\n"
      "v_exp_f32 v104, v104\n"
      "v_exp_f32 v105, v105\n"
      "s_waitcnt lgkmcnt(2)\n"
      "v_smfmac_f32_32x32x32_bf16 %[o0], v[132:135], v[96:103], %[ix]\n"
      "v_exp_f32 v106, v106\n"
      "v_exp_f32 v107, v107\n"
      "v_exp_f32 v108, v108\n"
      "v_exp_f32 v109, v109\n"
      "v_exp_f32 v110, v110\n"
      "v_mfma_f32_32x32x4_2b_bf16 v[64:95], v[140:141], %[qa], v[64:95]\n"
      "v_exp_f32 v111, v111\n"
      "v_exp_f32 v112, v112\n"
      "v_exp_f32 v113, v113\n"
      "v_exp_f32 v114, v114\n"
      "v_exp_f32 v115, v115\n"
      "v_exp_f32 v116, v116\n"
      "v_exp_f32 v117, v117\n"
      "v_exp_f32 v118, v118\n"
      "s_waitcnt lgkmcnt(1)\n"
      "v_smfmac_f32_32x32x32_bf16 %[o0], v[136:139], v[104:111], %[ix]\n"
      "v_exp_f32 v119, v119\n"
      "v_exp_f32 v120, v120\n"
      "v_exp_f32 v121, v121\n"
      "v_exp_f32 v122, v122\n"
      "v_smfmac_f32_32x32x32_bf16 %[o1], v[132:135], v[112:119], %[ix]\n"
      "ds_read_b128 v[132:135], %[va] offset:58368\n"
      "v_exp_f32 v123, v123\n"
      "v_exp_f32 v124, v124\n"
      "v_exp_f32 v125, v125\n"
      "v_exp_f32 v126, v126\n"
      "v_exp_f32 v127, v127\n"
      "v_add_u32 %[ka], 58368, %[ka]\n"
      "v_add_u32 %[va], 58368, %[va]\n"
      "v_add_u32 %[ta], 58368, %[ta]\n"
      "s_setprio 1\n"
      "s_waitcnt lgkmcnt(1)\n"
      "v_mfma_f32_32x32x16_bf16 v[96:111], v[128:131], %[q0], 0\n"
      "v_exp_f32 v64, v64\n"
      "v_exp_f32 v65, v65\n"
      "v_exp_f32 v66, v66\n"
      "v_smfmac_f32_32x32x32_bf16 %[o1], v[136:139], v[120:127], %[ix]\n"
      "ds_read_b128 v[136:139], %[va] offset:640\n"
      "v_exp_f32 v67, v67\n"
      "v_exp_f32 v68, v68\n"
      "v_exp_f32 v69, v69\n"
      "v_mfma_f32_32x32x16_bf16 v[112:127], v[128:131], %[q1], 0\n"
      "ds_read_b128 v[128:131], %[ka] offset:4096\n"
      "v_exp_f32 v70, v70\n"
      "v_exp_f32 v71, v71\n"
      "v_exp_f32 v72, v72\n"
      "v_exp_f32 v73, v73\n"
      "s_waitcnt lgkmcnt(2)\n"
      "v_smfmac_f32_32x32x32_bf16 %[o0], v[132:135], v[64:71], %[ix]\n"
      "v_exp_f32 v74, v74\n"
      "v_exp_f32 v75, v75\n"
      "v_exp_f32 v76, v76\n"
      "v_exp_f32 v77, v77\n"
      "v_exp_f32 v78, v78\n"
      "v_mfma_f32_32x32x4_2b_bf16 v[96:127], v[140:141], %[qc], v[96:127]\n"
      "v_exp_f32 v79, v79\n"
      "v_exp_f32 v80, v80\n"
      "v_exp_f32 v81, v81\n"
      "v_exp_f32 v82, v82\n"
      "v_exp_f32 v83, v83\n"
      "v_exp_f32 v84, v84\n"
      "v_exp_f32 v85, v85\n"
      "v_exp_f32 v86, v86\n"
      "s_waitcnt lgkmcnt(1)\n"
      "v_smfmac_f32_32x32x32_bf16 %[o0], v[136:139], v[72:79], %[ix]\n"
      "v_exp_f32 v87, v87\n"
      "v_exp_f32 v88, v88\n"
      "v_exp_f32 v89, v89\n"
      "v_exp_f32 v90, v90\n"
      "v_smfmac_f32_32x32x32_bf16 %[o1], v[132:135], v[80:87], %[ix]\n"
      "ds_read_b128 v[132:135], %[va] offset:2560\n"
      "v_exp_f32 v91, v91\n"
      "v_exp_f32 v92, v92\n"
      "v_exp_f32 v93, v93\n"
      "v_exp_f32 v94, v94\n"
      "v_exp_f32 v95, v95\n"
      "s_setprio 0\n"
      "s_waitcnt lgkmcnt(1)\n"
      "v_mfma_f32_32x32x16_bf16 v[64:79], v[128:131], %[q0], 0\n"
      "v_exp_f32 v96, v96\n"
      "v_exp_f32 v97, v97\n"
      "v_exp_f32 v98, v98\n"
      "v_smfmac_f32_32x32x32_bf16 %[o1], v[136:139], v[88:95], %[ix]\n"
      "ds_read_b128 v[136:139], %[va] offset:3200\n"
      "v_exp_f32 v99, v99\n"
      "v_exp_f32 v100, v100\n"
      "v_exp_f32 v101, v101\n"
      "v_mfma_f32_32x32x16_bf16 v[80:95], v[128:131], %[q1], 0\n"
      "ds_read_b128 v[128:131], %[ka] offset:6144\n"
      "v_exp_f32 v102, v102\n"
      "v_exp_f32 v103, v103\n"
      "v_exp_f32 v104, v104\n"
      "v_exp_f32 v105, v105\n"
      "s_waitcnt lgkmcnt(2)\n"
      "v_smfmac_f32_32x32x32_bf16 %[o0], v[132:135], v[96:103], %[ix]\n"
      "v_exp_f32 v106, v106\n"
      "v_exp_f32 v107, v107\n"
      "v_exp_f32 v108, v108\n"
      "v_exp_f32 v109, v109\n"
      "v_exp_f32 v110, v110\n"
      "v_mfma_f32_32x32x4_2b_bf16 v[64:95], v[142:143], %[qa], v[64:95]\n"
      "v_exp_f32 v111, v111\n"
      "v_exp_f32 v112, v112\n"
      "v_exp_f32 v113, v113\n"
      "v_exp_f32 v114, v114\n"
      "v_exp_f32 v115, v115\n"
      "v_exp_f32 v116, v116\n"
      "v_exp_f32 v117, v117\n"
      "v_exp_f32 v118, v118\n"
      "s_waitcnt lgkmcnt(1)\n"
      "v_smfmac_f32_32x32x32_bf16 %[o0], v[136:139], v[104:111], %[ix]\n"
      "v_exp_f32 v119, v119\n"
      "v_exp_f32 v120, v120\n"
      "v_exp_f32 v121, v121\n"
      "v_exp_f32 v122, v122\n"
      "v_smfmac_f32_32x32x32_bf16 %[o1], v[132:135], v[112:119], %[ix]\n"
      "ds_read_b128 v[132:135], %[va] offset:5120\n"
      "v_exp_f32 v123, v123\n"
      "v_exp_f32 v124, v124\n"
      "v_exp_f32 v125, v125\n"
      "v_exp_f32 v126, v126\n"
      "v_exp_f32 v127, v127\n"
      "s_setprio 3\n"
      "s_waitcnt lgkmcnt(1)\n"
      "v_mfma_f32_32x32x16_bf16 v[96:111], v[128:131], %[q0], 0\n"
      "v_exp_f32 v64, v64\n"
      "v_exp_f32 v65, v65\n"
      "v_exp_f32 v66, v66\n"
      "v_smfmac_f32_32x32x32_bf16 %[o1], v[136:139], v[120:127], %[ix]\n"
      "ds_read_b128 v[136:139], %[va] offset:5760\n"
      "v_exp_f32 v67, v67\n"
      "v_exp_f32 v68, v68\n"
      "v_exp_f32 v69, v69\n"
      "v_mfma_f32_32x32x16_bf16 v[112:127], v[128:131], %[q1], 0\n"
      "s_barrier\n"
      "ds_read_b128 v[128:131], %[ka] offset:19456\n"
      "v_exp_f32 v70, v70\n"
      "v_exp_f32 v71, v71\n"
      "v_exp_f32 v72, v72\n"
      "v_exp_f32 v73, v73\n"
      "s_waitcnt lgkmcnt(2)\n"
      "v_smfmac_f32_32x32x32_bf16 %[o0], v[132:135], v[64:71], %[ix]\n"
      "v_exp_f32 v74, v74\n"
      "v_exp_f32 v75, v75\n"
      "v_exp_f32 v76, v76\n"
      "v_exp_f32 v77, v77\n"
      "v_exp_f32 v78, v78\n"
      "v_mfma_f32_32x32x4_2b_bf16 v[96:127], v[142:143], %[qc], v[96:127]\n"
      "ds_read_b128 v[140:143], %[ta] offset:19456\n"
      "v_exp_f32 v79, v79\n"
      "v_exp_f32 v80, v80\n"
      "v_exp_f32 v81, v81\n"
      "v_exp_f32 v82, v82\n"
      "v_exp_f32 v83, v83\n"
      "v_exp_f32 v84, v84\n"
      "v_exp_f32 v85, v85\n"
      "v_exp_f32 v86, v86\n"
      "s_waitcnt lgkmcnt(2)\n"
      "v_smfmac_f32_32x32x32_bf16 %[o0], v[136:139], v[72:79], %[ix]\n"
      "v_exp_f32 v87, v87\n"
      "v_exp_f32 v88, v88\n"
      "v_exp_f32 v89, v89\n"
      "v_exp_f32 v90, v90\n"
      "v_smfmac_f32_32x32x32_bf16 %[o1], v[132:135], v[80:87], %[ix]\n"
      "ds_read_b128 v[132:135], %[va] offset:7680\n"
      "v_exp_f32 v91, v91\n"
      "v_exp_f32 v92, v92\n"
      "v_exp_f32 v93, v93\n"
      "v_exp_f32 v94, v94\n"
      "v_exp_f32 v95, v95\n"
      "s_setprio 2\n"
      "s_waitcnt lgkmcnt(1)\n"
      "v_mfma_f32_32x32x16_bf16 v[64:79], v[128:131], %[q0], 0\n"
      "v_exp_f32 v96, v96\n"
      "v_exp_f32 v97, v97\n"
      "v_exp_f32 v98, v98\n"
      "v_smfmac_f32_32x32x32_bf16 %[o1], v[136:139], v[88:95], %[ix]\n"
      "ds_read_b128 v[136:139], %[va] offset:8320\n"
      "v_exp_f32 v99, v99\n"
      "v_exp_f32 v100, v100\n"
      "v_exp_f32 v101, v101\n"
      "v_mfma_f32_32x32x16_bf16 v[80:95], v[128:131], %[q1], 0\n"
      "ds_read_b128 v[128:131], %[ka] offset:21504\n"
      "v_exp_f32 v102, v102\n"
      "v_exp_f32 v103, v103\n"
      "v_exp_f32 v104, v104\n"
      "v_exp_f32 v105, v105\n"
      "s_waitcnt lgkmcnt(2)\n"
      "v_smfmac_f32_32x32x32_bf16 %[o0], v[132:135], v[96:103], %[ix]\n"
      "v_exp_f32 v106, v106\n"
      "v_exp_f32 v107, v107\n"
      "v_exp_f32 v108, v108\n"
      "v_exp_f32 v109, v109\n"
      "v_exp_f32 v110, v110\n"
      "v_mfma_f32_32x32x4_2b_bf16 v[64:95], v[140:141], %[qa], v[64:95]\n"
      "v_exp_f32 v111, v111\n"
      "v_exp_f32 v112, v112\n"
      "v_exp_f32 v113, v113\n"
      "v_exp_f32 v114, v114\n"
      "v_exp_f32 v115, v115\n"
      "v_exp_f32 v116, v116\n"
      "v_exp_f32 v117, v117\n"
      "v_exp_f32 v118, v118\n"
      "s_waitcnt lgkmcnt(1)\n"
      "v_smfmac_f32_32x32x32_bf16 %[o0], v[136:139], v[104:111], %[ix]\n"
      "v_exp_f32 v119, v119\n"
      "v_exp_f32 v120, v120\n"
      "v_exp_f32 v121, v121\n"
      "v_exp_f32 v122, v122\n"
      "v_smfmac_f32_32x32x32_bf16 %[o1], v[132:135], v[112:119], %[ix]\n"
      "ds_read_b128 v[132:135], %[va] offset:19456\n"
      "v_exp_f32 v123, v123\n"
      "v_exp_f32 v124, v124\n"
      "v_exp_f32 v125, v125\n"
      "v_exp_f32 v126, v126\n"
      "v_exp_f32 v127, v127\n"
      "s_setprio 1\n"
      "s_waitcnt lgkmcnt(1)\n"
      "v_mfma_f32_32x32x16_bf16 v[96:111], v[128:131], %[q0], 0\n"
      "v_exp_f32 v64, v64\n"
      "v_exp_f32 v65, v65\n"
      "v_exp_f32 v66, v66\n"
      "v_smfmac_f32_32x32x32_bf16 %[o1], v[136:139], v[120:127], %[ix]\n"
      "ds_read_b128 v[136:139], %[va] offset:20096\n"
      "v_exp_f32 v67, v67\n"
      "v_exp_f32 v68, v68\n"
      "v_exp_f32 v69, v69\n"
      "v_mfma_f32_32x32x16_bf16 v[112:127], v[128:131], %[q1], 0\n"
      "ds_read_b128 v[128:131], %[ka] offset:23552\n"
      "v_exp_f32 v70, v70\n"
      "v_exp_f32 v71, v71\n"
      "v_exp_f32 v72, v72\n"
      "v_exp_f32 v73, v73\n"
      "s_waitcnt lgkmcnt(2)\n"
      "v_smfmac_f32_32x32x32_bf16 %[o0], v[132:135], v[64:71], %[ix]\n"
      "v_exp_f32 v74, v74\n"
      "v_exp_f32 v75, v75\n"
      "v_exp_f32 v76, v76\n"
      "v_exp_f32 v77, v77\n"
      "v_exp_f32 v78, v78\n"
      "v_mfma_f32_32x32x4_2b_bf16 v[96:127], v[140:141], %[qc], v[96:127]\n"
      "v_exp_f32 v79, v79\n"
      "v_exp_f32 v80, v80\n"
      "v_exp_f32 v81, v81\n"
      "v_exp_f32 v82, v82\n"
      "v_exp_f32 v83, v83\n"
      "v_exp_f32 v84, v84\n"
      "v_exp_f32 v85, v85\n"
      "v_exp_f32 v86, v86\n"
      "s_waitcnt lgkmcnt(1)\n"
      "v_smfmac_f32_32x32x32_bf16 %[o0], v[136:139], v[72:79], %[ix]\n"
      "v_exp_f32 v87, v87\n"
      "v_exp_f32 v88, v88\n"
      "v_exp_f32 v89, v89\n"
      "v_exp_f32 v90, v90\n"
      "v_smfmac_f32_32x32x32_bf16 %[o1], v[132:135], v[80:87], %[ix]\n"
      "ds_read_b128 v[132:135], %[va] offset:22016\n"
      "v_exp_f32 v91, v91\n"
      "v_exp_f32 v92, v92\n"
      "v_exp_f32 v93, v93\n"
      "v_exp_f32 v94, v94\n"
      "v_exp_f32 v95, v95\n"
      "s_setprio 0\n"
      "s_waitcnt lgkmcnt(1)\n"
      "v_mfma_f32_32x32x16_bf16 v[64:79], v[128:131], %[q0], 0\n"
      "v_exp_f32 v96, v96\n"
      "v_exp_f32 v97, v97\n"
      "v_exp_f32 v98, v98\n"
      "v_smfmac_f32_32x32x32_bf16 %[o1], v[136:139], v[88:95], %[ix]\n"
      "ds_read_b128 v[136:139], %[va] offset:22656\n"
      "v_exp_f32 v99, v99\n"
      "v_exp_f32 v100, v100\n"
      "v_exp_f32 v101, v101\n"
      "v_mfma_f32_32x32x16_bf16 v[80:95], v[128:131], %[q1], 0\n"
      "ds_read_b128 v[128:131], %[ka] offset:25600\n"
      "v_exp_f32 v102, v102\n"
      "v_exp_f32 v103, v103\n"
      "v_exp_f32 v104, v104\n"
      "v_exp_f32 v105, v105\n"
      "s_waitcnt lgkmcnt(2)\n"
      "v_smfmac_f32_32x32x32_bf16 %[o0], v[132:135], v[96:103], %[ix]\n"
      "v_exp_f32 v106, v106\n"
      "v_exp_f32 v107, v107\n"
      "v_exp_f32 v108, v108\n"
      "v_exp_f32 v109, v109\n"
      "v_exp_f32 v110, v110\n"
      "v_mfma_f32_32x32x4_2b_bf16 v[64:95], v[142:143], %[qa], v[64:95]\n"
      "v_exp_f32 v111, v111\n"
      "v_exp_f32 v112, v112\n"
      "v_exp_f32 v113, v113\n"
      "v_exp_f32 v114, v114\n"
      "v_exp_f32 v115, v115\n"
      "v_exp_f32 v116, v116\n"
      "v_exp_f32 v117, v117\n"
      "v_exp_f32 v118, v118\n"
      "s_waitcnt lgkmcnt(1)\n"
      "v_smfmac_f32_32x32x32_bf16 %[o0], v[136:139], v[104:111], %[ix]\n"
      "v_exp_f32 v119, v119\n"
      "v_exp_f32 v120, v120\n"
      "v_exp_f32 v121, v121\n"
      "v_exp_f32 v122, v122\n"
      "v_smfmac_f32_32x32x32_bf16 %[o1], v[132:135], v[112:119], %[ix]\n"
      "ds_read_b128 v[132:135], %[va] offset:24576\n"
      "v_exp_f32 v123, v123\n"
      "v_exp_f32 v124, v124\n"
      "v_exp_f32 v125, v125\n"
      "v_exp_f32 v126, v126\n"
      "v_exp_f32 v127, v127\n"
      "s_setprio 3\n"
      "s_waitcnt lgkmcnt(1)\n"
      "v_mfma_f32_32x32x16_bf16 v[96:111], v[128:131], %[q0], 0\n"
      "v_exp_f32 v64, v64\n"
      "v_exp_f32 v65, v65\n"
      "v_exp_f32 v66, v66\n"
      "v_smfmac_f32_32x32x32_bf16 %[o1], v[136:139], v[120:127], %[ix]\n"
      "ds_read_b128 v[136:139], %[va] offset:25216\n"
      "v_exp_f32 v67, v67\n"
      "v_exp_f32 v68, v68\n"
      "v_exp_f32 v69, v69\n"
      "v_mfma_f32_32x32x16_bf16 v[112:127], v[128:131], %[q1], 0\n"
      "s_barrier\n"
      "ds_read_b128 v[128:131], %[ka] offset:38912\n"
      "v_exp_f32 v70, v70\n"
      "v_exp_f32 v71, v71\n"
      "v_exp_f32 v72, v72\n"
      "v_exp_f32 v73, v73\n"
      "s_waitcnt lgkmcnt(2)\n"
      "v_smfmac_f32_32x32x32_bf16 %[o0], v[132:135], v[64:71], %[ix]\n"
      "v_exp_f32 v74, v74\n"
      "v_exp_f32 v75, v75\n"
      "v_exp_f32 v76, v76\n"
      "v_exp_f32 v77, v77\n"
      "v_exp_f32 v78, v78\n"
      "v_mfma_f32_32x32x4_2b_bf16 v[96:127], v[142:143], %[qc], v[96:127]\n"
      "ds_read_b128 v[140:143], %[ta] offset:38912\n"
      "v_exp_f32 v79, v79\n"
      "v_exp_f32 v80, v80\n"
      "v_exp_f32 v81, v81\n"
      "v_exp_f32 v82, v82\n"
      "v_exp_f32 v83, v83\n"
      "v_exp_f32 v84, v84\n"
      "v_exp_f32 v85, v85\n"
      "v_exp_f32 v86, v86\n"
      "s_waitcnt lgkmcnt(2)\n"
      "v_smfmac_f32_32x32x32_bf16 %[o0], v[136:139], v[72:79], %[ix]\n"
      "v_exp_f32 v87, v87\n"
      "v_exp_f32 v88, v88\n"
      "v_exp_f32 v89, v89\n"
      "v_exp_f32 v90, v90\n"
      "v_smfmac_f32_32x32x32_bf16 %[o1], v[132:135], v[80:87], %[ix]\n"
      "ds_read_b128 v[132:135], %[va] offset:27136\n"
      "v_exp_f32 v91, v91\n"
      "v_exp_f32 v92, v92\n"
      "v_exp_f32 v93, v93\n"
      "v_exp_f32 v94, v94\n"
      "v_exp_f32 v95, v95\n"
      "s_setprio 2\n"
      "s_waitcnt lgkmcnt(1)\n"
      "v_mfma_f32_32x32x16_bf16 v[64:79], v[128:131], %[q0], 0\n"
      "v_exp_f32 v96, v96\n"
      "v_exp_f32 v97, v97\n"
      "v_exp_f32 v98, v98\n"
      "v_smfmac_f32_32x32x32_bf16 %[o1], v[136:139], v[88:95], %[ix]\n"
      "ds_read_b128 v[136:139], %[va] offset:27776\n"
      "v_exp_f32 v99, v99\n"
      "v_exp_f32 v100, v100\n"
      "v_exp_f32 v101, v101\n"
      "v_mfma_f32_32x32x16_bf16 v[80:95], v[128:131], %[q1], 0\n"
      "ds_read_b128 v[128:131], %[ka] offset:40960\n"
      "v_exp_f32 v102, v102\n"
      "v_exp_f32 v103, v103\n"
      "v_exp_f32 v104, v104\n"
      "v_exp_f32 v105, v105\n"
      "s_waitcnt lgkmcnt(2)\n"
      "v_smfmac_f32_32x32x32_bf16 %[o0], v[132:135], v[96:103], %[ix]\n"
      "v_exp_f32 v106, v106\n"
      "v_exp_f32 v107, v107\n"
      "v_exp_f32 v108, v108\n"
      "v_exp_f32 v109, v109\n"
      "v_exp_f32 v110, v110\n"
      "v_mfma_f32_32x32x4_2b_bf16 v[64:95], v[140:141], %[qa], v[64:95]\n"
      "v_exp_f32 v111, v111\n"
      "v_exp_f32 v112, v112\n"
      "v_exp_f32 v113, v113\n"
      "v_exp_f32 v114, v114\n"
      "v_exp_f32 v115, v115\n"
      "v_exp_f32 v116, v116\n"
      "v_exp_f32 v117, v117\n"
      "v_exp_f32 v118, v118\n"
      "s_waitcnt lgkmcnt(1)\n"
      "v_smfmac_f32_32x32x32_bf16 %[o0], v[136:139], v[104:111], %[ix]\n"
      "v_exp_f32 v119, v119\n"
      "v_exp_f32 v120, v120\n"
      "v_exp_f32 v121, v121\n"
      "v_exp_f32 v122, v122\n"
      "v_smfmac_f32_32x32x32_bf16 %[o1], v[132:135], v[112:119], %[ix]\n"
      "ds_read_b128 v[132:135], %[va] offset:38912\n"
      "v_exp_f32 v123, v123\n"
      "v_exp_f32 v124, v124\n"
      "v_exp_f32 v125, v125\n"
      "v_exp_f32 v126, v126\n"
      "v_exp_f32 v127, v127\n"
      "s_setprio 1\n"
      "s_waitcnt lgkmcnt(1)\n"
      "v_mfma_f32_32x32x16_bf16 v[96:111], v[128:131], %[q0], 0\n"
      "v_exp_f32 v64, v64\n"
      "v_exp_f32 v65, v65\n"
      "v_exp_f32 v66, v66\n"
      "v_smfmac_f32_32x32x32_bf16 %[o1], v[136:139], v[120:127], %[ix]\n"
      "ds_read_b128 v[136:139], %[va] offset:39552\n"
      "v_exp_f32 v67, v67\n"
      "v_exp_f32 v68, v68\n"
      "v_exp_f32 v69, v69\n"
      "v_mfma_f32_32x32x16_bf16 v[112:127], v[128:131], %[q1], 0\n"
      "ds_read_b128 v[128:131], %[ka] offset:43008\n"
      "v_exp_f32 v70, v70\n"
      "v_exp_f32 v71, v71\n"
      "v_exp_f32 v72, v72\n"
      "v_exp_f32 v73, v73\n"
      "s_waitcnt lgkmcnt(2)\n"
      "v_smfmac_f32_32x32x32_bf16 %[o0], v[132:135], v[64:71], %[ix]\n"
      "v_exp_f32 v74, v74\n"
      "v_exp_f32 v75, v75\n"
      "v_exp_f32 v76, v76\n"
      "v_exp_f32 v77, v77\n"
      "v_exp_f32 v78, v78\n"
      "v_mfma_f32_32x32x4_2b_bf16 v[96:127], v[140:141], %[qc], v[96:127]\n"
      "v_exp_f32 v79, v79\n"
      "v_exp_f32 v80, v80\n"
      "v_exp_f32 v81, v81\n"
      "v_exp_f32 v82, v82\n"
      "v_exp_f32 v83, v83\n"
      "v_exp_f32 v84, v84\n"
      "v_exp_f32 v85, v85\n"
      "v_exp_f32 v86, v86\n"
      "s_waitcnt lgkmcnt(1)\n"
      "v_smfmac_f32_32x32x32_bf16 %[o0], v[136:139], v[72:79], %[ix]\n"
      "v_exp_f32 v87, v87\n"
      "v_exp_f32 v88, v88\n"
      "v_exp_f32 v89, v89\n"
      "v_exp_f32 v90, v90\n"
      "v_smfmac_f32_32x32x32_bf16 %[o1], v[132:135], v[80:87], %[ix]\n"
      "ds_read_b128 v[132:135], %[va] offset:41472\n"
      "v_exp_f32 v91, v91\n"
      "v_exp_f32 v92, v92\n"
      "v_exp_f32 v93, v93\n"
      "v_exp_f32 v94, v94\n"
      "v_exp_f32 v95, v95\n"
      "s_setprio 0\n"
      "s_waitcnt lgkmcnt(1)\n"
      "v_mfma_f32_32x32x16_bf16 v[64:79], v[128:131], %[q0], 0\n"
      "v_exp_f32 v96, v96\n"
      "v_exp_f32 v97, v97\n"
      "v_exp_f32 v98, v98\n"
      "v_smfmac_f32_32x32x32_bf16 %[o1], v[136:139], v[88:95], %[ix]\n"
      "ds_read_b128 v[136:139], %[va] offset:42112\n"
      "v_exp_f32 v99, v99\n"
      "v_exp_f32 v100, v100\n"
      "v_exp_f32 v101, v101\n"
      "v_mfma_f32_32x32x16_bf16 v[80:95], v[128:131], %[q1], 0\n"
      "ds_read_b128 v[128:131], %[ka] offset:45056\n"
      "v_exp_f32 v102, v102\n"
      "v_exp_f32 v103, v103\n"
      "v_exp_f32 v104, v104\n"
      "v_exp_f32 v105, v105\n"
      "s_waitcnt lgkmcnt(2)\n"
      "v_smfmac_f32_32x32x32_bf16 %[o0], v[132:135], v[96:103], %[ix]\n"
      "v_exp_f32 v106, v106\n"
      "v_exp_f32 v107, v107\n"
      "v_exp_f32 v108, v108\n"
      "v_exp_f32 v109, v109\n"
      "v_exp_f32 v110, v110\n"
      "v_mfma_f32_32x32x4_2b_bf16 v[64:95], v[142:143], %[qa], v[64:95]\n"
      "v_exp_f32 v111, v111\n"
      "v_exp_f32 v112, v112\n"
      "v_exp_f32 v113, v113\n"
      "v_exp_f32 v114, v114\n"
      "v_exp_f32 v115, v115\n"
      "v_exp_f32 v116, v116\n"
      "v_exp_f32 v117, v117\n"
      "v_exp_f32 v118, v118\n"
      "s_waitcnt lgkmcnt(1)\n"
      "v_smfmac_f32_32x32x32_bf16 %[o0], v[136:139], v[104:111], %[ix]\n"
      "v_exp_f32 v119, v119\n"
      "v_exp_f32 v120, v120\n"
      "v_exp_f32 v121, v121\n"
      "v_exp_f32 v122, v122\n"
      "v_smfmac_f32_32x32x32_bf16 %[o1], v[132:135], v[112:119], %[ix]\n"
      "ds_read_b128 v[132:135], %[va] offset:44032\n"
      "v_exp_f32 v123, v123\n"
      "v_exp_f32 v124, v124\n"
      "v_exp_f32 v125, v125\n"
      "v_exp_f32 v126, v126\n"
      "v_exp_f32 v127, v127\n"
      "s_setprio 3\n"
      "s_waitcnt lgkmcnt(1)\n"
      "v_mfma_f32_32x32x16_bf16 v[96:111], v[128:131], %[q0], 0\n"
      "v_exp_f32 v64, v64\n"
      "v_exp_f32 v65, v65\n"
      "v_exp_f32 v66, v66\n"
      "v_smfmac_f32_32x32x32_bf16 %[o1], v[136:139], v[120:127], %[ix]\n"
      "ds_read_b128 v[136:139], %[va] offset:44672\n"
      "v_exp_f32 v67, v67\n"
      "v_exp_f32 v68, v68\n"
      "v_exp_f32 v69, v69\n"
      "v_mfma_f32_32x32x16_bf16 v[112:127], v[128:131], %[q1], 0\n"
      "s_barrier\n"
      "ds_read_b128 v[128:131], %[ka] offset:58368\n"
      "v_exp_f32 v70, v70\n"
      "v_exp_f32 v71, v71\n"
      "v_exp_f32 v72, v72\n"
      "v_exp_f32 v73, v73\n"
      "s_waitcnt lgkmcnt(2)\n"
      "v_smfmac_f32_32x32x32_bf16 %[o0], v[132:135], v[64:71], %[ix]\n"
      "v_exp_f32 v74, v74\n"
      "v_exp_f32 v75, v75\n"
      "v_exp_f32 v76, v76\n"
      "v_exp_f32 v77, v77\n"
      "v_exp_f32 v78, v78\n"
      "v_mfma_f32_32x32x4_2b_bf16 v[96:127], v[142:143], %[qc], v[96:127]\n"
      "ds_read_b128 v[140:143], %[ta] offset:58368\n"
      "v_exp_f32 v79, v79\n"
      "v_exp_f32 v80, v80\n"
      "v_exp_f32 v81, v81\n"
      "v_exp_f32 v82, v82\n"
      "v_exp_f32 v83, v83\n"
      "v_exp_f32 v84, v84\n"
      "v_exp_f32 v85, v85\n"
      "v_exp_f32 v86, v86\n"
      "s_waitcnt lgkmcnt(2)\n"
      "v_smfmac_f32_32x32x32_bf16 %[o0], v[136:139], v[72:79], %[ix]\n"
      "v_exp_f32 v87, v87\n"
      "v_exp_f32 v88, v88\n"
      "v_exp_f32 v89, v89\n"
      "v_exp_f32 v90, v90\n"
      "v_smfmac_f32_32x32x32_bf16 %[o1], v[132:135], v[80:87], %[ix]\n"
      "ds_read_b128 v[132:135], %[va] offset:46592\n"
      "v_exp_f32 v91, v91\n"
      "v_exp_f32 v92, v92\n"
      "v_exp_f32 v93, v93\n"
      "v_exp_f32 v94, v94\n"
      "v_exp_f32 v95, v95\n"
      "s_setprio 2\n"
      "s_waitcnt lgkmcnt(1)\n"
      "v_mfma_f32_32x32x16_bf16 v[64:79], v[128:131], %[q0], 0\n"
      "v_exp_f32 v96, v96\n"
      "v_exp_f32 v97, v97\n"
      "v_exp_f32 v98, v98\n"
      "v_smfmac_f32_32x32x32_bf16 %[o1], v[136:139], v[88:95], %[ix]\n"
      "ds_read_b128 v[136:139], %[va] offset:47232\n"
      "v_exp_f32 v99, v99\n"
      "v_exp_f32 v100, v100\n"
      "v_exp_f32 v101, v101\n"
      "v_mfma_f32_32x32x16_bf16 v[80:95], v[128:131], %[q1], 0\n"
      "ds_read_b128 v[128:131], %[ka] offset:60416\n"
      "v_exp_f32 v102, v102\n"
      "v_exp_f32 v103, v103\n"
      "v_exp_f32 v104, v104\n"
      "v_exp_f32 v105, v105\n"
      "s_waitcnt lgkmcnt(2)\n"
      "v_smfmac_f32_32x32x32_bf16 %[o0], v[132:135], v[96:103], %[ix]\n"
      "v_exp_f32 v106, v106\n"
      "v_exp_f32 v107, v107\n"
      "v_exp_f32 v108, v108\n"
      "v_exp_f32 v109, v109\n"
      "v_exp_f32 v110, v110\n"
      "v_mfma_f32_32x32x4_2b_bf16 v[64:95], v[140:141], %[qa], v[64:95]\n"
      "v_exp_f32 v111, v111\n"
      "v_exp_f32 v112, v112\n"
      "v_exp_f32 v113, v113\n"
      "v_exp_f32 v114, v114\n"
      "v_exp_f32 v115, v115\n"
      "v_exp_f32 v116, v116\n"
      "v_exp_f32 v117, v117\n"
      "v_exp_f32 v118, v118\n"
      "s_waitcnt lgkmcnt(1)\n"
      "v_smfmac_f32_32x32x32_bf16 %[o0], v[136:139], v[104:111], %[ix]\n"
      "v_exp_f32 v119, v119\n"
      "v_exp_f32 v120, v120\n"
      "v_exp_f32 v121, v121\n"
      "v_exp_f32 v122, v122\n"
      "v_smfmac_f32_32x32x32_bf16 %[o1], v[132:135], v[112:119], %[ix]\n"
      "ds_read_b128 v[132:135], %[va] offset:58368\n"
      "v_exp_f32 v123, v123\n"
      "v_exp_f32 v124, v124\n"
      "v_exp_f32 v125, v125\n"
      "v_exp_f32 v126, v126\n"
      "v_exp_f32 v127, v127\n"
      "v_add_u32 %[ka], 58368, %[ka]\n"
      "v_add_u32 %[va], 58368, %[va]\n"
      "v_add_u32 %[ta], 58368, %[ta]\n"
      "s_setprio 1\n"
      "s_waitcnt lgkmcnt(1)\n"
      "v_mfma_f32_32x32x16_bf16 v[96:111], v[128:131], %[q0], 0\n"
      "v_exp_f32 v64, v64\n"
      "v_exp_f32 v65, v65\n"
      "v_exp_f32 v66, v66\n"
      "v_smfmac_f32_32x32x32_bf16 %[o1], v[136:139], v[120:127], %[ix]\n"
      "ds_read_b128 v[136:139], %[va] offset:640\n"
      "v_exp_f32 v67, v67\n"
      "v_exp_f32 v68, v68\n"
      "v_exp_f32 v69, v69\n"
      "v_mfma_f32_32x32x16_bf16 v[112:127], v[128:131], %[q1], 0\n"
      "ds_read_b128 v[128:131], %[ka] offset:4096\n"
      "v_exp_f32 v70, v70\n"
      "v_exp_f32 v71, v71\n"
      "v_exp_f32 v72, v72\n"
      "v_exp_f32 v73, v73\n"
      "s_waitcnt lgkmcnt(2)\n"
      "v_smfmac_f32_32x32x32_bf16 %[o0], v[132:135], v[64:71], %[ix]\n"
      "v_exp_f32 v74, v74\n"
      "v_exp_f32 v75, v75\n"
      "v_exp_f32 v76, v76\n"
      "v_exp_f32 v77, v77\n"
      "v_exp_f32 v78, v78\n"
      "v_mfma_f32_32x32x4_2b_bf16 v[96:127], v[140:141], %[qc], v[96:127]\n"
      "v_exp_f32 v79, v79\n"
      "v_exp_f32 v80, v80\n"
      "v_exp_f32 v81, v81\n"
      "v_exp_f32 v82, v82\n"
      "v_exp_f32 v83, v83\n"
      "v_exp_f32 v84, v84\n"
      "v_exp_f32 v85, v85\n"
      "v_exp_f32 v86, v86\n"
      "s_waitcnt lgkmcnt(1)\n"
      "v_smfmac_f32_32x32x32_bf16 %[o0], v[136:139], v[72:79], %[ix]\n"
      "v_exp_f32 v87, v87\n"
      "v_exp_f32 v88, v88\n"
      "v_exp_f32 v89, v89\n"
      "v_exp_f32 v90, v90\n"
      "v_smfmac_f32_32x32x32_bf16 %[o1], v[132:135], v[80:87], %[ix]\n"
      "ds_read_b128 v[132:135], %[va] offset:2560\n"
      "v_exp_f32 v91, v91\n"
      "v_exp_f32 v92, v92\n"
      "v_exp_f32 v93, v93\n"
      "v_exp_f32 v94, v94\n"
      "v_exp_f32 v95, v95\n"
      "s_setprio 0\n"
      "s_waitcnt lgkmcnt(1)\n"
      "v_mfma_f32_32x32x16_bf16 v[64:79], v[128:131], %[q0], 0\n"
      "v_exp_f32 v96, v96\n"
      "v_exp_f32 v97, v97\n"
      "v_exp_f32 v98, v98\n"
      "v_smfmac_f32_32x32x32_bf16 %[o1], v[136:139], v[88:95], %[ix]\n"
      "ds_read_b128 v[136:139], %[va] offset:3200\n"
      "v_exp_f32 v99, v99\n"
      "v_exp_f32 v100, v100\n"
      "v_exp_f32 v101, v101\n"
      "v_mfma_f32_32x32x16_bf16 v[80:95], v[128:131], %[q1], 0\n"
      "ds_read_b128 v[128:131], %[ka] offset:6144\n"
      "v_exp_f32 v102, v102\n"
      "v_exp_f32 v103, v103\n"
      "v_exp_f32 v104, v104\n"
      "v_exp_f32 v105, v105\n"
      "s_waitcnt lgkmcnt(2)\n"
      "v_smfmac_f32_32x32x32_bf16 %[o0], v[132:135], v[96:103], %[ix]\n"
      "v_exp_f32 v106, v106\n"
      "v_exp_f32 v107, v107\n"
      "v_exp_f32 v108, v108\n"
      "v_exp_f32 v109, v109\n"
      "v_exp_f32 v110, v110\n"
      "v_mfma_f32_32x32x4_2b_bf16 v[64:95], v[142:143], %[qa], v[64:95]\n"
      "v_exp_f32 v111, v111\n"
      "v_exp_f32 v112, v112\n"
      "v_exp_f32 v113, v113\n"
      "v_exp_f32 v114, v114\n"
      "v_exp_f32 v115, v115\n"
      "v_exp_f32 v116, v116\n"
      "v_exp_f32 v117, v117\n"
      "v_exp_f32 v118, v118\n"
      "s_waitcnt lgkmcnt(1)\n"
      "v_smfmac_f32_32x32x32_bf16 %[o0], v[136:139], v[104:111], %[ix]\n"
      "v_exp_f32 v119, v119\n"
      "v_exp_f32 v120, v120\n"
      "v_exp_f32 v121, v121\n"
      "v_exp_f32 v122, v122\n"
      "v_smfmac_f32_32x32x32_bf16 %[o1], v[132:135], v[112:119], %[ix]\n"
      "ds_read_b128 v[132:135], %[va] offset:5120\n"
      "v_exp_f32 v123, v123\n"
      "v_exp_f32 v124, v124\n"
      "v_exp_f32 v125, v125\n"
      "v_exp_f32 v126, v126\n"
      "v_exp_f32 v127, v127\n"
      "s_setprio 3\n"
      "s_waitcnt lgkmcnt(1)\n"
      "v_mfma_f32_32x32x16_bf16 v[96:111], v[128:131], %[q0], 0\n"
      "v_exp_f32 v64, v64\n"
      "v_exp_f32 v65, v65\n"
      "v_exp_f32 v66, v66\n"
      "v_smfmac_f32_32x32x32_bf16 %[o1], v[136:139], v[120:127], %[ix]\n"
      "ds_read_b128 v[136:139], %[va] offset:5760\n"
      "v_exp_f32 v67, v67\n"
      "v_exp_f32 v68, v68\n"
      "v_exp_f32 v69, v69\n"
      "v_mfma_f32_32x32x16_bf16 v[112:127], v[128:131], %[q1], 0\n"
      "s_barrier\n"
      "ds_read_b128 v[128:131], %[ka] offset:19456\n"
      "v_exp_f32 v70, v70\n"
      "v_exp_f32 v71, v71\n"
      "v_exp_f32 v72, v72\n"
      "v_exp_f32 v73, v73\n"
      "s_waitcnt lgkmcnt(2)\n"
      "v_smfmac_f32_32x32x32_bf16 %[o0], v[132:135], v[64:71], %[ix]\n"
      "v_exp_f32 v74, v74\n"
      "v_exp_f32 v75, v75\n"
      "v_exp_f32 v76, v76\n"
      "v_exp_f32 v77, v77\n"
      "v_exp_f32 v78, v78\n"
      "v_mfma_f32_32x32x4_2b_bf16 v[96:127], v[142:143], %[qc], v[96:127]\n"
      "ds_read_b128 v[140:143], %[ta] offset:19456\n"
      "v_exp_f32 v79, v79\n"
      "v_exp_f32 v80, v80\n"
      "v_exp_f32 v81, v81\n"
      "v_exp_f32 v82, v82\n"
      "v_exp_f32 v83, v83\n"
      "v_exp_f32 v84, v84\n"
      "v_exp_f32 v85, v85\n"
      "v_exp_f32 v86, v86\n"
      "s_waitcnt lgkmcnt(2)\n"
      "v_smfmac_f32_32x32x32_bf16 %[o0], v[136:139], v[72:79], %[ix]\n"
      "v_exp_f32 v87, v87\n"
      "v_exp_f32 v88, v88\n"
      "v_exp_f32 v89, v89\n"
      "v_exp_f32 v90, v90\n"
      "v_smfmac_f32_32x32x32_bf16 %[o1], v[132:135], v[80:87], %[ix]\n"
      "ds_read_b128 v[132:135], %[va] offset:7680\n"
      "v_exp_f32 v91, v91\n"
      "v_exp_f32 v92, v92\n"
      "v_exp_f32 v93, v93\n"
      "v_exp_f32 v94, v94\n"
      "v_exp_f32 v95, v95\n"
      "s_setprio 2\n"
      "s_waitcnt lgkmcnt(1)\n"
      "v_mfma_f32_32x32x16_bf16 v[64:79], v[128:131], %[q0], 0\n"
      "v_exp_f32 v96, v96\n"
      "v_exp_f32 v97, v97\n"
      "v_exp_f32 v98, v98\n"
      "v_smfmac_f32_32x32x32_bf16 %[o1], v[136:139], v[88:95], %[ix]\n"
      "ds_read_b128 v[136:139], %[va] offset:8320\n"
      "v_exp_f32 v99, v99\n"
      "v_exp_f32 v100, v100\n"
      "v_exp_f32 v101, v101\n"
      "v_mfma_f32_32x32x16_bf16 v[80:95], v[128:131], %[q1], 0\n"
      "ds_read_b128 v[128:131], %[ka] offset:21504\n"
      "v_exp_f32 v102, v102\n"
      "v_exp_f32 v103, v103\n"
      "v_exp_f32 v104, v104\n"
      "v_exp_f32 v105, v105\n"
      "s_waitcnt lgkmcnt(2)\n"
      "v_smfmac_f32_32x32x32_bf16 %[o0], v[132:135], v[96:103], %[ix]\n"
      "v_exp_f32 v106, v106\n"
      "v_exp_f32 v107, v107\n"
      "v_exp_f32 v108, v108\n"
      "v_exp_f32 v109, v109\n"
      "v_exp_f32 v110, v110\n"
      "v_mfma_f32_32x32x4_2b_bf16 v[64:95], v[140:141], %[qa], v[64:95]\n"
      "v_exp_f32 v111, v111\n"
      "v_exp_f32 v112, v112\n"
      "v_exp_f32 v113, v113\n"
      "v_exp_f32 v114, v114\n"
      "v_exp_f32 v115, v115\n"
      "v_exp_f32 v116, v116\n"
      "v_exp_f32 v117, v117\n"
      "v_exp_f32 v118, v118\n"
      "s_waitcnt lgkmcnt(1)\n"
      "v_smfmac_f32_32x32x32_bf16 %[o0], v[136:139], v[104:111], %[ix]\n"
      "v_exp_f32 v119, v119\n"
      "v_exp_f32 v120, v120\n"
      "v_exp_f32 v121, v121\n"
      "v_exp_f32 v122, v122\n"
      "v_smfmac_f32_32x32x32_bf16 %[o1], v[132:135], v[112:119], %[ix]\n"
      "ds_read_b128 v[132:135], %[va] offset:19456\n"
      "v_exp_f32 v123, v123\n"
      "v_exp_f32 v124, v124\n"
      "v_exp_f32 v125, v125\n"
      "v_exp_f32 v126, v126\n"
      "v_exp_f32 v127, v127\n"
      "s_setprio 1\n"
      "s_waitcnt lgkmcnt(1)\n"
      "v_mfma_f32_32x32x16_bf16 v[96:111], v[128:131], %[q0], 0\n"
      "v_exp_f32 v64, v64\n"
      "v_exp_f32 v65, v65\n"
      "v_exp_f32 v66, v66\n"
      "v_smfmac_f32_32x32x32_bf16 %[o1], v[136:139], v[120:127], %[ix]\n"
      "ds_read_b128 v[136:139], %[va] offset:20096\n"
      "v_exp_f32 v67, v67\n"
      "v_exp_f32 v68, v68\n"
      "v_exp_f32 v69, v69\n"
      "v_mfma_f32_32x32x16_bf16 v[112:127], v[128:131], %[q1], 0\n"
      "ds_read_b128 v[128:131], %[ka] offset:23552\n"
      "v_exp_f32 v70, v70\n"
      "v_exp_f32 v71, v71\n"
      "v_exp_f32 v72, v72\n"
      "v_exp_f32 v73, v73\n"
      "s_waitcnt lgkmcnt(2)\n"
      "v_smfmac_f32_32x32x32_bf16 %[o0], v[132:135], v[64:71], %[ix]\n"
      "v_exp_f32 v74, v74\n"
      "v_exp_f32 v75, v75\n"
      "v_exp_f32 v76, v76\n"
      "v_exp_f32 v77, v77\n"
      "v_exp_f32 v78, v78\n"
      "v_mfma_f32_32x32x4_2b_bf16 v[96:127], v[140:141], %[qc], v[96:127]\n"
      "v_exp_f32 v79, v79\n"
      "v_exp_f32 v80, v80\n"
      "v_exp_f32 v81, v81\n"
      "v_exp_f32 v82, v82\n"
      "v_exp_f32 v83, v83\n"
      "v_exp_f32 v84, v84\n"
      "v_exp_f32 v85, v85\n"
      "v_exp_f32 v86, v86\n"
      "s_waitcnt lgkmcnt(1)\n"
      "v_smfmac_f32_32x32x32_bf16 %[o0], v[136:139], v[72:79], %[ix]\n"
      "v_exp_f32 v87, v87\n"
      "v_exp_f32 v88, v88\n"
      "v_exp_f32 v89, v89\n"
      "v_exp_f32 v90, v90\n"
      "v_smfmac_f32_32x32x32_bf16 %[o1], v[132:135], v[80:87], %[ix]\n"
      "ds_read_b128 v[132:135], %[va] offset:22016\n"
      "v_exp_f32 v91, v91\n"
      "v_exp_f32 v92, v92\n"
      "v_exp_f32 v93, v93\n"
      "v_exp_f32 v94, v94\n"
      "v_exp_f32 v95, v95\n"
      "s_setprio 0\n"
      "s_waitcnt lgkmcnt(1)\n"
      "v_mfma_f32_32x32x16_bf16 v[64:79], v[128:131], %[q0], 0\n"
      "v_exp_f32 v96, v96\n"
      "v_exp_f32 v97, v97\n"
      "v_exp_f32 v98, v98\n"
      "v_smfmac_f32_32x32x32_bf16 %[o1], v[136:139], v[88:95], %[ix]\n"
      "ds_read_b128 v[136:139], %[va] offset:22656\n"
      "v_exp_f32 v99, v99\n"
      "v_exp_f32 v100, v100\n"
      "v_exp_f32 v101, v101\n"
      "v_mfma_f32_32x32x16_bf16 v[80:95], v[128:131], %[q1], 0\n"
      "ds_read_b128 v[128:131], %[ka] offset:25600\n"
      "v_exp_f32 v102, v102\n"
      "v_exp_f32 v103, v103\n"
      "v_exp_f32 v104, v104\n"
      "v_exp_f32 v105, v105\n"
      "s_waitcnt lgkmcnt(2)\n"
      "v_smfmac_f32_32x32x32_bf16 %[o0], v[132:135], v[96:103], %[ix]\n"
      "v_exp_f32 v106, v106\n"
      "v_exp_f32 v107, v107\n"
      "v_exp_f32 v108, v108\n"
      "v_exp_f32 v109, v109\n"
      "v_exp_f32 v110, v110\n"
      "v_mfma_f32_32x32x4_2b_bf16 v[64:95], v[142:143], %[qa], v[64:95]\n"
      "v_exp_f32 v111, v111\n"
      "v_exp_f32 v112, v112\n"
      "v_exp_f32 v113, v113\n"
      "v_exp_f32 v114, v114\n"
      "v_exp_f32 v115, v115\n"
      "v_exp_f32 v116, v116\n"
      "v_exp_f32 v117, v117\n"
      "v_exp_f32 v118, v118\n"
      "s_waitcnt lgkmcnt(1)\n"
      "v_smfmac_f32_32x32x32_bf16 %[o0], v[136:139], v[104:111], %[ix]\n"
      "v_exp_f32 v119, v119\n"
      "v_exp_f32 v120, v120\n"
      "v_exp_f32 v121, v121\n"
      "v_exp_f32 v122, v122\n"
      "v_smfmac_f32_32x32x32_bf16 %[o1], v[132:135], v[112:119], %[ix]\n"
      "ds_read_b128 v[132:135], %[va] offset:24576\n"
      "v_exp_f32 v123, v123\n"
      "v_exp_f32 v124, v124\n"
      "v_exp_f32 v125, v125\n"
      "v_exp_f32 v126, v126\n"
      "v_exp_f32 v127, v127\n"
      "s_setprio 3\n"
      "s_waitcnt lgkmcnt(1)\n"
      "v_mfma_f32_32x32x16_bf16 v[96:111], v[128:131], %[q0], 0\n"
      "v_exp_f32 v64, v64\n"
      "v_exp_f32 v65, v65\n"
      "v_exp_f32 v66, v66\n"
      "v_smfmac_f32_32x32x32_bf16 %[o1], v[136:139], v[120:127], %[ix]\n"
      "ds_read_b128 v[136:139], %[va] offset:25216\n"
      "v_exp_f32 v67, v67\n"
      "v_exp_f32 v68, v68\n"
      "v_exp_f32 v69, v69\n"
      "v_mfma_f32_32x32x16_bf16 v[112:127], v[128:131], %[q1], 0\n"
      "s_barrier\n"
      "v_exp_f32 v70, v70\n"
      "v_exp_f32 v71, v71\n"
      "v_exp_f32 v72, v72\n"
      "v_exp_f32 v73, v73\n"
      "s_waitcnt lgkmcnt(1)\n"
      "v_smfmac_f32_32x32x32_bf16 %[o0], v[132:135], v[64:71], %[ix]\n"
      "v_exp_f32 v74, v74\n"
      "v_exp_f32 v75, v75\n"
      "v_exp_f32 v76, v76\n"
      "v_exp_f32 v77, v77\n"
      "v_exp_f32 v78, v78\n"
      "v_mfma_f32_32x32x4_2b_bf16 v[96:127], v[142:143], %[qc], v[96:127]\n"
      "v_exp_f32 v79, v79\n"
      "v_exp_f32 v80, v80\n"
      "v_exp_f32 v81, v81\n"
      "v_exp_f32 v82, v82\n"
      "v_exp_f32 v83, v83\n"
      "v_exp_f32 v84, v84\n"
      "v_exp_f32 v85, v85\n"
      "v_exp_f32 v86, v86\n"
      "s_waitcnt lgkmcnt(0)\n"
      "v_smfmac_f32_32x32x32_bf16 %[o0], v[136:139], v[72:79], %[ix]\n"
      "v_exp_f32 v87, v87\n"
      "v_exp_f32 v88, v88\n"
      "v_exp_f32 v89, v89\n"
      "v_exp_f32 v90, v90\n"
      "v_smfmac_f32_32x32x32_bf16 %[o1], v[132:135], v[80:87], %[ix]\n"
      "ds_read_b128 v[132:135], %[va] offset:27136\n"
      "v_exp_f32 v91, v91\n"
      "v_exp_f32 v92, v92\n"
      "v_exp_f32 v93, v93\n"
      "v_exp_f32 v94, v94\n"
      "v_exp_f32 v95, v95\n"
      "s_setprio 2\n"
      "v_exp_f32 v96, v96\n"
      "v_exp_f32 v97, v97\n"
      "v_exp_f32 v98, v98\n"
      "v_smfmac_f32_32x32x32_bf16 %[o1], v[136:139], v[88:95], %[ix]\n"
      "ds_read_b128 v[136:139], %[va] offset:27776\n"
      "v_exp_f32 v99, v99\n"
      "v_exp_f32 v100, v100\n"
      "v_exp_f32 v101, v101\n"
      "v_exp_f32 v102, v102\n"
      "v_exp_f32 v103, v103\n"
      "v_exp_f32 v104, v104\n"
      "v_exp_f32 v105, v105\n"
      "s_waitcnt lgkmcnt(1)\n"
      "v_smfmac_f32_32x32x32_bf16 %[o0], v[132:135], v[96:103], %[ix]\n"
      "v_exp_f32 v106, v106\n"
      "v_exp_f32 v107, v107\n"
      "v_exp_f32 v108, v108\n"
      "v_exp_f32 v109, v109\n"
      "v_exp_f32 v110, v110\n"
      "v_exp_f32 v111, v111\n"
      "v_exp_f32 v112, v112\n"
      "v_exp_f32 v113, v113\n"
      "v_exp_f32 v114, v114\n"
      "v_exp_f32 v115, v115\n"
      "v_exp_f32 v116, v116\n"
      "v_exp_f32 v117, v117\n"
      "v_exp_f32 v118, v118\n"
      "s_waitcnt lgkmcnt(0)\n"
      "v_smfmac_f32_32x32x32_bf16 %[o0], v[136:139], v[104:111], %[ix]\n"
      "v_exp_f32 v119, v119\n"
      "v_exp_f32 v120, v120\n"
      "v_exp_f32 v121, v121\n"
      "v_exp_f32 v122, v122\n"
      "v_smfmac_f32_32x32x32_bf16 %[o1], v[132:135], v[112:119], %[ix]\n"
      "v_exp_f32 v123, v123\n"
      "v_exp_f32 v124, v124\n"
      "v_exp_f32 v125, v125\n"
      "v_exp_f32 v126, v126\n"
      "v_exp_f32 v127, v127\n"
      "s_nop 1\n"
      "v_smfmac_f32_32x32x32_bf16 %[o1], v[136:139], v[120:127], %[ix]\n"
      "s_setprio 0\n"
      "s_waitcnt lgkmcnt(0)\n"
      "s_nop 15\n"
          : [o0] "+v"(o0), [o1] "+v"(o1), [ka] "+v"(ka), [va] "+v"(va), [ta] "+v"(ta), [it] "+s"(it)
          : [q0] "v"(q0), [q1] "v"(q1), [qa] "v"(qa), [qc] "v"(qc), [ix] "v"(0xdddddddd)
          : "v64", "v65", "v66", "v67", "v68", "v69", "v70", "v71", "v72", "v73", "v74", "v75", "v76", "v77", "v78", "v79", "v80", "v81", "v82", "v83", "v84", "v85", "v86", "v87", "v88", "v89", "v90", "v91", "v92", "v93", "v94", "v95", "v96", "v97", "v98", "v99", "v100", "v101", "v102", "v103", "v104", "v105", "v106", "v107", "v108", "v109", "v110", "v111", "v112", "v113", "v114", "v115", "v116", "v117", "v118", "v119", "v120", "v121", "v122", "v123", "v124", "v125", "v126", "v127", "v128", "v129", "v130", "v131", "v132", "v133", "v134", "v135", "v136", "v137", "v138", "v139", "v140", "v141", "v142", "v143", "scc", "memory");
    }
    const lptr xo = lds + C_OX + lane * 16;
    auto put_half = [&]() {
#pragma unroll
      for (int i = 0; i < 3; ++i) {
        *(LAS f32x4*)(xo + (wid * 3 + i) * 1024) = (f32x4){o1[4 * i], o1[4 * i + 1], o1[4 * i + 2], o1[4 * i + 3]};
      }
    };
    put_half();
    BAR();
    f32x4 pp[3];
#pragma unroll
    for (int i = 0; i < 3; ++i) pp[i] = *(LAS const f32x4*)(xo + ((wid ^ 4) * 3 + i) * 1024);
    if (__builtin_expect(any_bad(), 0)) {
      BAR(); BAR();
      const bf16x8 qf0 = __builtin_bit_cast(bf16x8, q0), qf1 = __builtin_bit_cast(bf16x8, q1);
      float m0 = -INFINITY, m1 = -INFINITY;
#pragma unroll 1
      for (int i = 0; i < 32; ++i) {
        const int off = (i >> 2) * CH_BYTES;
        const f32x32 s = qk_tile((lptr)(uintptr_t)(ka0 + off + (i & 3) * 2048), qf0, qf1, *(LAS const unsigned*)(uintptr_t)(ta0 + off + (i & 3) * 4), qa);
#pragma unroll
        for (int j = 0; j < 16; ++j) { m0 = fmaxf(m0, s[j]); m1 = fmaxf(m1, s[16 + j]); }
      }
      m0 = fmaxf(m0, __shfl_xor(m0, 32)); m1 = fmaxf(m1, __shfl_xor(m1, 32));
      *(LAS f32x2*)(lds + SPARE + tid * 8) = (f32x2){m0, m1};
      BAR();
      const f32x2 mo = *(LAS const f32x2*)(lds + SPARE + (tid ^ 256) * 8);
      m0 = fmaxf(m0, mo[1]); m1 = fmaxf(m1, mo[0]);
#pragma unroll
      for (int j = 0; j < 16; ++j) { o0[j] = 0.f; o1[j] = 0.f; }
#pragma unroll 1
      for (int i = 0; i < 32; ++i) {
        const int off = (i >> 2) * CH_BYTES;
        f32x32 s = qk_tile((lptr)(uintptr_t)(ka0 + off + (i & 3) * 2048), qf0, qf1, *(LAS const unsigned*)(uintptr_t)(ta0 + off + (i & 3) * 4), qa);
#pragma unroll
        for (int j = 0; j < 16; ++j) { s[j] = __builtin_amdgcn_exp2f(s[j] - m0); s[16 + j] = __builtin_amdgcn_exp2f(s[16 + j] - m1); }
        const bf16x8 v0 = *(LAS const bf16x8*)(uintptr_t)(va0 + off + (i & 3) * 8 * V_OCT), v1 = *(LAS const bf16x8*)(uintptr_t)(va0 + off + ((i & 3) * 8 + 2) * V_OCT);
        o0 = SMF(v0, sub8(s, 0), o0); o0 = SMF(v1, sub8(s, 8), o0); o1 = SMF(v0, sub8(s, 16), o1); o1 = SMF(v1, sub8(s, 24), o1);
      }
      BAR();
      put_half();
      BAR();
#pragma unroll
      for (int i = 0; i < 3; ++i) pp[i] = *(LAS const f32x4*)(xo + ((wid ^ 4) * 3 + i) * 1024);
    }
    float f[12];
#pragma unroll
    for (int i = 0; i < 12; ++i) f[i] = o0[i] + pp[i >> 2][i & 3];
    const auto sw = __builtin_amdgcn_permlane32_swap(__float_as_uint(f[10]), __float_as_uint(f[10]), false, false);
    const float inv = __builtin_amdgcn_rcpf(__uint_as_float(sw[0]));
    const char* ob = ws + WS_O + (qb * 4 + kp * 2) * 18432 + hg * 2304;
    const u32x4 ov = {nosent(pk2(f[0] * inv, f[1] * inv)), nosent(pk2(f[2] * inv, f[3] * inv)), nosent(pk2(f[4] * inv, f[5] * inv)), nosent(pk2(f[6] * inv, f[7] * inv))};
    asm volatile("global_store_dwordx4 %0, %1, %2 sc1\n\ts_nop 1" ::"v"(oo), "v"(ov), "s"(ob + head * 512) : "memory");
    const unsigned ot = nosent(pk2(f[8] * inv, f[9] * inv));
    if (h == 0) asm volatile("global_store_dword %0, %1, %2 offset:2048 sc1" ::"v"(oo), "v"(ot), "s"(ob + head * 4) : "memory");
  } else {
    const int sv = wid - 8, chl = sv >> 1, half = sv & 1, rot = b & 7;
    const unsigned l16 = lane * 16;
    unsigned vo[9];
    const char* wsrc = (const char*)Wq + hg * 144 - 4096;
    if (sv == 3) {
      slice_offsets(vo, lane);
#pragma unroll
      for (int a = 0; a < 3; ++a) glds9(vo, wsrc + a * 64 * ROWB, lds0 + A_W + a * 9216);
      touchs(1024 + (lane & 3) * (2u << 20), ws, lds0 + A_W - 256);
    }
    const int rr = lane >> 3;
    unsigned rem0, rem1, rem2, wva, wvt, f2o, yrd, yre, oom, ooe;
    {
      auto remoff = [&](int a) { const int O = 8 * a + rr, gq = O / 9, o = O - gq * 9, e8 = lane & 7;
                                 return (unsigned)((gq * 72 + (o < 8 ? 18 * (o >> 1) + 8 * (o & 1) + e8 : 18 * (e8 >> 1) + 16 + (e8 & 1))) * ROWB); };
      rem0 = remoff(sv); rem1 = remoff(sv + 4); rem2 = remoff(8);
      const unsigned wsw = ((lane & 7) ^ (2 * ((rr >> 1) & 1))) * 16;
      wva = rr * ROWB + wsw; wvt = (18 * (rr >> 1) + 16 + (rr & 1)) * ROWB + wsw;
      const int pb0 = 32 * hg + r32;
      f2o = (h ? qb * 8 + (lane & 7) : ((pb0 & 7) * 4 + (pb0 >> 6)) * 8 + ((pb0 >> 3) & 7)) * 128;
      yrd = lds0 + C_YT + sv * 2304 + (((4 * g + p4) & 7) * 8 + q4) * 32 + (g >> 1) * 8; yre = lds0 + C_YT + sv * 2304 + 2048 + q4 * 32 + p4 * 8;
      oom = (((lane & 3) + h * 4) * OUTLD + ((lane >> 2) & 7) * 8) * 2; ooe = ((lane & 3) + q4 * 4) * OUTLD * 2;
      PIN(rem0); PIN(rem1); PIN(rem2); PIN(wva); PIN(wvt); PIN(f2o); PIN(yrd); PIN(yre); PIN(oom); PIN(ooe);
    }
    if (sv == 3) { VMWAIT(1); LSET(4, 3u); } else VMWAIT(0);
    const unsigned tag = __builtin_amdgcn_readfirstlane(TAG());
    const float dsk = bf2f(((LAS const u16*)(lds + msc + 16))[kch & 7]);
#define KVC(s0) do { kv_group(ws, lds0, (s0) + chl, rot, 2 * half, l16); kv_group(ws, lds0, (s0) + chl, rot, 2 * half + 1, l16);            \
                     if (half) kv_group(ws, lds0, (s0) + chl, rot, 4, l16); } while (0)
    const char* fp0 = ws + WS_F + (((rot + 2 * sv + 2 + h) & 7) * 32 + r32) * 128;
    u32x4 fl0;
    unsigned qn2 = 0;
    if (sv < 3) {
      __builtin_amdgcn_s_setprio(3);
      kg_in in; kg_read(in);
      f32x4 d[2][2];
      const lptr tb = lds + A_SCR + 2 * sv * KG_TAB;
      kgen_half(in, tb, lane, d);
      VMWAIT(0);
      lds_wait(&ctrl[16 + sv], 1u);
#pragma unroll
      for (int i = 0; i < 4; ++i) d[i >> 1][i & 1] += *(LAS const f32x4*)(tb + KG_TAB + i * 1024 + lane * 16);
      if (lane == 0) d[0][0][0] += dsk;
      kgen_compact(d, tb, lane);
      lds_wait(&ctrl[5], 1u);
      {
        const char* fup = ws + WS_F + (r32 * 8 + (sv == 2 ? 7 : b / 36)) * 128;
        u32x4 fu;
        asm volatile("global_load_dwordx4 %0, %2, off sc1\n\tglobal_load_dwordx4 %1, %3, off sc1\n\ts_waitcnt vmcnt(0)" : "=&v"(fu), "=&v"(fl0) : "v"(fup), "v"(fp0) : "memory");
        flag_wait<2>(fup, tag, fu);
        lds_wait(&ctrl[5], 8u);
        const char* ur = ws + WS_UT + (size_t)kch * UT_ROW + 128;
        const int upad = sv == 2 ? UE_PADB : U_PADB, u1 = sv == 2 ? CV_UE1 : CV_U1;
        glds4c(l16, ur - 2, lds0 + CVB + CV_U + sv * CV_UCH + upad); glds4c(l16, ur, lds0 + CVB + CV_U + sv * CV_UCH + u1 + upad);
      }
      kgen_expand(tb, lds + CVB + sv * R_BYTES, lane);
      flag_wait<0>(fp0, tag, fl0);
      if (sv < 2) { if (lane < 56) *(LAS u32x4*)(lds + CVB + CV_U + sv * CV_UCH + (lane < 28 ? lane * 16 : CV_U1 + (lane - 28) * 16)) = (u32x4){0u, 0u, 0u, 0u}; }
      else if (lane < 62) {
#pragma unroll
        for (int i = 0; i < 4; ++i) *(LAS u32x4*)(lds + CVB + CV_UE + (i >> 1) * CV_UE1 + (i & 1) * 992 + l16) = (u32x4){0u, 0u, 0u, 0u};
      }
      __builtin_amdgcn_s_setprio(3);
    } else {
      __builtin_amdgcn_s_setprio(3);
#pragma unroll
      for (int a = 3; a < 9; ++a) {
        if (a & 1) lds_wait(&ctrl[12 + (a >> 1)], 2u);
        glds9(vo, wsrc + a * 64 * ROWB, lds0 + A_W + a * 9216);
      }
      VMWAIT(36); LSET(4, 5u);
      VMWAIT(18); LSET(4, 7u);
      VMWAIT(9); LSET(4, 8u);
      VMWAIT(0); LSET(4, 9u);
      const unsigned tg = tag;
      asm volatile("global_load_dwordx4 %0, %1, off sc1\n\ts_waitcnt vmcnt(0)" : "=&v"(fl0) : "v"(fp0) : "memory");
#pragma unroll 1
      for (int spin = 0; spin < SPIN_MAX; ++spin) {
        if (__all(h == 1 || fl0[0] == tg)) break;
        __builtin_amdgcn_s_sleep(POLL0);
        if (h == 0 && fl0[0] != tg) asm volatile("global_load_dwordx4 %0, %1, off sc1\n\ts_waitcnt vmcnt(0)" : "+v"(fl0) : "v"(fp0) : "memory");
      }
      lds_wait(&ctrl[12], 2u);
#pragma unroll
      for (int i = 0; i < 5; ++i) kv_group(ws, lds0, 0, rot, i, l16);
      VMWAIT(0);
      LSET(7, 1u);
      flag_wait<0>(fp0, tg, fl0);
#pragma unroll
      for (int i = 0; i < 4; ++i) kv_group(ws, lds0, 1, rot, i, l16);
      lds_wait(&ctrl[5], 8u);
      {
#define DOT2(x, c) __builtin_amdgcn_fdot2_f32_bf16(__builtin_bit_cast(bf16x2, x), __builtin_bit_cast(bf16x2, x), (c), false)
        u32x4 qr[9];
#pragma unroll
        for (int i = 0; i < 9; ++i) qr[i] = *(LAS const u32x4*)(lds + A_Q + lane * Q_ROW + i * 16);
        CFENCE();
        lds_wait(&ctrl[6], 8u);
        kv_group(ws, lds0, 1, rot, 4, l16);
        float n[4] = {0.f, 0.f, 0.f, 0.f};
#pragma unroll
        for (int i = 0; i < 36; ++i) n[i / 9] = DOT2(qr[i >> 2][i & 3], n[i / 9]);
        qn2 = umax(umax(__float_as_uint(n[0]) & 0x7fffffffu, __float_as_uint(n[1]) & 0x7fffffffu), umax(__float_as_uint(n[2]) & 0x7fffffffu, __float_as_uint(n[3]) & 0x7fffffffu));
        PIN(qn2);
      }
    }
    {
      unsigned m = fl0[1];
      m = umax(m, dppu<0x128>(m));
      { const auto t = __builtin_amdgcn_permlane16_swap(m, m, false, false); m = umax(t[0], t[1]); }
      { const auto t = __builtin_amdgcn_permlane32_swap(m, m, false, false); m = umax(t[0], t[1]); }
      if (lane < 8) bctl[-32 + sv * 8 + lane] = m;
    }
    const char* wo = (const char*)Wo + hg * 128;
    auto wo_group = [&](int gq) {
      const unsigned va = wva ^ ((gq & 1) * 64), vb = (va ^ 64) + 8 * ROWB - 1024, vt = wvt ^ ((gq & 1) * 64);
      const char* sb = wo + gq * 72 * ROWB;
      unsigned sv_;
      asm volatile("s_mov_b32 %0, m0\n\ts_add_u32 m0, %8, 0x2000\n\ts_nop 0\n\tglobal_load_lds_dwordx4 %3, %4\n\t"
                   "s_mov_b32 m0, %8\n\ts_nop 0\n\tglobal_load_lds_dwordx4 %1, %4\n\tglobal_load_lds_dwordx4 %2, %4 offset:1024\n\t"
                   "s_add_u32 m0, m0, 0x800\n\ts_nop 0\n\tglobal_load_lds_dwordx4 %1, %5\n\tglobal_load_lds_dwordx4 %2, %5 offset:1024\n\t"
                   "s_add_u32 m0, m0, 0x800\n\ts_nop 0\n\tglobal_load_lds_dwordx4 %1, %6\n\tglobal_load_lds_dwordx4 %2, %6 offset:1024\n\t"
                   "s_add_u32 m0, m0, 0x800\n\ts_nop 0\n\tglobal_load_lds_dwordx4 %1, %7\n\tglobal_load_lds_dwordx4 %2, %7 offset:1024\n\ts_mov_b32 m0, %0"
                   : "=&s"(sv_) : "v"(va), "v"(vb), "v"(vt), "s"(sb), "s"(sb + 18 * ROWB), "s"(sb + 36 * ROWB), "s"(sb + 54 * ROWB), "s"(lds0 + gq * 9216)
                   : "memory", "scc");
    };
    auto wo_rem = [&]() {
      const char* wr = (const char*)Wo + 1024 + hg * 16;
      gldss(rem0, wr, lds0 + C_REM + sv * 1024); gldss(rem1, wr, lds0 + C_REM + 4096 + sv * 1024);
      if (sv == 0) gldss(rem2, wr, lds0 + C_REM + 8192);
    };
    const int rofs = CVB + (1023 - l15 + 4 * g) * 4, uofs = CVB + CV_U + (l15 & 1) * CV_U1 + U_PADB + (32 * (l15 >> 1) + 8 * g) * 2;
    f32x4 acc[8], ea = {0.f, 0.f, 0.f, 0.f}, eb = ea;
#pragma unroll
    for (int c = 0; c < 8; ++c) acc[c] = ea;
    {
      const lptr rb = lds + chl * R_BYTES + rofs - 3584 - 192 - half * 256, ub = lds + chl * CV_UCH + uofs - 192 - half * 256;
      const lptr rbe = lds + 2 * R_BYTES + rofs - 3584 - 64 - sv * 128;
      const lptr ube = lds + CVB + CV_UE + (l15 & 1) * CV_UE1 + UE_PADB + (32 * (l15 >> 1) + 8 * g) * 2 - 64 - sv * 128 - cq * 512, uae = ube + (2 * cq - 3) * 512;
      bf16x8 uf[8], kf[8];
#define LDK(base, z, d) do { LAS const unsigned* rp = (LAS const unsigned*)((base) + 512 * (7 - (z)) + 64 * (d)); kf[z] = __builtin_bit_cast(bf16x8, (u32x4){rp[0], rp[1], rp[2], rp[3]}); } while (0)
#define LDU(base, z, d) uf[z] = *(LAS const bf16x8*)((base) + (z) * 512 + 64 * (d))
#pragma unroll
      for (int iv = 0; iv < 4; ++iv) {
        if (iv == 0) { BARVM(0); } else asm volatile("s_waitcnt vmcnt(0)\n\ts_barrier" ::: "memory");
        if (iv == 0) {
#pragma unroll
          for (int z = 0; z < 8; ++z) { LDK(rb, z, 3); LDU(ub, 7 - z, 3); }
        }
        if (iv < 2) { KVC(2 + 2 * iv); }
        if (iv == 0) touchs(lane * 80, (const char*)Wo + ((b >> 3) * 4 + sv) * 5184, lds0 + B_CTRL - 448);
        if (iv == 1 && sv == 3) {
          const int l7 = lane & 7;
          const unsigned m = umax(umax(bctl[-32 + l7], bctl[-24 + l7]), umax(bctl[-16 + l7], bctl[-8 + l7]));
          const float k2 = __uint_as_float(__builtin_amdgcn_readlane(m, 0)) + __uint_as_float(__builtin_amdgcn_readlane(m, 1)) +
                           __uint_as_float(__builtin_amdgcn_readlane(m, 2)) + __uint_as_float(__builtin_amdgcn_readlane(m, 3));
          const float vf = __uint_as_float(umax(umax(__builtin_amdgcn_readlane(m, 4), __builtin_amdgcn_readlane(m, 5)), umax(__builtin_amdgcn_readlane(m, 6), __builtin_amdgcn_readlane(m, 7))));
          const bool bad = __any(!(__uint_as_float(qn2) * k2 <= 8000.f)) || !(vf <= 1048576.f && vf >= 9.5367431640625e-7f);
          if (lane < 16) bctl[lane] = (lane == 8 && bad) ? 1u : 0u;
        }
        if (iv == 3 && sv < 3) wo_group(3 + sv);
        if (iv == 2) {
          const char* p = ws + WS_O + (qb * 4 + sv) * 18432 + 9 * hg * 256;
          const u32x4 sv4 = {SENT, SENT, SENT, SENT};
          asm volatile("global_store_dwordx4 %0, %1, %2 sc1\n\tglobal_store_dwordx4 %0, %1, %2 offset:1024 sc1\n\t"
                       "global_store_dwordx4 %0, %1, %2 offset:1280 sc1\n\ts_nop 1" ::"v"(l16), "v"(sv4), "s"(p) : "memory");
        }
        const bool ex = iv & 1;
#pragma unroll
        for (int m1 = 0; m1 < 8; ++m1) {
          __builtin_amdgcn_sched_barrier(0);
#pragma unroll
          for (int c = 7; c >= m1; --c) {
            acc[c] = MFMA16(uf[c - m1], kf[m1], acc[c]);
          }
          __builtin_amdgcn_sched_barrier(0);
          if (ex) { LDK(rbe, m1, 1 - (iv >> 1)); LDU(ube, 7 - m1, 1 - (iv >> 1)); }
          else { LDK(rb, m1, 2 - iv); LDU(ub, 7 - m1, 2 - iv); }
        }
        if (ex) {
          bf16x8 ua[4];
#pragma unroll
          for (int i = 0; i < 4; ++i) ua[i] = *(LAS const bf16x8*)(uae + (3 - i) * 512 + 64 * (1 - (iv >> 1)));
#pragma unroll
          for (int m1 = 0; m1 < 8; ++m1) {
            __builtin_amdgcn_sched_barrier(0);
            eb = MFMA16(uf[7 - m1], kf[m1], eb);
            if (m1 >= 4) ea = MFMA16(ua[m1 - 4], kf[m1 - 4], ea);
            __builtin_amdgcn_sched_barrier(0);
            if (iv == 1) { LDU(ub, 7 - m1, 1); if (m1 >= 4) { LDK(rb, m1 - 4, 1); LDK(rb, m1, 1); } }
          }
        }
      }
    }
    const int ec = chl ? 7 - cq : cq;
    const lptr sc = lds + CV_SCR + l16;
    if (half) {
#pragma unroll
      for (int i = 0; i < 4; ++i) { *(LAS f32x4*)(sc + (sv * 4 + i) * 1024) = acc[i]; acc[i] = acc[4 + i]; }
    } else {
#pragma unroll
      for (int i = 0; i < 4; ++i) *(LAS f32x4*)(sc + (sv * 4 + i) * 1024) = acc[4 + i];
    }
    if (sv != 0) *(LAS f32x4*)(sc + 16384 + (sv - 1) * 1024) = ea;
    if (sv != 2) *(LAS f32x4*)(sc + 19456 + (sv > 2 ? 2 : sv) * 1024) = eb;
    BAR();
    KVC(6);
    {
      const unsigned yo = g * 128 + l15 * 4;
      const char* yb = ws + WS_YT + (2 * b + chl) * 4096 + half * 2048;
#define YST(base, off, v) asm volatile("global_store_dword %0, %1, %2 offset:" #off " sc1" ::"v"(yo), "v"(v), "s"(base) : "memory")
      f32x4 e = chl ? eb : ea;
#pragma unroll
      for (int i = 0; i < 4; ++i) acc[i] += *(LAS const f32x4*)(sc + ((sv ^ 1) * 4 + i) * 1024);
#pragma unroll
      for (int s = 0; s < 3; ++s) e += *(LAS const f32x4*)(sc + 16384 + chl * 3072 + s * 1024);
#define YTILE(i, o0, o1) do { YST(yb, o0, pk2(acc[i][0], acc[i][1])); YST(yb, o1, pk2(acc[i][2], acc[i][3])); } while (0)
      YTILE(0, 0, 64); YTILE(1, 512, 576); YTILE(2, 1024, 1088); YTILE(3, 1536, 1600);
      if (half == 0) {
        const char* ye = ws + WS_YT + ce * 4096 + ec * 512;
        YST(ye, 0, pk2(e[0], e[1])); YST(ye, 64, pk2(e[2], e[3]));
      }
    }
    BARVM(0);
    if (half) wo_group(6 + chl);
    if (wid == 8 && lane == 0) st4c(myflag + 12, tag);
    if (sv) wo_group(sv - 1);
    wo_rem();
    const char* f2a = ws + WS_F + f2o;
    const char* f2b = ws + WS_F + (sv * 64 + lane) * 128;
    BAR();
    u32x4 fa, fb = {tag, tag, tag, tag};
    asm volatile("global_load_dwordx4 %0, %1, off sc1\n\ts_waitcnt vmcnt(0)" : "=&v"(fa) : "v"(f2a) : "memory");
    if (__builtin_expect(b == 0, 0)) asm volatile("global_load_dwordx4 %0, %1, off sc1\n\ts_waitcnt vmcnt(0)" : "+v"(fb) : "v"(f2b) : "memory");
    flag_wait<3>(f2a, tag, fa); flag_wait<3>(f2b, tag, fb);
    {
      const char* yp = ws + WS_YT + hg * 64 * 4096 + qb * 128 + sv * 32;
      const unsigned yv = (lane >> 1) * 4096 + (lane & 1) * 16;
      gldssc(yv, yp, lds0 + C_YT + sv * 2304); gldssc(yv, yp + 32 * 4096, lds0 + C_YT + sv * 2304 + 1024);
      if (lane < 16) gldssc(yv, yp + (512 - hg * 56) * 4096, lds0 + C_YT + sv * 2304 + 2048);
    }
    BAR();
    VMWAIT(0);
    {
      const lptr rp = (lptr)(uintptr_t)yrd, re = (lptr)(uintptr_t)yre;
      const char* ob = (const char*)(out + (size_t)(qb * 64 + sv * 16) * OUTLD);
      const u32x4 v0 = __builtin_bit_cast(u32x4, trd2(rp, rp + 128)), v1 = __builtin_bit_cast(u32x4, trd2(rp + 16, rp + 144));
      const u32x4 ve = __builtin_bit_cast(u32x4, trd2(re, re + 128));
#define OST16S(vo, v, base) asm volatile("global_store_dwordx4 %0, %1, %2\n\ts_nop 1" ::"v"(vo), "v"(v), "s"(base) : "memory")
      OST16S(oom, v0, ob + hg * 128); OST16S(oom, v1, ob + hg * 128 + 8 * OUTLD * 2);
      if (lane < 16) OST16S(ooe, ve, ob + 1024 + hg * 16);
    }
    if (b == 0 && wid == 8 && lane == 0) st4c(ws + WS_E, tag);
    __builtin_amdgcn_s_setprio(0);
    BAR();
    if (__builtin_expect(any_bad(), 0)) { BAR(); _Pragma("unroll 1") for (int i = 0; i < 8; i += 2) KVC(i); BARVM(0); BAR(); BAR(); wo_group(sv); wo_group(sv + 4); wo_rem(); BARVM(0); }
  }
  {
    FRESH_LANE();
    const int kt = wid >> 2;
    bf16x8 wf[6][4];
    {
      const int sw = 2 * (((q4 >> 1) & 1) | ((g & 1) << 1));
      const lptr wb = lds + kt * 24576 + (g * 8 + q4) * 128 + (p4 & 1) * 8;
#pragma unroll
      for (int nt = 0; nt < 4; ++nt) {
        const lptr p = wb + (((2 * nt + (p4 >> 1)) ^ sw) * 16);
#pragma unroll
        for (int kk = 0; kk < 6; ++kk) wf[kk][nt] = trd2(p + kk * 4096, p + kk * 4096 + 512);
      }
    }
    bf16x8 wfr[6];
    {
      const lptr wr = lds + C_REM + kt * 3072 + (g * 8 + q4) * 16 + (p4 & 1) * 8;
#pragma unroll
      for (int kk = 0; kk < 6; ++kk) wfr[kk] = trd2(wr + kk * 512, wr + kk * 512 + 64);
    }
    const lptr rd = lds + C_RED2 + mt * 5120 + lane * 16, rd2 = lds + C_RED + mt * 5120 + lane * 16;
    u16* cp = out + (size_t)(qb * 64 + mt * 16 + l15) * OUTLD + DM + g * 4 + (kt < 2 ? hg * 64 + kt * 32 : 512 + hg * 8);
    const char* op = ws + WS_O + (size_t)((qb * 4 + mt) * 18 + kt * 6) * 1024 + lane * 16;
    const char* op2 = op + 3072;
    PIN(cp); PIN(op); PIN(op2);
    VMWAIT(0);
    if (__builtin_expect(wid >= 8, 0)) __builtin_amdgcn_s_sleep(OSLEEP);
    __builtin_amdgcn_s_sleep(CSLEEP);
    u32x4 of0, of1, of2, of3, of4, of5;
    asm volatile("global_load_dwordx4 %0, %6, off sc1\n\tglobal_load_dwordx4 %1, %6, off offset:1024 sc1\n\t"
                 "global_load_dwordx4 %2, %6, off offset:2048 sc1\n\tglobal_load_dwordx4 %3, %7, off sc1\n\t"
                 "global_load_dwordx4 %4, %7, off offset:1024 sc1\n\tglobal_load_dwordx4 %5, %7, off offset:2048 sc1\n\ts_waitcnt vmcnt(0)"
                 : "=&v"(of0), "=&v"(of1), "=&v"(of2), "=&v"(of3), "=&v"(of4), "=&v"(of5) : "v"(op), "v"(op2) : "memory");
#define MX4(x) umax(umax(x[0], x[1]), umax(x[2], x[3]))
#define OMISS() (umax(umax(umax(MX4(of0), MX4(of1)), umax(MX4(of2), MX4(of3))), umax(MX4(of4), MX4(of5))) == SENT)
    if (__builtin_expect(__any(OMISS()), 0)) {
#pragma unroll 1
      for (int spin = 0; spin < SPIN_MAX; ++spin) {
        if (OMISS())
          asm volatile("global_load_dwordx4 %0, %6, off sc1\n\tglobal_load_dwordx4 %1, %6, off offset:1024 sc1\n\t"
                       "global_load_dwordx4 %2, %6, off offset:2048 sc1\n\tglobal_load_dwordx4 %3, %7, off sc1\n\t"
                       "global_load_dwordx4 %4, %7, off offset:1024 sc1\n\tglobal_load_dwordx4 %5, %7, off offset:2048 sc1\n\ts_waitcnt vmcnt(0)"
                       : "+v"(of0), "+v"(of1), "+v"(of2), "+v"(of3), "+v"(of4), "+v"(of5) : "v"(op), "v"(op2) : "memory");
        if (!__any(OMISS())) break;
      }
    }
    const u32x4 of[6] = {of0, of1, of2, of3, of4, of5};
    f32x4 acc[5];
#pragma unroll
    for (int nt = 0; nt < 5; ++nt) acc[nt] = (f32x4){0.f, 0.f, 0.f, 0.f};
#pragma unroll
    for (int kk = 0; kk < 6; ++kk) {
#pragma unroll
      for (int nt = 0; nt < 4; ++nt) acc[nt] = MFMA16(wf[kk][nt], __builtin_bit_cast(bf16x8, of[kk]), acc[nt]);
      acc[4] = MFMA16(wfr[kk], __builtin_bit_cast(bf16x8, of[kk]), acc[4]);
    }
#define LW(p, v) *(LAS f32x4*)(p) = (v)
#define LR(p) *(LAS const f32x4*)(p)
    if (kt == 0) { LW(rd + 2048, acc[2]); LW(rd + 3072, acc[3]); LW(rd2 + 4096, acc[4]); }
    else if (kt == 1) { LW(rd2, acc[0]); LW(rd2 + 1024, acc[1]); LW(rd + 4096, acc[4]); acc[0] = acc[2]; acc[1] = acc[3]; }
    else { LW(rd, acc[0]); LW(rd + 1024, acc[1]); LW(rd2 + 2048, acc[2]); LW(rd2 + 3072, acc[3]); acc[0] = acc[4]; }
    BAR();
    const f32x4 v = acc[0] + LR(rd + kt * 2048) + LR(rd2 + kt * 2048), w = acc[1] + LR(rd + kt * 2048 + 1024) + LR(rd2 + kt * 2048 + 1024);
    if (kt < 2 || g < 2) ost8(cp, (u32x2){pk2(v[0], v[1]), pk2(v[2], v[3])});
    if (kt < 2) ost8(cp + 16, (u32x2){pk2(w[0], w[1]), pk2(w[2], w[3])});
  }
}

extern "C" void kernel_launch(void* const* d_in, const int* in_sizes, int n_in, void* d_out, int out_size, void* d_ws, size_t ws_size,
                              hipStream_t stream) {
  fused_kernel<<<256, 768, 0, stream>>>((const u16*)d_in[0], (const u16*)d_in[1], (const u16*)d_in[2], (const u16*)d_in[3], (const u16*)d_in[4],
                                        (const u16*)d_in[5], (const u16*)d_in[6], (const u16*)d_in[7], (const u16*)d_in[8], (const u16*)d_in[9],
                                        (const u16*)d_in[10], (const u16*)d_in[11], (const u16*)d_in[12], (const u16*)d_in[13], (u16*)d_out,
                                        (char*)d_ws);
}
```
